```python
import torch

def _softmax(x, axis=-1):
    return torch.softmax(x, dim=axis)

def _gelu(x):
    return torch.nn.functional.gelu(x, approximate='tanh')

def _silu(x):
    return torch.nn.functional.silu(x)

def _sigmoid(x):
    return torch.sigmoid(x)

def _layernorm(x, axis=-1, eps=1e-05):
    m = x.mean(dim=axis, keepdim=True)
    v = x.var(dim=axis, keepdim=True, unbiased=False)
    return (x - m) / torch.sqrt(v + eps)

def _scatter_add(tgt, idx, src, axis=0):
    return tgt.clone().index_add_(axis, idx, src)

def _segment_sum(src, seg, n):
    out = torch.zeros((n,) + src.shape[1:], dtype=src.dtype)
    return out.index_add_(0, seg, src)

def _pad(x, pw):
    flat = tuple((v for pair in reversed(pw) for v in pair))
    return torch.nn.functional.pad(x, flat)
kernel_name = 'parallel_sum__grouped_kv_mqa__mixer_gmlp__prompt'

def setup_inputs():

    _w = lambda a: (lambda t: t.to(torch.bfloat16) if t.is_floating_point() else t)(torch.as_tensor(a))

    def _A_setup():
        B, S, D, H = (1, 2048, 640, 32)
        Dh = D // H
        return {'x': _w((lambda *s: torch.randn(*s, dtype=torch.bfloat16) * 0.05)(B, S, D)), 'wq': _w((lambda *s: torch.randn(*s, dtype=torch.bfloat16) * 0.2)(D, H * Dh)), 'wk': _w((lambda *s: torch.randn(*s, dtype=torch.bfloat16) * 0.2)(D, Dh)), 'wv': _w((lambda *s: torch.randn(*s, dtype=torch.bfloat16) * 0.1)(D, Dh)), 'wo': _w((lambda *s: torch.randn(*s, dtype=torch.bfloat16) * 0.1)(H * Dh, D))}
    _A_in = _A_setup()

    def _B_setup():
        B, S, D, F = (1, 2048, 640, 1728)
        Fh = F // 2
        return {'x': _w((lambda *s: torch.randn(*s, dtype=torch.bfloat16) * 0.05)(B, S, D)), 'w_in': _w((lambda *s: torch.randn(*s, dtype=torch.bfloat16) * 0.8)(D, 2 * Fh)), 'w_sp': _w(0.01 * (lambda *s: torch.randn(*s, dtype=torch.bfloat16) * 0.8)(S, S)), 'b_sp': _w(1.0 + 0.01 * (lambda *s: torch.randn(*s, dtype=torch.bfloat16) * 0.1)(S)), 'w_out': _w((lambda *s: torch.randn(*s, dtype=torch.bfloat16) * 0.1)(Fh, D))}
    _B_in = _B_setup()
    return {'A_x': _A_in['x'], 'A_wq': _A_in['wq'], 'A_wk': _A_in['wk'], 'A_wv': _A_in['wv'], 'A_wo': _A_in['wo'], 'B_x': _B_in['x'], 'B_w_in': _B_in['w_in'], 'B_w_sp': _B_in['w_sp'], 'B_b_sp': _B_in['b_sp'], 'B_w_out': _B_in['w_out']}

def reference(A_x, A_wq, A_wk, A_wv, A_wo, B_x, B_w_in, B_w_sp, B_b_sp, B_w_out):

    def _A_ref(x, wq, wk, wv, wo):
        B, S, D = x.shape
        H = 32
        Dh = D // H
        q = (lambda x, *d: x.permute(*d))((x @ wq).reshape(B, S, H, Dh), 0, 2, 1, 3)
        k = x @ wk
        v = x @ wv
        scores = torch.einsum('bhqd,bkd->bhqk', q, k) * Dh ** (-0.5)
        p = _softmax(scores, axis=-1)
        o = torch.einsum('bhqk,bkd->bhqd', p, v)
        return (lambda x, *d: x.permute(*d))(o, 0, 2, 1, 3).reshape(B, S, H * Dh) @ wo

    def _B_ref(x, w_in, w_sp, b_sp, w_out):
        B, S, D = x.shape
        F = w_in.shape[-1]
        Fh = F // 2
        z = _gelu(torch.einsum('bsd,df->bsf', x, w_in))
        z1 = z[..., :Fh].clone()
        z2 = _layernorm(z[..., Fh:], axis=-1)
        g = torch.einsum('ts,bsf->btf', w_sp, z2) + b_sp[None, :, None]
        return torch.einsum('bsf,fd->bsd', z1 * g, w_out)
    x = A_x
    xn = _layernorm(x, axis=-1)
    return x + _A_ref(xn, A_wq, A_wk, A_wv, A_wo) + _B_ref(xn, B_w_in, B_w_sp, B_b_sp, B_w_out)
```

```cpp
#include <hip/hip_runtime.h>
#include <hip/hip_bf16.h>
typedef __hip_bfloat16 bf16;
static constexpr int S=2048, D=640, H=32, DH=20, F=1728, FH=864;
__device__ inline float b2f(bf16 x){ return __bfloat162float(x); }
__device__ inline bf16 f2b(float x){ return __float2bfloat16(x); }

__global__ void k_ln(const bf16* x, float* xn){
  int r=blockIdx.x; __shared__ float red[256];
  float s=0; for(int i=threadIdx.x;i<D;i+=256) s+=b2f(x[r*D+i]);
  red[threadIdx.x]=s; __syncthreads();
  for(int o=128;o>0;o>>=1){ if(threadIdx.x<o) red[threadIdx.x]+=red[threadIdx.x+o]; __syncthreads(); }
  float m=red[0]/D; __syncthreads();
  float v=0; for(int i=threadIdx.x;i<D;i+=256){ float d=b2f(x[r*D+i])-m; v+=d*d; }
  red[threadIdx.x]=v; __syncthreads();
  for(int o=128;o>0;o>>=1){ if(threadIdx.x<o) red[threadIdx.x]+=red[threadIdx.x+o]; __syncthreads(); }
  float rs=rsqrtf(red[0]/D+1e-5f);
  for(int i=threadIdx.x;i<D;i+=256) xn[r*D+i]=(b2f(x[r*D+i])-m)*rs;
}
__global__ void k_gemm(const float* A, int lda, const bf16* B, int ldb, float* C, int ldc, int M,int N,int K,int mode){
  int n=blockIdx.x*64+threadIdx.x%64; int m=blockIdx.y*4+threadIdx.x/64;
  if(n>=N||m>=M) return; float acc=0;
  for(int k=0;k<K;k++) acc+=A[m*lda+k]*b2f(B[k*ldb+n]);
  if(mode==1){ float x=acc; acc=0.5f*x*(1.f+tanhf(0.7978845608f*(x+0.044715f*x*x*x))); }
  C[m*ldc+n]=acc;
}
__global__ void k_attn(const float* q,const float* k,const float* v,float* o){
  int idx=blockIdx.x*64+threadIdx.x; int t=idx/H,h=idx%H;
  float qv[DH]; for(int d=0;d<DH;d++) qv[d]=q[t*D+h*DH+d];
  float m=-1e30f,l=0,acc[DH]; for(int d=0;d<DH;d++) acc[d]=0;
  for(int j=0;j<S;j++){ float s=0; for(int d=0;d<DH;d++) s+=qv[d]*k[j*DH+d]; s*=0.22360679775f;
    float mn=fmaxf(m,s); float a=__expf(m-mn), p=__expf(s-mn); l=l*a+p; for(int d=0;d<DH;d++) acc[d]=acc[d]*a+p*v[j*DH+d]; m=mn; }
  for(int d=0;d<DH;d++) o[t*D+h*DH+d]=acc[d]/l;
}
__global__ void k_ln2(const float* z, float* z2n){
  int r=blockIdx.x; __shared__ float red[256];
  float s=0; for(int i=threadIdx.x;i<FH;i+=256) s+=z[r*F+FH+i];
  red[threadIdx.x]=s; __syncthreads();
  for(int o=128;o>0;o>>=1){ if(threadIdx.x<o) red[threadIdx.x]+=red[threadIdx.x+o]; __syncthreads(); }
  float m=red[0]/FH; __syncthreads();
  float v=0; for(int i=threadIdx.x;i<FH;i+=256){ float d=z[r*F+FH+i]-m; v+=d*d; }
  red[threadIdx.x]=v; __syncthreads();
  for(int o=128;o>0;o>>=1){ if(threadIdx.x<o) red[threadIdx.x]+=red[threadIdx.x+o]; __syncthreads(); }
  float rs=rsqrtf(red[0]/FH+1e-5f);
  for(int i=threadIdx.x;i<FH;i+=256) z2n[r*FH+i]=(z[r*F+FH+i]-m)*rs;
}
__global__ void k_sp(const bf16* wsp,const bf16* bsp,const float* z,const float* z2n,float* u){
  int f=blockIdx.x*64+threadIdx.x%64; int t=blockIdx.y*4+threadIdx.x/64;
  if(f>=FH) return; float acc=0;
  for(int s=0;s<S;s++) acc+=b2f(wsp[t*S+s])*z2n[s*FH+f];
  u[t*FH+f]=z[t*F+f]*(acc+b2f(bsp[t]));
}
__global__ void k_fin(const bf16* x,const float* o,const bf16* wo,const float* u,const bf16* wout,bf16* out){
  int n=blockIdx.x*64+threadIdx.x%64; int m=blockIdx.y*4+threadIdx.x/64;
  float acc=b2f(x[m*D+n]);
  for(int k=0;k<D;k++) acc+=o[m*D+k]*b2f(wo[k*D+n]);
  for(int k=0;k<FH;k++) acc+=u[m*FH+k]*b2f(wout[k*D+n]);
  out[m*D+n]=f2b(acc);
}
extern "C" void kernel_launch(void* const* d_in, const int* in_sizes, int n_in, void* d_out, int out_size, void* d_ws, size_t ws_size, hipStream_t stream){
  const bf16* x=(const bf16*)d_in[0]; const bf16* wq=(const bf16*)d_in[1]; const bf16* wk=(const bf16*)d_in[2]; const bf16* wv=(const bf16*)d_in[3]; const bf16* wo=(const bf16*)d_in[4];
  const bf16* win=(const bf16*)d_in[6]; const bf16* wsp=(const bf16*)d_in[7]; const bf16* bsp=(const bf16*)d_in[8]; const bf16* wout=(const bf16*)d_in[9];
  float* ws=(float*)d_ws; float* xn=ws; float* q=xn+S*D; float* k=q+S*D; float* v=k+S*DH; float* z=v+S*DH; float* o=z+S*F; float* z2n=o+S*D; float* u=z2n+S*FH;
  k_ln<<<S,256,0,stream>>>(x,xn);
  k_gemm<<<dim3(D/64,S/4),256,0,stream>>>(xn,D,wq,D,q,D,S,D,D,0);
  k_gemm<<<dim3(1,S/4),256,0,stream>>>(xn,D,wk,DH,k,DH,S,DH,D,0);
  k_gemm<<<dim3(1,S/4),256,0,stream>>>(xn,D,wv,DH,v,DH,S,DH,D,0);
  k_gemm<<<dim3(F/64,S/4),256,0,stream>>>(xn,D,win,F,z,F,S,F,D,1);
  k_attn<<<S*H/64,64,0,stream>>>(q,k,v,o);
  k_ln2<<<S,256,0,stream>>>(z,z2n);
  k_sp<<<dim3((FH+63)/64,S/4),256,0,stream>>>(wsp,bsp,z,z2n,u);
  k_fin<<<dim3(D/64,S/4),256,0,stream>>>(x,o,wo,u,wout,(bf16*)d_out);
}
```

```cpp
#include <hip/hip_runtime.h>
#include <stdint.h>
#include <type_traits>
typedef __bf16 bf16x8 __attribute__((ext_vector_type(8)));
typedef __bf16 bf16x2 __attribute__((ext_vector_type(2)));
typedef short s16x4 __attribute__((ext_vector_type(4)));
typedef float f32x16 __attribute__((ext_vector_type(16)));
typedef float f32x4 __attribute__((ext_vector_type(4)));
typedef float f32x2 __attribute__((ext_vector_type(2)));
typedef unsigned u32x4 __attribute__((ext_vector_type(4)));
typedef unsigned u32x2 __attribute__((ext_vector_type(2)));
#define DI __device__ __forceinline__
#define LAS __attribute__((address_space(3)))
#define SB() __builtin_amdgcn_sched_barrier(0)
#define RFL(x) __builtin_amdgcn_readfirstlane(x)
#define MFMA32(a, b, c) __builtin_amdgcn_mfma_f32_32x32x16_bf16(__builtin_bit_cast(bf16x8, a), __builtin_bit_cast(bf16x8, b), c, 0, 0, 0)
#define MFMA16(a, b, c) __builtin_amdgcn_mfma_f32_16x16x32_bf16(__builtin_bit_cast(bf16x8, a), __builtin_bit_cast(bf16x8, b), c, 0, 0, 0)
DI f32x4 mfma4sq(unsigned x0, unsigned x1, f32x4 c) { u32x2 v = {x0, x1}; s16x4 a = __builtin_bit_cast(s16x4, v); return __builtin_amdgcn_mfma_f32_4x4x4bf16_1k(a, a, c, 0, 0, 0); }
#define MFMA4(x0, x1, c) mfma4sq(x0, x1, c)
#define LDS128(p) (*(const LAS u32x4*)(p))
DI unsigned cvtpk(float lo, float hi) { f32x2 v = {lo, hi}; bf16x2 b = __builtin_convertvector(v, bf16x2); return __builtin_bit_cast(unsigned, b); }
DI float bflo(unsigned u) { return __uint_as_float(u << 16); }
DI float bfhi(unsigned u) { return __uint_as_float(u & 0xffff0000u); }
DI float dot2(unsigned a, unsigned b, float c) { return __builtin_amdgcn_fdot2_f32_bf16(__builtin_bit_cast(bf16x2, a), __builtin_bit_cast(bf16x2, b), c, false); }
DI float half_sum(float m) { auto r = __builtin_amdgcn_permlane32_swap(__float_as_uint(m), __float_as_uint(m), false, false); return __uint_as_float(r[0]) + __uint_as_float(r[1]); }
DI float half_max(float m) { auto r = __builtin_amdgcn_permlane32_swap(__float_as_uint(m), __float_as_uint(m), false, false); return fmaxf(__uint_as_float(r[0]), __uint_as_float(r[1])); }
DI void glds1(const void* sbase, unsigned voff, unsigned lds_base) {
  unsigned sv;
  asm volatile("s_mov_b32 %0, m0\n\ts_mov_b32 m0, %3\n\ts_nop 0\n\tglobal_load_lds_dwordx4 %1, %2\n\ts_mov_b32 m0, %0"
               : "=&s"(sv) : "v"(voff), "s"(sbase), "s"(lds_base) : "memory"); }
DI void glds3(const void* sbase, unsigned voff, unsigned lds_base) {
  unsigned sv;
  asm volatile("s_mov_b32 %0, m0\n\ts_mov_b32 m0, %3\n\ts_nop 0\n\tglobal_load_lds_dwordx4 %1, %2\n\tglobal_load_lds_dwordx4 %1, %2 offset:1024\n\tglobal_load_lds_dwordx4 %1, %2 offset:2048\n\ts_mov_b32 m0, %0"
               : "=&s"(sv) : "v"(voff), "s"(sbase), "s"(lds_base) : "memory"); }
DI u32x4 ldtr(const LAS char* p, int off0, int off1) {
  typedef LAS s16x4* lp;
  s16x4 a = __builtin_amdgcn_ds_read_tr16_b64_v4i16((lp)(p + off0)), b = __builtin_amdgcn_ds_read_tr16_b64_v4i16((lp)(p + off1));
  u32x2 x = __builtin_bit_cast(u32x2, a), y = __builtin_bit_cast(u32x2, b);
  return u32x4{x[0], x[1], y[0], y[1]};
}
#define WAITV(n) asm volatile("s_waitcnt vmcnt(" #n ")" ::: "memory")
#define BARRIER() asm volatile("s_waitcnt lgkmcnt(0)\n\ts_barrier" ::: "memory")

constexpr int SEQ = 2048, DM = 640, NFH = 864;
constexpr size_t WS_FLAG = 0;
constexpr size_t WS_QI  = 4096;
constexpr size_t WS_KV  = WS_QI + 2621440;
constexpr size_t WS_Z1  = WS_KV + 196608;
constexpr size_t WS_Z2R = WS_Z1 + 3538944;
constexpr size_t WS_Z2N = WS_Z2R + 3538944;
constexpr size_t WS_AO  = WS_Z2N + 4194304;
constexpr size_t WS_END = WS_AO + 6160384;
constexpr int KV_CHUNK = 12288, KV_VOFF = 6144;
struct Params {
  const char *x, *wq, *wk, *wv, *wo, *win, *wsp, *bsp, *wout;
  char* ws; char* out;
};


constexpr int K1_STAGE = 36864, K1_NS = 4, K1_XT = 16384;
constexpr int K1_CSP = K1_NS * K1_STAGE;
constexpr int K1_CSF = K1_CSP + 2560;
constexpr int K1_STP = K1_CSF + 640;
constexpr int K1_LDS = K1_STP + 2048;
constexpr float QSCALE = 0.22360679774997896f * 1.4426950408889634f;
enum { T_Q = 0, T_K = 1, T_V = 2, T_Z1 = 3, T_Z2 = 4, T_E = 5 };
DI void k1_slot(int cg, int j, int& type, int& idx) {
  if (j < 3 || (j == 3 && cg < 6)) { const int z = cg < 6 ? cg * 4 + j : 24 + (cg - 6) * 3 + j; type = z < 27 ? T_Z1 : T_Z2; idx = z < 27 ? z : z - 27; }
  else if (cg == 15) { type = j == 3 ? T_K : T_V; idx = 0; }
  else if (cg < 4) { type = T_E; idx = 0; }
  else if (cg < 6) { type = T_Q; idx = cg - 4; }
  else { type = T_Q; idx = 2 + (cg - 6) * 2 + (j - 3); }
}
DI float gelu_t(float x) {
  const float p = __builtin_fmaf(x * x, -0.10294324f, -2.3022082f);
  const float e = __builtin_amdgcn_exp2f(x * p);
  return x * __builtin_amdgcn_rcpf(1.f + e);
}
struct Seg { const char* base; unsigned stride, voff; int type, idx; };
template <int ROLE>
DI void k1_body(const Params& p, char* lds_, int rb, int cg, int wq4) {
  constexpr int NJ = ROLE == 0 ? 3 : 2, J0 = ROLE == 0 ? 0 : 3, NDMA = 2 + NJ;
  const int lane = threadIdx.x & 63, r = lane & 31, h = lane >> 5;
  const int wid = wq4 + 4 * ROLE, th = wq4 & 1, kh = wq4 >> 1;
  const unsigned l0 = (unsigned)(uintptr_t)lds_;
  LAS char* lds = (LAS char*)lds_;
  const bool kv = ROLE == 1 && cg == 15;
  Seg sg[NJ];
#pragma unroll
  for (int j = 0; j < NJ; j++) {
    int type, idx; k1_slot(cg, J0 + j, type, idx); sg[j].type = type; sg[j].idx = idx;
    const unsigned rowp = 16 * wq4 + (lane >> 2), ch = (lane & 3) * 16;
    if (kv) { sg[j].base = j == 0 ? p.wk : p.wv; sg[j].stride = 2560; const unsigned o = (wq4 < 2 ? wq4 : 2) * 1024 + lane * 16; sg[j].voff = o < 2544u ? o : 2544u; }
    else if (type == T_Q || type == T_E) { sg[j].base = p.wq + idx * 64; sg[j].stride = 64 * 1280; sg[j].voff = rowp * 1280 + ch; }
    else { sg[j].base = p.win + (type == T_Z2 ? 1728 : 0) + idx * 64; sg[j].stride = 64 * 3456; sg[j].voff = rowp * 3456 + ch; }
  }
  const char* xb = p.x + (size_t)rb * 128 * 1280;
  unsigned xv[2];
#pragma unroll
  for (int i = 0; i < 2; i++) { const unsigned row = 8 * (2 * wid + i) + (lane >> 3); xv[i] = row * 1280 + (((lane & 7) ^ ((row >> 1) & 7)) << 4); }
  auto issue = [&](int st) {
    const unsigned slot = l0 + (st & 3) * K1_STAGE;
#pragma unroll
    for (int i = 0; i < 2; i++) glds1(xb + st * 128, xv[i], slot + (2 * wid + i) * 1024);
#pragma unroll
    for (int j = 0; j < NJ; j++) glds1(sg[j].base + (size_t)st * sg[j].stride, sg[j].voff, slot + K1_XT + (J0 + j) * 4096 + wq4 * 1024);
  };
  issue(0); issue(1); issue(2);
  unsigned xa[2][2], wa[2];
  const unsigned pitch = kv ? 40 : 64;
  const int blk = (lane >> 4) & 1, q = (lane & 15) >> 2, pp = lane & 3;
#pragma unroll
  for (int i = 0; i < 2; i++) {
    const unsigned ks = 2 * kh + ((i + th) & 1);
#pragma unroll
    for (int t = 0; t < 2; t++) { const unsigned row = 64 * th + 32 * t + r; xa[i][t] = row * 128 + ((32 * ks + 16 * h) ^ (16 * ((row >> 1) & 7))); }
    wa[i] = K1_XT + J0 * 4096 + (16 * ks + 8 * h + q) * pitch + 32 * blk + 8 * pp;
  }
  f32x16 acc[2][NJ];
#pragma unroll
  for (int t = 0; t < 2; t++)
#pragma unroll
    for (int j = 0; j < NJ; j++) acc[t][j] = f32x16{};
  f32x4 cs[NJ], s1[2] = {f32x4{}, f32x4{}}, s2[2] = {f32x4{}, f32x4{}};
#pragma unroll
  for (int j = 0; j < NJ; j++) cs[j] = f32x4{};
  const unsigned ONES = 0x3F803F80u;
  const unsigned selw = ((lane & 15) == ((lane >> 4) & 1)) ? ONES : 0u;
  const u32x4 SEL = {selw, selw, selw, selw};
  auto mainloop = [&](auto KVC) {
    constexpr bool KV = decltype(KVC)::value;
    u32x4 xf[2][2] = {}, wf[2][NJ] = {};
    auto rdx = [&](int buf, LAS char* sl, int i) {
#pragma unroll
      for (int t = 0; t < 2; t++) xf[buf][t] = LDS128(sl + xa[i][t]);
    };
    auto rdw = [&](int buf, LAS char* sl, int i, int j) { wf[buf][j] = ldtr(sl + wa[i], j * 4096, j * 4096 + (KV ? 160 : 256)); };
    auto dma = [&](int st_issue, int nd) {
      if (st_issue >= 0) {
        const unsigned slot = l0 + (st_issue & 3) * K1_STAGE;
        if (nd < 2) glds1(xb + st_issue * 128, xv[nd & 1], slot + (2 * wid + nd) * 1024);
        else if (nd < NDMA) glds1(sg[(nd - 2) % NJ].base + (size_t)st_issue * sg[(nd - 2) % NJ].stride, sg[(nd - 2) % NJ].voff, slot + K1_XT + (J0 + nd - 2) * 4096 + wq4 * 1024);
      }
    };
    auto mm = [&](int c, bool colsum, bool pre, LAS char* nsl, int ni, int st_issue) {
#pragma unroll
      for (int m = 0; m < 2 * NJ; m++) {
        const int j = m >> 1, t = m & 1;
        if (KV && j == 1) acc[t][j] = MFMA32(xf[c][t], wf[c][j], acc[t][j]);
        else acc[t][j] = MFMA32(wf[c][j], xf[c][t], acc[t][j]);
        SB();
        if (pre) { if (m == 0) rdx(c ^ 1, nsl, ni); else if (m - 1 < NJ) rdw(c ^ 1, nsl, ni, m - 1); }
        if (NJ == 3) { if (m >= 1) dma(st_issue, m - 1); } else { if (m >= 1) dma(st_issue, m - 1); if (m == 3) dma(st_issue, 3); }
        SB();
      }
      if (ROLE == 1) {
#pragma unroll
        for (int t = 0; t < 2; t++) s1[t] = MFMA16(SEL, xf[c][t], s1[t]);
#pragma unroll
        for (int t = 0; t < 2; t++) s2[t] = MFMA4(xf[c][t][0], xf[c][t][1], s2[t]);
#pragma unroll
        for (int t = 0; t < 2; t++) s2[t] = MFMA4(xf[c][t][2], xf[c][t][3], s2[t]);
      }
      if (colsum) {
#pragma unroll
        for (int j = 0; j < NJ; j++) cs[j] = MFMA16(SEL, wf[c][j], cs[j]);
      }
      SB();
    };
    if (NDMA == 5) WAITV(10); else WAITV(8);
    BARRIER();
    rdx(0, lds, 0);
#pragma unroll
    for (int j = 0; j < NJ; j++) rdw(0, lds, 0, j);
#pragma unroll 1
    for (int st = 0; st < 7; st++) {
      mm(0, true, true, lds + (st & 3) * K1_STAGE, 1, -1);
      if (NDMA == 5) WAITV(5); else WAITV(4);
      BARRIER();
      mm(1, false, true, lds + ((st + 1) & 3) * K1_STAGE, 0, st + 3);
    }
#pragma unroll 1
    for (int st = 7; st < 10; st++) {
      mm(0, true, true, lds + (st & 3) * K1_STAGE, 1, -1);
      if (st == 7) { if (NDMA == 5) WAITV(5); else WAITV(4); } else WAITV(0);
      BARRIER();
      mm(1, false, true, lds + ((st + 1) & 3) * K1_STAGE, 0, -1);
    }
  };
  if (kv) mainloop(std::true_type{}); else mainloop(std::false_type{});
  BARRIER();
  LAS f32x4* ex = (LAS f32x4*)(lds + wid * 12288 + lane * 16);
  LAS const f32x4* exr = (LAS const f32x4*)(lds + (wid ^ 2) * 12288 + lane * 16);
#pragma unroll
  for (int j = 0; j < NJ; j++)
#pragma unroll
    for (int g = 0; g < 4; g++) {
      const f32x16& a = kh ? acc[0][j] : acc[1][j];
      ex[(j * 4 + g) * 64] = f32x4{a[4 * g], a[4 * g + 1], a[4 * g + 2], a[4 * g + 3]};
    }
  LAS float* csp = (LAS float*)(lds + K1_CSP);
  LAS float* csf = (LAS float*)(lds + K1_CSF);
  LAS f32x2* stp = (LAS f32x2*)(lds + K1_STP);
  if (lane < 16) {
#pragma unroll
    for (int j = 0; j < NJ; j++) { csp[wq4 * 160 + (J0 + j) * 32 + lane] = cs[j][0]; csp[wq4 * 160 + (J0 + j) * 32 + 16 + lane] = cs[j][1]; }
  }
  if (ROLE == 1) {
    LAS float* sp = (LAS float*)stp;
#pragma unroll
    for (int t = 0; t < 2; t++) {
      const int i3 = lane & 3;
      const float d = i3 == 0 ? s2[t][0] : i3 == 1 ? s2[t][1] : i3 == 2 ? s2[t][2] : s2[t][3];
      const float b = half_sum(d);
      const int base = kh * 128 + 64 * th + 32 * t;
      if (h == 0) sp[(base + r) * 2 + 1] = b;
      if (lane < 16) { sp[(base + lane) * 2] = s1[t][0]; sp[(base + 16 + lane) * 2] = s1[t][1]; }
    }
  }
  BARRIER();
  { const int c = threadIdx.x; if (c < 160) csf[c] = (csp[c] + csp[160 + c]) + (csp[320 + c] + csp[480 + c]); }
  f32x16 o[NJ];
#pragma unroll
  for (int j = 0; j < NJ; j++) {
    o[j] = kh ? acc[1][j] : acc[0][j];
#pragma unroll
    for (int g = 0; g < 4; g++) { const f32x4 t = exr[(j * 4 + g) * 64]; o[j][4 * g] += t[0]; o[j][4 * g + 1] += t[1]; o[j][4 * g + 2] += t[2]; o[j][4 * g + 3] += t[3]; }
  }
  const int tt = 2 * th + kh, row = 32 * tt + r;
  auto rowstat = [&](int rw) { const f32x2 a = stp[rw], b = stp[128 + rw]; const float mu = (a[0] + b[0]) * (1.f / 640.f);
    const float var = fmaxf((a[1] + b[1]) * (1.f / 640.f) - mu * mu, 0.f), rs = __builtin_amdgcn_rsqf(var + 1e-5f); return f32x2{mu * rs, rs}; };
  const f32x2 st_ = rowstat(row);
  BARRIER();
  const int tb = rb * 4 + tt;
#pragma unroll
  for (int j = 0; j < NJ; j++) {
    const int type = sg[j].type;
    if (type == T_E) continue;
    if (ROLE == 1 && type == T_V) {
      const float csum = csf[(J0 + j) * 32 + r];
      char* vb = p.ws + WS_KV + (size_t)rb * KV_CHUNK + KV_VOFF + tt * 1536 + h * 384 + r * 16;
#pragma unroll
      for (int s = 0; s < 2; s++) {
        float v[8];
#pragma unroll
        for (int i = 0; i < 8; i++) { const f32x2 t = rowstat(32 * tt + 16 * s + 8 * (i >> 2) + 4 * h + (i & 3)); v[i] = t[1] * o[j][8 * s + i] - t[0] * csum; }
        u32x4 w = {cvtpk(v[0], v[1]), cvtpk(v[2], v[3]), cvtpk(v[4], v[5]), cvtpk(v[6], v[7])};
        if (r == 20) w = u32x4{ONES, ONES, ONES, ONES};
        if (r > 20) w = u32x4{0u, 0u, 0u, 0u};
        if (r < 24) *(u32x4*)(vb + s * 768) = w;
      }
      continue;
    }
    float v[16];
#pragma unroll
    for (int g = 0; g < 4; g++) {
      const f32x4 c4 = *(LAS f32x4*)(csf + (J0 + j) * 32 + 8 * g + 4 * h);
#pragma unroll
      for (int i = 0; i < 4; i++) v[4 * g + i] = st_[1] * o[j][4 * g + i] - st_[0] * c4[i];
    }
    if (ROLE == 1 && type == T_K) {
      char* kb = p.ws + WS_KV + (size_t)rb * KV_CHUNK + tt * 1536 + r * 16 + h * 8;
      *(u32x2*)(kb) = u32x2{cvtpk(v[0], v[1]), cvtpk(v[2], v[3])};
      *(u32x2*)(kb + 512) = u32x2{cvtpk(v[4], v[5]), cvtpk(v[6], v[7])};
      *(u32x2*)(kb + 1024) = h ? u32x2{0x00003F80u, 0u} : u32x2{cvtpk(v[8], v[9]), cvtpk(v[10], v[11])};
      continue;
    }
    char* dst;
    if (type == T_Q) {
      dst = p.ws + WS_QI + ((size_t)(tb * 160 + sg[j].idx * 8 + h) * 32 + r) * 8;
#pragma unroll
      for (int i = 0; i < 16; i++) v[i] *= QSCALE;
    } else {
      dst = p.ws + (type == T_Z1 ? WS_Z1 : WS_Z2R) + ((size_t)(tb * 216 + sg[j].idx * 8 + h) * 32 + r) * 8;
#pragma unroll
      for (int i = 0; i < 16; i++) v[i] = gelu_t(v[i]);
    }
#pragma unroll
    for (int g = 0; g < 4; g++) *(u32x2*)(dst + g * 512) = u32x2{cvtpk(v[4 * g], v[4 * g + 1]), cvtpk(v[4 * g + 2], v[4 * g + 3])};
  }
}
__global__ void __launch_bounds__(512, 2) k1_proj(Params p) {
  extern __shared__ __attribute__((aligned(16))) char lds[];
  const int b = blockIdx.x, x = b & 7, j = b >> 3;
  const int rb = (x & 1) * 8 + (j & 7), cg = (x >> 1) * 4 + (j >> 3);
  const int wid = RFL(threadIdx.x >> 6);
  if (b == 0 && threadIdx.x < 256) ((u32x4*)(p.ws + WS_FLAG))[threadIdx.x] = u32x4{0u, 0u, 0u, 0u};
  if (wid < 4) k1_body<0>(p, lds, rb, cg, wid); else k1_body<1>(p, lds, rb, cg, wid - 4);
}

constexpr int K2_LDS = 81920;
constexpr float SM_OFF = 90.f;
#define EX(x) __builtin_amdgcn_exp2f(x)
#define AUNIT(Sc, Sn, qn0, qn1, Oc, Op, LD0, LD1, LD2, LD3) do { \
    Sc[0] = EX(Sc[0]); Sc[1] = EX(Sc[1]); SB(); \
    Sn = MFMA32(k0, qn0, zero16); SB(); LD0; SB(); \
    Sc[2] = EX(Sc[2]); Sc[3] = EX(Sc[3]); SB(); p1[2] = cvtpk(e12, e13); p1[3] = cvtpk(e14, e15); SB(); \
    Sc[4] = EX(Sc[4]); Sc[5] = EX(Sc[5]); SB(); \
    Op = MFMA32(v1, p1, Op); SB(); LD1; SB(); \
    Sc[6] = EX(Sc[6]); Sc[7] = EX(Sc[7]); SB(); p0[0] = cvtpk(Sc[0], Sc[1]); p0[1] = cvtpk(Sc[2], Sc[3]); SB(); \
    Sc[8] = EX(Sc[8]); Sc[9] = EX(Sc[9]); SB(); \
    Sn = MFMA32(k1, qn1, Sn); SB(); LD2; SB(); \
    Sc[10] = EX(Sc[10]); Sc[11] = EX(Sc[11]); SB(); p0[2] = cvtpk(Sc[4], Sc[5]); p0[3] = cvtpk(Sc[6], Sc[7]); SB(); \
    e12 = EX(Sc[12]); e13 = EX(Sc[13]); SB(); \
    Oc = MFMA32(v0, p0, Oc); SB(); LD3; SB(); \
    e14 = EX(Sc[14]); e15 = EX(Sc[15]); SB(); p1[0] = cvtpk(Sc[8], Sc[9]); p1[1] = cvtpk(Sc[10], Sc[11]); SB(); \
  } while (0)
#define ATILE(kn, v1c, v0n) do { \
    AUNIT(sA, sB, qB0, qB1, oA, oB, k0 = LDS128(kn), v1 = LDS128(v1c), k1 = LDS128((kn) + k1d), (void)0); \
    AUNIT(sB, sA, qA0, qA1, oB, oA, (void)0, (void)0, (void)0, v0 = LDS128(v0n)); \
  } while (0)
DI void load_q(const char* qb, int h, u32x4& q0, u32x4& q1) {
  u32x2 ga = *(const u32x2*)(qb + (2 * h) * 256), gb = *(const u32x2*)(qb + (2 * h + 1) * 256), gc = *(const u32x2*)(qb + 4 * 256);
  q0 = u32x4{ga[0], ga[1], gb[0], gb[1]};
  q1 = h ? u32x4{0u, 0u, 0u, 0u} : u32x4{gc[0], gc[1], 0u, 0u};
}
DI f32x16 attn_exact(const char* kv, u32x4 q0, u32x4 q1, int r, int h) {
  const f32x16 zero16 = {};
  q1[2] = 0u;
  f32x16 o = {}; float m = -3.0e38f;
  const int rr = r < 21 ? r : 21;
#pragma unroll 1
  for (int t = 0; t < 64; t++) {
    const char* cb = kv + (size_t)(t >> 2) * KV_CHUNK;
    const char* kb = cb + (t & 3) * 1536 + r * 16;
    const u32x4 k0 = *(const u32x4*)(kb + h * 512), k1 = *(const u32x4*)(kb + 1024);
    const char* vb = cb + KV_VOFF + (t & 3) * 1536 + h * 384 + rr * 16;
    const u32x4 v0 = *(const u32x4*)(vb), v1 = *(const u32x4*)(vb + 768);
    f32x16 s = MFMA32(k0, q0, zero16); s = MFMA32(k1, q1, s);
    float mx = s[0];
#pragma unroll
    for (int i = 1; i < 16; i++) mx = fmaxf(mx, s[i]);
    const float mn = fmaxf(m, half_max(mx)), a = EX(m - mn);
    m = mn;
#pragma unroll
    for (int i = 0; i < 16; i++) { o[i] *= a; s[i] = EX(s[i] - mn); }
    const u32x4 p0 = {cvtpk(s[0], s[1]), cvtpk(s[2], s[3]), cvtpk(s[4], s[5]), cvtpk(s[6], s[7])};
    const u32x4 p1 = {cvtpk(s[8], s[9]), cvtpk(s[10], s[11]), cvtpk(s[12], s[13]), cvtpk(s[14], s[15])};
    o = MFMA32(v0, p0, o); o = MFMA32(v1, p1, o);
  }
  return o;
}
DI void attn_role(const Params& p, char* lds_, int wg) {
  const int tid = threadIdx.x, lane = tid & 63, r = lane & 31, h = lane >> 5;
  const int wid = RFL(tid >> 6);
  const int pi = wid & 3, kh = wid >> 2;
  const int tb = wg >> 2, headA = (wg & 3) * 8 + 2 * pi;
  const unsigned l0 = (unsigned)(uintptr_t)lds_;
  LAS char* lds = (LAS char*)lds_;
  const char* kvg = p.ws + WS_KV;
  const char* kvsrc = kvg + (size_t)kh * 8 * KV_CHUNK + pi * 3072;
  const unsigned voff = lane * 16;
  const unsigned ring = l0 + kh * 3 * KV_CHUNK + pi * 3072;
  glds3(kvsrc, voff, ring);
  glds3(kvsrc + KV_CHUNK, voff, ring + KV_CHUNK);
  u32x4 qA0, qA1, qB0, qB1;
  const char* qb = p.ws + WS_QI + ((size_t)(tb * 160 + headA * 5) * 32 + r) * 8;
  load_q(qb, h, qA0, qA1); load_q(qb + 5 * 256, h, qB0, qB1);
  asm volatile("s_waitcnt vmcnt(0)\n\ts_barrier" ::: "memory");
  const f32x16 zero16 = {};
  const int k1d = 1024 - h * 512;
  const int rr = r < 21 ? r : 21;
  u32x4 k0, k1; f32x16 sA, sB;
  {
    LAS char* kb0 = lds + h * 512 + r * 16;
    k0 = LDS128(kb0); k1 = LDS128(kb0 + k1d);
    sA = MFMA32(k0, qA0, zero16); sA = MFMA32(k1, qA1, sA); sB = MFMA32(k0, qB0, zero16); sB = MFMA32(k1, qB1, sB);
    float mA = sA[0], mB = sB[0];
#pragma unroll
    for (int i = 1; i < 16; i++) { mA = fmaxf(mA, sA[i]); mB = fmaxf(mB, sB[i]); }
    k0 = LDS128(kb0 + 1536); k1 = LDS128(kb0 + 1536 + k1d);
    sA = MFMA32(k0, qA0, zero16); sA = MFMA32(k1, qA1, sA); sB = MFMA32(k0, qB0, zero16); sB = MFMA32(k1, qB1, sB);
#pragma unroll
    for (int i = 0; i < 16; i++) { mA = fmaxf(mA, sA[i]); mB = fmaxf(mB, sB[i]); }
    mA = half_max(mA); mB = half_max(mB);
    if (h == 0) { qA1[2] = cvtpk(-(mA + SM_OFF), 0.f); qB1[2] = cvtpk(-(mB + SM_OFF), 0.f); }
  }
  LAS char* kbase = lds + kh * 3 * KV_CHUNK + h * 512 + r * 16;
  LAS char* vbase = lds + kh * 3 * KV_CHUNK + KV_VOFF + h * 384 + rr * 16;
  k0 = LDS128(kbase); k1 = LDS128(kbase + k1d);
  sA = MFMA32(k0, qA0, zero16); sA = MFMA32(k1, qA1, sA);
  u32x4 v0 = LDS128(vbase), v1 = v0, p0 = {0u, 0u, 0u, 0u}, p1 = {0u, 0u, 0u, 0u};
  f32x16 oA = {}, oB = {};
  float e12 = 0.f, e13 = 0.f, e14 = 0.f, e15 = 0.f;
  int sc = 0;
#pragma unroll 1
  for (int c = 0; c < 8; c++) {
    const int sn = sc == 2 * KV_CHUNK ? 0 : sc + KV_CHUNK, sp = sn == 2 * KV_CHUNK ? 0 : sn + KV_CHUNK;
    if (c + 2 < 8) glds3(kvsrc + (size_t)(c + 2) * KV_CHUNK, voff, ring + sp);
    LAS char* kc = kbase + sc; LAS char* kn = kbase + sn; LAS char* vc = vbase + sc; LAS char* vn = vbase + sn;
    ATILE(kc + 1 * 1536, vc + 1 * 768, vc + 2 * 768);
    ATILE(kc + 2 * 1536, vc + 3 * 768, vc + 4 * 768);
    ATILE(kc + 3 * 1536, vc + 5 * 768, vc + 6 * 768);
    ATILE(kn, vc + 7 * 768, vn);
    asm volatile("s_waitcnt vmcnt(0) lgkmcnt(0)\n\ts_barrier" ::: "memory");
    sc = sn;
  }
  p1[2] = cvtpk(e12, e13); p1[3] = cvtpk(e14, e15);
  oB = MFMA32(v1, p1, oB);
  LAS f32x4* ex = (LAS f32x4*)(lds + (pi * 2 + kh) * 3072 + lane * 16);
  LAS const f32x4* exr = (LAS const f32x4*)(lds + (pi * 2 + (kh ^ 1)) * 3072 + lane * 16);
  f32x16 o;
  if (kh == 0) { o = oA;
#pragma unroll
    for (int g = 0; g < 3; g++) ex[g * 64] = f32x4{oB[4 * g], oB[4 * g + 1], oB[4 * g + 2], oB[4 * g + 3]};
  } else { o = oB;
#pragma unroll
    for (int g = 0; g < 3; g++) ex[g * 64] = f32x4{oA[4 * g], oA[4 * g + 1], oA[4 * g + 2], oA[4 * g + 3]};
  }
  BARRIER();
#pragma unroll
  for (int g = 0; g < 3; g++) { f32x4 t = exr[g * 64]; o[4 * g] += t[0]; o[4 * g + 1] += t[1]; o[4 * g + 2] += t[2]; o[4 * g + 3] += t[3]; }
  {
    float big = 0.f;
#pragma unroll
    for (int i = 0; i < 12; i++) big = fmaxf(big, fabsf(o[i]));
    float chk = 0.f;
#pragma unroll
    for (int i = 0; i < 12; i++) chk += o[i] * 0.f;
    const bool bad = !(big < 1.0e36f) || !(chk == 0.f);
    if (__builtin_expect(__any(bad), 0)) o = attn_exact(kvg, kh ? qB0 : qA0, kh ? qB1 : qA1, r, h);
  }
  auto lr = __builtin_amdgcn_permlane32_swap(__float_as_uint(o[8]), __float_as_uint(o[8]), false, false);
  const float il = __builtin_amdgcn_rcpf(__uint_as_float(lr[1]));
  const int tok = tb * 32 + r, head = headA + kh;
#pragma unroll
  for (int g = 0; g < 3; g++) {
    if (g < 2 || h == 0) {
      const int kk = head * 20 + (2 * g + h) * 4;
      u32x2 w = {cvtpk(o[4 * g] * il, o[4 * g + 1] * il), cvtpk(o[4 * g + 2] * il, o[4 * g + 3] * il)};
      *(u32x2*)(p.ws + WS_AO + ((size_t)(kk >> 3) * 2048 + tok) * 16 + ((kk >> 2) & 1) * 8) = w;
    }
  }
}

DI int fb_start(int fb) { return (fb >> 2) * 216 + (fb & 3) * 56; }
constexpr int SP_STAGE = 24576, SP_ZOFF = 16384;
typedef __attribute__((address_space(1))) unsigned gu32;
DI void sp_role(const Params& p, char* lds_, int b) {
  const int wid = RFL(threadIdx.x >> 6);
  if (wid >= 4) return;
  const int tid = threadIdx.x, lane = tid & 63, r = lane & 31, h = lane >> 5;
  const unsigned l0 = (unsigned)(uintptr_t)lds_;
  LAS char* lds = (LAS char*)lds_;
  const int x = b & 7, jj = b >> 3;
  const int tbk = (x >> 1) * 4 + (jj & 3), fb = (x & 1) * 8 + (jj >> 2);
  const char* wb = p.wsp + (size_t)tbk * 128 * 4096;
  unsigned wv4[4];
#pragma unroll
  for (int i = 0; i < 4; i++) { const unsigned row = 8 * (4 * wid + i) + (lane >> 3); wv4[i] = row * 4096 + (((lane & 7) ^ ((row >> 1) & 7)) << 4); }
  const char* zb = p.ws + WS_Z2N + (size_t)fb * 32 * 8192 + wid * 2048;
  const unsigned zv = lane * 16;
  auto issue_w = [&](int st) {
#pragma unroll
    for (int i = 0; i < 4; i++) glds1(wb + st * 128, wv4[i], l0 + (st % 3) * SP_STAGE + (4 * wid + i) * 1024);
  };
  auto issue_z = [&](int st) {
    glds1(zb + (size_t)st * 8192, zv, l0 + (st % 3) * SP_STAGE + SP_ZOFF + wid * 2048);
    glds1(zb + (size_t)st * 8192 + 1024, zv, l0 + (st % 3) * SP_STAGE + SP_ZOFF + wid * 2048 + 1024);
  };
  {
    LAS float* red = (LAS float*)(lds + 3 * SP_STAGE);
    const int fg = tid < 216 ? tid : 215;
    const char* src = p.ws + WS_Z2R + ((size_t)((b >> 2) * 216 + fg) * 32 + (b & 3) * 8) * 8;
    u32x4 raw[4];
#pragma unroll
    for (int i = 0; i < 4; i++) raw[i] = *(const u32x4*)(src + i * 16);
    issue_w(0); issue_w(1);
    float v[8][4];
#pragma unroll
    for (int i = 0; i < 4; i++) {
      v[2 * i][0] = bflo(raw[i][0]); v[2 * i][1] = bfhi(raw[i][0]); v[2 * i][2] = bflo(raw[i][1]); v[2 * i][3] = bfhi(raw[i][1]);
      v[2 * i + 1][0] = bflo(raw[i][2]); v[2 * i + 1][1] = bfhi(raw[i][2]); v[2 * i + 1][2] = bflo(raw[i][3]); v[2 * i + 1][3] = bfhi(raw[i][3]);
    }
    const float act = tid < 216 ? 1.f : 0.f;
    auto wsum = [&](float s) {
#pragma unroll
      for (int o = 1; o < 32; o <<= 1) s += __shfl_xor(s, o);
      return half_sum(s); };
    float mu[8], rs[8];
#pragma unroll
    for (int t = 0; t < 8; t++) { const float s = wsum(act * ((v[t][0] + v[t][1]) + (v[t][2] + v[t][3]))); if (lane == 0) red[wid * 8 + t] = s; }
    BARRIER();
#pragma unroll
    for (int t = 0; t < 8; t++) mu[t] = ((red[t] + red[8 + t]) + (red[16 + t] + red[24 + t])) * (1.f / 864.f);
#pragma unroll
    for (int t = 0; t < 8; t++) {
#pragma unroll
      for (int i = 0; i < 4; i++) v[t][i] -= mu[t];
      const float s = wsum(act * ((v[t][0] * v[t][0] + v[t][1] * v[t][1]) + (v[t][2] * v[t][2] + v[t][3] * v[t][3])));
      if (lane == 0) red[32 + wid * 8 + t] = s;
    }
    BARRIER();
#pragma unroll
    for (int t = 0; t < 8; t++) rs[t] = __builtin_amdgcn_rsqf(((red[32 + t] + red[40 + t]) + (red[48 + t] + red[56 + t])) * (1.f / 864.f) + 1e-5f);
    if (tid < 216) {
      const int f0 = 4 * fg, q4 = f0 / 216, rem = f0 - q4 * 216, sub = rem / 56 < 3 ? rem / 56 : 3, fbw = q4 * 4 + sub, fl = rem - sub * 56;
      char* dst = p.ws + WS_Z2N + ((((size_t)fbw * 32 + (b >> 3)) * 4 + ((b >> 1) & 3)) * 2 + (b & 1)) * 1024 + fl * 16;
      const __amdgpu_buffer_rsrc_t rsrc = __builtin_amdgcn_make_buffer_rsrc((void*)(p.ws + WS_Z2N), 0, 4194304, 0x00020000);
      const unsigned off = (unsigned)(dst - (p.ws + WS_Z2N));
#pragma unroll
      for (int i = 0; i < 4; i++) {
        const u32x4 w = {cvtpk(v[0][i] * rs[0], v[1][i] * rs[1]), cvtpk(v[2][i] * rs[2], v[3][i] * rs[3]), cvtpk(v[4][i] * rs[4], v[5][i] * rs[5]), cvtpk(v[6][i] * rs[6], v[7][i] * rs[7])};
        __builtin_amdgcn_raw_buffer_store_b128(w, rsrc, off + i * 16, 0, 16);
      }
    }
    asm volatile("s_waitcnt vmcnt(0)" ::: "memory");
    BARRIER();
    gu32* cnt = (gu32*)(p.ws + WS_FLAG);
    if (tid == 0) __hip_atomic_fetch_add(cnt + (b >> 3) * 16, 1u, __ATOMIC_RELAXED, __HIP_MEMORY_SCOPE_AGENT);
    if (wid == 0) {
      unsigned spins = 0;
      for (;;) {
        const unsigned c = __hip_atomic_load(cnt + (lane & 31) * 16, __ATOMIC_RELAXED, __HIP_MEMORY_SCOPE_AGENT);
        if (__all(c >= 8u)) break;
        if (++spins > (1u << 22)) break;
        __builtin_amdgcn_s_sleep(2);
      }
      __builtin_amdgcn_fence(__ATOMIC_ACQUIRE, "agent");
      asm volatile("s_waitcnt vmcnt(0)" ::: "memory");
    }
    BARRIER();
  }
  issue_z(0); issue_z(1);
  const unsigned row = 32 * wid + r;
  unsigned wa[4];
#pragma unroll
  for (int ks = 0; ks < 4; ks++) wa[ks] = row * 128 + ((32 * ks + 16 * h) ^ (16 * ((row >> 1) & 7)));
  const unsigned za = SP_ZOFF + h * 1024 + r * 16;
  f32x16 acc0 = {}, acc1 = {};
#pragma unroll 1
  for (int st = 0; st < 32; st++) {
    if (st == 0) WAITV(2); else if (st < 31) WAITV(6); else WAITV(0);
    BARRIER();
    if (st + 2 < 32) { issue_w(st + 2); issue_z(st + 2); }
    LAS char* sl = lds + (st % 3) * SP_STAGE;
#pragma unroll
    for (int ks = 0; ks < 4; ks++) {
      const u32x4 wf = LDS128(sl + wa[ks]);
      const u32x4 z0 = LDS128(sl + za + ks * 2048), z1 = LDS128(sl + za + ks * 2048 + 512);
      acc0 = MFMA32(z0, wf, acc0); acc1 = MFMA32(z1, wf, acc1);
    }
  }
  const int tok = tbk * 128 + row, f0 = fb_start(fb), width = (fb & 3) == 3 ? 48 : 56;
  const float bias = bflo(*(const unsigned short*)(p.bsp + tok * 2));
  const char* z1b = p.ws + WS_Z1 + ((size_t)((tok >> 5) * 216 + (f0 >> 2) + h) * 32 + r) * 8;
#pragma unroll
  for (int j = 0; j < 2; j++) {
    const f32x16& a = j ? acc1 : acc0;
    u32x2 w[4];
#pragma unroll
    for (int g = 0; g < 4; g++) {
      const int fl = 32 * j + 8 * g;
      u32x2 z = {0u, 0u};
      if (fl < width) z = *(const u32x2*)(z1b + (8 * j + 2 * g) * 256);
      w[g] = u32x2{cvtpk(bflo(z[0]) * (a[4 * g] + bias), bfhi(z[0]) * (a[4 * g + 1] + bias)), cvtpk(bflo(z[1]) * (a[4 * g + 2] + bias), bfhi(z[1]) * (a[4 * g + 3] + bias))};
    }
#pragma unroll
    for (int g = 0; g < 4; g += 2) {
      auto s0 = __builtin_amdgcn_permlane32_swap(w[g][0], w[g + 1][0], false, false);
      auto s1 = __builtin_amdgcn_permlane32_swap(w[g][1], w[g + 1][1], false, false);
      const int fl = 32 * j + 8 * (g + h);
      if (fl < width) *(u32x4*)(p.ws + WS_AO + ((size_t)(80 + ((f0 + fl) >> 3)) * 2048 + tok) * 16) = u32x4{s0[0], s1[0], s0[1], s1[1]};
    }
  }
}
__global__ void __launch_bounds__(512, 4) k2_mix(Params p) {
  extern __shared__ __attribute__((aligned(16))) char lds[];
  if (blockIdx.x < 256) sp_role(p, lds, blockIdx.x); else attn_role(p, lds, blockIdx.x - 256);
}

constexpr int K3_ACT = 8192, K3_W = 10240, K3_STAGE = K3_ACT + K3_W, K3_NS = 6;
constexpr int K3_LDS = K3_NS * K3_STAGE + 256;
__global__ void __launch_bounds__(384, 2) k3_out(Params p) {
  extern __shared__ __attribute__((aligned(16))) char lds_[];
  const int b = blockIdx.x, x = b & 7, jj = b >> 3;
  const int tb = x * 4 + (jj & 3), nb = jj >> 2;
  const int tid = threadIdx.x, lane = tid & 63, r = lane & 31, h = lane >> 5;
  const int wid = RFL(tid >> 6), nt = wid % 3, tt = wid / 3;
  const unsigned l0 = (unsigned)(uintptr_t)lds_;
  LAS char* lds = (LAS char*)lds_;
  const char* ab = p.ws + WS_AO + (size_t)tb * 64 * 16;
  unsigned pv[3]; unsigned pdst[3]; bool isw[3];
#pragma unroll
  for (int i = 0; i < 3; i++) {
    const int pc = wid * 3 + i;
    if (pc < 8) { isw[i] = false; pv[i] = (unsigned)pc * 32768u + lane * 16; pdst[i] = pc * 1024; }
    else { isw[i] = true; const unsigned o = (pc - 8) * 1024 + lane * 16, rw = o / 160, c = o - rw * 160; pv[i] = rw * 1280 + c; pdst[i] = K3_ACT + (pc - 8) * 1024; }
  }
  auto issue = [&](int st) {
    const char* wsrc = (st < 10 ? p.wo + (size_t)st * 64 * 1280 : p.wout + (size_t)(st - 10) * 64 * 1280) + nb * 160;
    const char* asrc = ab + (size_t)st * 8 * 32768;
    const unsigned slot = l0 + (st % K3_NS) * K3_STAGE;
#pragma unroll
    for (int i = 0; i < 3; i++) {
      unsigned v = pv[i];
      if (st == 23) { if (isw[i]) v = v < 32u * 1280u ? v : 0u; else v = v < 4u * 32768u ? v : lane * 16; }
      glds1(isw[i] ? wsrc : asrc, v, slot + pdst[i]);
    }
  };
#pragma unroll
  for (int s = 0; s < K3_NS - 1; s++) issue(s);
  const int blk = (lane >> 4) & 1, q = (lane & 15) >> 2, pp = lane & 3;
  const unsigned wa = K3_ACT + (8 * h + q) * 160 + nt * 64 + 32 * blk + 8 * pp;
  const unsigned aa = h * 1024 + (32 * tt + r) * 16;
  f32x16 acc = {};
#pragma unroll 1
  for (int st = 0; st < 24; st++) {
    if (st < 20) WAITV(12); else if (st == 20) WAITV(9); else if (st == 21) WAITV(6); else if (st == 22) WAITV(3); else WAITV(0);
    BARRIER();
    if (st + K3_NS - 1 < 24) issue(st + K3_NS - 1);
    LAS char* sl = lds + (st % K3_NS) * K3_STAGE;
    const int nks = st == 23 ? 2 : 4;
#pragma unroll
    for (int ks = 0; ks < 4; ks++) {
      if (ks < nks) {
        const u32x4 af = LDS128(sl + aa + ks * 2048);
        const u32x4 wf = ldtr(sl + wa, ks * 2560, ks * 2560 + 640);
        acc = MFMA32(wf, af, acc);
      }
    }
  }
  BARRIER();
  LAS float* ot = (LAS float*)lds;
  constexpr int OP = 84;
#pragma unroll
  for (int g = 0; g < 4; g++) {
    const int n = nt * 32 + 8 * g + 4 * h;
    if (n < 80) *(LAS f32x4*)(ot + (32 * tt + r) * OP + n) = f32x4{acc[4 * g], acc[4 * g + 1], acc[4 * g + 2], acc[4 * g + 3]};
  }
  BARRIER();
  for (int i = tid; i < 640; i += 384) {
    const int row = i / 10, c = i - row * 10;
    const size_t go = ((size_t)(tb * 64 + row) * 640 + nb * 80 + c * 8) * 2;
    const u32x4 xr = *(const u32x4*)(p.x + go);
    const f32x4 a = *(LAS f32x4*)(ot + row * OP + c * 8), a2 = *(LAS f32x4*)(ot + row * OP + c * 8 + 4);
    const u32x4 w = {cvtpk(a[0] + bflo(xr[0]), a[1] + bfhi(xr[0])), cvtpk(a[2] + bflo(xr[1]), a[3] + bfhi(xr[1])),
                     cvtpk(a2[0] + bflo(xr[2]), a2[1] + bfhi(xr[2])), cvtpk(a2[2] + bflo(xr[3]), a2[3] + bfhi(xr[3]))};
    *(u32x4*)(p.out + go) = w;
  }
}

extern "C" void kernel_launch(void* const* d_in, const int* in_sizes, int n_in, void* d_out, int out_size, void* d_ws, size_t ws_size, hipStream_t stream) {
  Params p;
  p.x = (const char*)d_in[0]; p.wq = (const char*)d_in[1]; p.wk = (const char*)d_in[2]; p.wv = (const char*)d_in[3]; p.wo = (const char*)d_in[4];
  p.win = (const char*)d_in[6]; p.wsp = (const char*)d_in[7]; p.bsp = (const char*)d_in[8]; p.wout = (const char*)d_in[9];
  p.ws = (char*)d_ws; p.out = (char*)d_out;
  (void)hipFuncSetAttribute((const void*)k1_proj, hipFuncAttributeMaxDynamicSharedMemorySize, K1_LDS);
  (void)hipFuncSetAttribute((const void*)k2_mix, hipFuncAttributeMaxDynamicSharedMemorySize, K2_LDS);
  (void)hipFuncSetAttribute((const void*)k3_out, hipFuncAttributeMaxDynamicSharedMemorySize, K3_LDS);
  k1_proj<<<256, 512, K1_LDS, stream>>>(p);
  k2_mix<<<512, 512, K2_LDS, stream>>>(p);
  k3_out<<<256, 384, K3_LDS, stream>>>(p);
}
```

```cpp
#include <hip/hip_runtime.h>
#include <stdint.h>
#include <type_traits>
typedef __bf16 bf16x8 __attribute__((ext_vector_type(8)));
typedef __bf16 bf16x2 __attribute__((ext_vector_type(2)));
typedef short s16x4 __attribute__((ext_vector_type(4)));
typedef float f32x16 __attribute__((ext_vector_type(16)));
typedef float f32x4 __attribute__((ext_vector_type(4)));
typedef float f32x2 __attribute__((ext_vector_type(2)));
typedef unsigned u32x4 __attribute__((ext_vector_type(4)));
typedef unsigned u32x2 __attribute__((ext_vector_type(2)));
#define DI __device__ __forceinline__
#define LAS __attribute__((address_space(3)))
#define SB() __builtin_amdgcn_sched_barrier(0)
#define RFL(x) __builtin_amdgcn_readfirstlane(x)
#define MFMA32(a, b, c) __builtin_amdgcn_mfma_f32_32x32x16_bf16(__builtin_bit_cast(bf16x8, a), __builtin_bit_cast(bf16x8, b), c, 0, 0, 0)
#define MFMA16(a, b, c) __builtin_amdgcn_mfma_f32_16x16x32_bf16(__builtin_bit_cast(bf16x8, a), __builtin_bit_cast(bf16x8, b), c, 0, 0, 0)
DI f32x4 mfma4sq(unsigned x0, unsigned x1, f32x4 c) { u32x2 v = {x0, x1}; s16x4 a = __builtin_bit_cast(s16x4, v); return __builtin_amdgcn_mfma_f32_4x4x4bf16_1k(a, a, c, 0, 0, 0); }
#define MFMA4(x0, x1, c) mfma4sq(x0, x1, c)
#define LDS128(p) (*(const LAS u32x4*)(p))
DI unsigned cvtpk(float lo, float hi) { f32x2 v = {lo, hi}; bf16x2 b = __builtin_convertvector(v, bf16x2); return __builtin_bit_cast(unsigned, b); }
DI float bflo(unsigned u) { return __uint_as_float(u << 16); }
DI float bfhi(unsigned u) { return __uint_as_float(u & 0xffff0000u); }
DI void sqacc(float& a, float x) { asm("v_fmac_f32_e32 %0, %1, %1" : "+v"(a) : "v"(x)); }
DI float dot2(unsigned a, unsigned b, float c) { return __builtin_amdgcn_fdot2_f32_bf16(__builtin_bit_cast(bf16x2, a), __builtin_bit_cast(bf16x2, b), c, false); }
DI float half_sum(float m) { auto r = __builtin_amdgcn_permlane32_swap(__float_as_uint(m), __float_as_uint(m), false, false); return __uint_as_float(r[0]) + __uint_as_float(r[1]); }
DI float half_max(float m) { auto r = __builtin_amdgcn_permlane32_swap(__float_as_uint(m), __float_as_uint(m), false, false); return fmaxf(__uint_as_float(r[0]), __uint_as_float(r[1])); }
template <int CTRL, int RMASK> DI float dpp_add(float v) { return v + __uint_as_float(__builtin_amdgcn_update_dpp(0u, __float_as_uint(v), CTRL, RMASK, 0xf, true)); }
DI float wave_sum(float v) {
  v = dpp_add<0x111, 0xf>(v); v = dpp_add<0x112, 0xf>(v); v = dpp_add<0x114, 0xf>(v); v = dpp_add<0x118, 0xf>(v);
  v = dpp_add<0x142, 0xa>(v); v = dpp_add<0x143, 0xc>(v);
  return __uint_as_float(__builtin_amdgcn_readlane(__float_as_uint(v), 63));
}
DI void glds1(const void* sbase, unsigned voff, unsigned lds_base) {
  unsigned sv;
  asm volatile("s_mov_b32 %0, m0\n\ts_mov_b32 m0, %3\n\ts_nop 0\n\tglobal_load_lds_dwordx4 %1, %2\n\ts_mov_b32 m0, %0"
               : "=&s"(sv) : "v"(voff), "s"(sbase), "s"(lds_base) : "memory"); }
DI void glds3(const void* sbase, unsigned voff, unsigned lds_base) {
  unsigned sv;
  asm volatile("s_mov_b32 %0, m0\n\ts_mov_b32 m0, %3\n\ts_nop 0\n\tglobal_load_lds_dwordx4 %1, %2\n\tglobal_load_lds_dwordx4 %1, %2 offset:1024\n\tglobal_load_lds_dwordx4 %1, %2 offset:2048\n\ts_mov_b32 m0, %0"
               : "=&s"(sv) : "v"(voff), "s"(sbase), "s"(lds_base) : "memory"); }
DI void gldsn(const void* sbase, unsigned voff, unsigned lds_base) {
  asm volatile("s_mov_b32 m0, %2\n\ts_nop 0\n\tglobal_load_lds_dwordx4 %0, %1" :: "v"(voff), "s"(sbase), "s"(lds_base) : "memory"); }
DI u32x4 ldtr(const LAS char* p, int off0, int off1) {
  typedef LAS s16x4* lp;
  s16x4 a = __builtin_amdgcn_ds_read_tr16_b64_v4i16((lp)(p + off0)), b = __builtin_amdgcn_ds_read_tr16_b64_v4i16((lp)(p + off1));
  u32x2 x = __builtin_bit_cast(u32x2, a), y = __builtin_bit_cast(u32x2, b);
  return u32x4{x[0], x[1], y[0], y[1]};
}
#define WAITV(n) asm volatile("s_waitcnt vmcnt(" #n ")" ::: "memory")
#define BARRIER() asm volatile("s_waitcnt lgkmcnt(0)\n\ts_barrier" ::: "memory")

constexpr int SEQ = 2048, DM = 640, NFH = 864;
constexpr size_t WS_FLAG = 0;
constexpr size_t WS_QI  = 4096;
constexpr size_t WS_KV  = WS_QI + 2621440;
constexpr size_t WS_Z1  = WS_KV + 196608;
constexpr size_t WS_Z2R = WS_Z1 + 3538944;
constexpr size_t WS_Z2N = WS_Z2R + 3538944;
constexpr size_t WS_AO  = WS_Z2N + 4194304;
constexpr size_t WS_W3  = WS_AO + 6160384;
constexpr size_t WS_END = WS_W3 + 8 * 94 * 2560;
constexpr int KV_CHUNK = 12288, KV_VOFF = 6144;
struct Params {
  const char *x, *wq, *wk, *wv, *wo, *win, *wsp, *bsp, *wout;
  char* ws; char* out;
};

constexpr int K1_STAGE = 36864, K1_NS = 4, K1_XT = 16384;
constexpr int K1_CSP = K1_NS * K1_STAGE;
constexpr int K1_CSF = K1_CSP + 2560;
constexpr int K1_STP = K1_CSF + 640;
constexpr int K1_LDS = K1_STP + 1024;
constexpr float QSCALE = 0.22360679774997896f * 1.4426950408889634f;
enum { T_Q = 0, T_K = 1, T_V = 2, T_Z1 = 3, T_Z2 = 4, T_E = 5 };
DI void k1_slot(int cg, int j, int& type, int& idx) {
  if (j < 3 || (j == 3 && cg < 6)) { const int z = cg < 6 ? cg * 4 + j : 24 + (cg - 6) * 3 + j; type = z < 27 ? T_Z1 : T_Z2; idx = z < 27 ? z : z - 27; }
  else if (cg == 15) { type = j == 3 ? T_K : T_V; idx = 0; }
  else if (cg < 4) { type = T_E; idx = 0; }
  else if (cg < 6) { type = T_Q; idx = cg - 4; }
  else { type = T_Q; idx = 2 + (cg - 6) * 2 + (j - 3); }
}
DI float gelu_t(float x) {
  const float p = __builtin_fmaf(x * x, -0.10294324f, -2.3022082f);
  const float e = __builtin_amdgcn_exp2f(x * p);
  return x * __builtin_amdgcn_rcpf(1.f + e);
}
DI void k1_loader(const Params& p, char* lds_, int rb, int cg, int lw) {
  const int lane = threadIdx.x & 63;
  __builtin_amdgcn_s_setprio(3);
  const unsigned l0 = (unsigned)(uintptr_t)lds_;
  const char* base[5]; unsigned stride[5], voff[5];
  const uintptr_t pwq = (uintptr_t)p.wq, pwin = (uintptr_t)p.win, pwk = (uintptr_t)p.wk, pwv = (uintptr_t)p.wv;
#pragma unroll
  for (int j = 0; j < 5; j++) {
    int type, idx; k1_slot(cg, j, type, idx);
    const unsigned rowp = 16 * lw + (lane >> 2), ch = (lane & 3) * 16;
    const bool kvs = j >= 3 && cg == 15, isq = type == T_Q || type == T_E;
    const uintptr_t a = kvs ? (j == 3 ? pwk : pwv) : isq ? pwq + idx * 64 : pwin + (type == T_Z2 ? 1728 : 0) + idx * 64;
    base[j] = (const char*)a;
    stride[j] = kvs ? 2560u : isq ? 64u * 1280u : 64u * 3456u;
    const unsigned o = (lw < 2 ? lw : 2) * 1024 + lane * 16;
    voff[j] = kvs ? (o < 2544u ? o : 2544u) : rowp * (isq ? 1280u : 3456u) + ch;
  }
  const char* xb = p.x + (size_t)rb * 128 * 1280;
  unsigned xv[4];
#pragma unroll
  for (int i = 0; i < 4; i++) { const unsigned row = 8 * (4 * lw + i) + (lane >> 3); xv[i] = row * 1280 + (((lane & 7) ^ ((row >> 1) & 7)) << 4); }
  auto issue = [&](int st) {
    const unsigned slot = l0 + (st & 3) * K1_STAGE;
#pragma unroll
    for (int i = 0; i < 4; i++) gldsn(xb + st * 128, xv[i], slot + (4 * lw + i) * 1024);
#pragma unroll
    for (int j = 0; j < 5; j++) gldsn(base[j] + (size_t)st * stride[j], voff[j], slot + K1_XT + j * 4096 + lw * 1024);
  };
  issue(0); issue(1); issue(2);
  WAITV(18);
  BARRIER();
#pragma unroll 1
  for (int st = 0; st < 10; st++) {
    if (st < 8) WAITV(9); else WAITV(0);
    BARRIER();
    if (st + 3 < 10) issue(st + 3);
  }
}
template <int ROLE>
DI void k1_body(const Params& p, char* lds_, int rb, int cg, int tt) {
  constexpr int NJ = ROLE == 0 ? 3 : 2, J0 = ROLE == 0 ? 0 : 3;
  const int lane = threadIdx.x & 63, r = lane & 31, h = lane >> 5;
  LAS char* lds = (LAS char*)lds_;
  const bool kv = ROLE == 1 && cg == 15;
  int stype[NJ], sidx[NJ];
#pragma unroll
  for (int j = 0; j < NJ; j++) k1_slot(cg, J0 + j, stype[j], sidx[j]);
#define K1_BPRIO 1
  if (ROLE == 1) __builtin_amdgcn_s_setprio(K1_BPRIO);
  const int row = 32 * tt + r;
  unsigned xa[4], wa[4];
  const unsigned pitch = kv ? 40 : 64;
  const int blk = (lane >> 4) & 1, q = (lane & 15) >> 2, pp = lane & 3;
#pragma unroll
  for (int i = 0; i < 4; i++) {
    const unsigned ks = (i + tt) & 3;
    xa[i] = row * 128 + ((32 * ks + 16 * h) ^ (16 * ((row >> 1) & 7)));
    wa[i] = K1_XT + J0 * 4096 + (16 * ks + 8 * h + q) * pitch + 32 * blk + 8 * pp;
  }
  f32x16 acc[NJ];
#pragma unroll
  for (int j = 0; j < NJ; j++) acc[j] = f32x16{};
  f32x4 cs[NJ], s1 = {};
  float sq[4] = {0.f, 0.f, 0.f, 0.f};
#pragma unroll
  for (int j = 0; j < NJ; j++) cs[j] = f32x4{};
  const unsigned ONES = 0x3F803F80u;
  const unsigned selw = ((lane & 15) == ((lane >> 4) & 1)) ? ONES : 0u;
  const u32x4 SEL = {selw, selw, selw, selw};
  auto mainloop = [&](auto KVC) {
    constexpr bool KV = decltype(KVC)::value;
    u32x4 xf[4] = {}, wf[4][NJ] = {};
    auto rd = [&](LAS char* sl, int i) {
      xf[i] = LDS128(sl + xa[i]);
#pragma unroll
      for (int j = 0; j < NJ; j++) wf[i][j] = ldtr(sl + wa[i], j * 4096, j * 4096 + (KV ? 160 : 256));
    };
    auto mm = [&](int i, LAS char* nsl, int ni) {
#pragma unroll
      for (int j = 0; j < NJ; j++) {
        if (KV && j == 1) acc[j] = MFMA32(xf[i], wf[i][j], acc[j]);
        else acc[j] = MFMA32(wf[i][j], xf[i], acc[j]);
        SB();
        if (j == 0) { xf[ni] = LDS128(nsl + xa[ni]); wf[ni][0] = ldtr(nsl + wa[ni], 0, KV ? 160 : 256); }
        else { wf[ni][j] = ldtr(nsl + wa[ni], j * 4096, j * 4096 + (KV ? 160 : 256)); }
        SB();
      }
      if (ROLE == 1) {
#pragma unroll
        for (int e = 0; e < 4; e++) { sqacc(sq[(2 * e) & 3], bflo(xf[i][e])); sqacc(sq[(2 * e + 1) & 3], bfhi(xf[i][e])); }
        SB();
      }
    };
    BARRIER();
    rd(lds, 0); rd(lds, 1);
#pragma unroll 1
    for (int st = 0; st < 10; st++) {
      LAS char* sl = lds + (st & 3) * K1_STAGE;
      LAS char* sn = lds + ((st + 1) & 3) * K1_STAGE;
      mm(0, sl, 2);
      mm(1, sl, 3);
#pragma unroll
      for (int j = 0; j < NJ; j++) cs[j] = MFMA16(SEL, wf[0][j], cs[j]);
      if (ROLE == 1) { s1 = MFMA16(SEL, xf[0], s1); s1 = MFMA16(SEL, xf[1], s1); }
      SB();
      BARRIER();
      mm(2, sn, 0);
      mm(3, sn, 1);
      if (ROLE == 1) { s1 = MFMA16(SEL, xf[2], s1); s1 = MFMA16(SEL, xf[3], s1); }
      SB();
    }
  };
  if (kv) mainloop(std::true_type{}); else mainloop(std::false_type{});
  LAS float* csp = (LAS float*)(lds + K1_CSP);
  LAS float* csf = (LAS float*)(lds + K1_CSF);
  LAS f32x2* stt = (LAS f32x2*)(lds + K1_STP);
  if (lane < 16) {
#pragma unroll
    for (int j = 0; j < NJ; j++) { csp[tt * 160 + (J0 + j) * 32 + lane] = cs[j][0]; csp[tt * 160 + (J0 + j) * 32 + 16 + lane] = cs[j][1]; }
  }
  if (ROLE == 1) {
    const float sqs = half_sum((sq[0] + sq[1]) + (sq[2] + sq[3]));
    const float a0 = __shfl(s1[0], r & 15), a1 = __shfl(s1[1], r & 15), su = r < 16 ? a0 : a1;
    const float mu = su * (1.f / 640.f), var = fmaxf(sqs * (1.f / 640.f) - mu * mu, 0.f), rs = __builtin_amdgcn_rsqf(var + 1e-5f);
    if (h == 0) stt[row] = f32x2{mu * rs, rs};
  }
  BARRIER();
  { const int c = threadIdx.x; if (c < 160) csf[c] = (csp[c] + csp[160 + c]) + (csp[320 + c] + csp[480 + c]); }
  const f32x2 st_ = stt[row];
  BARRIER();
  const int tb = rb * 4 + tt;
#pragma unroll
  for (int j = 0; j < NJ; j++) {
    const int type = stype[j];
    if (type == T_E) continue;
    if (ROLE == 1 && type == T_V) {
      const float csum = csf[(J0 + j) * 32 + r];
      char* vb = p.ws + WS_KV + (size_t)rb * KV_CHUNK + KV_VOFF + tt * 1536 + h * 384 + r * 16;
#pragma unroll
      for (int s = 0; s < 2; s++) {
        float v[8];
#pragma unroll
        for (int i = 0; i < 8; i++) { const f32x2 t = stt[32 * tt + 16 * s + 8 * (i >> 2) + 4 * h + (i & 3)]; v[i] = t[1] * acc[j][8 * s + i] - t[0] * csum; }
        u32x4 w = {cvtpk(v[0], v[1]), cvtpk(v[2], v[3]), cvtpk(v[4], v[5]), cvtpk(v[6], v[7])};
        if (r == 20) w = u32x4{ONES, ONES, ONES, ONES};
        if (r > 20) w = u32x4{0u, 0u, 0u, 0u};
        if (r < 24) *(u32x4*)(vb + s * 768) = w;
      }
      continue;
    }
    float v[16];
#pragma unroll
    for (int g = 0; g < 4; g++) {
      const f32x4 c4 = *(LAS f32x4*)(csf + (J0 + j) * 32 + 8 * g + 4 * h);
#pragma unroll
      for (int i = 0; i < 4; i++) v[4 * g + i] = st_[1] * acc[j][4 * g + i] - st_[0] * c4[i];
    }
    if (ROLE == 1 && type == T_K) {
      char* kb = p.ws + WS_KV + (size_t)rb * KV_CHUNK + tt * 1536 + r * 16 + h * 8;
      *(u32x2*)(kb) = u32x2{cvtpk(v[0], v[1]), cvtpk(v[2], v[3])};
      *(u32x2*)(kb + 512) = u32x2{cvtpk(v[4], v[5]), cvtpk(v[6], v[7])};
      *(u32x2*)(kb + 1024) = h ? u32x2{0x00003F80u, 0u} : u32x2{cvtpk(v[8], v[9]), cvtpk(v[10], v[11])};
      continue;
    }
    char* dst;
    if (type == T_Q) {
      dst = p.ws + WS_QI + ((size_t)(tb * 160 + sidx[j] * 8 + h) * 32 + r) * 8;
#pragma unroll
      for (int i = 0; i < 16; i++) v[i] *= QSCALE;
    } else {
      dst = p.ws + (type == T_Z1 ? WS_Z1 : WS_Z2R) + ((size_t)(tb * 216 + sidx[j] * 8 + h) * 32 + r) * 8;
#pragma unroll
      for (int i = 0; i < 16; i++) v[i] = gelu_t(v[i]);
    }
#pragma unroll
    for (int g = 0; g < 4; g++) *(u32x2*)(dst + g * 512) = u32x2{cvtpk(v[4 * g], v[4 * g + 1]), cvtpk(v[4 * g + 2], v[4 * g + 3])};
  }
}
__global__ void __launch_bounds__(768, 3) k1_proj(Params p) {
  extern __shared__ __attribute__((aligned(16))) char lds[];
  const int b = blockIdx.x, x = b & 7, j = b >> 3;
  const int rb = (x & 1) * 8 + (j & 7), cg = (x >> 1) * 4 + (j >> 3);
  const int wid = RFL(threadIdx.x >> 6);
  if (b == 0 && threadIdx.x < 256) ((u32x4*)(p.ws + WS_FLAG))[threadIdx.x] = u32x4{0u, 0u, 0u, 0u};
  if (wid < 4) k1_body<0>(p, lds, rb, cg, wid); else if (wid < 8) k1_body<1>(p, lds, rb, cg, wid - 4); else k1_loader(p, lds, rb, cg, wid - 8);
}

constexpr int K2_LDS = 81920;
constexpr float SM_OFF = 90.f;
#define EX(x) __builtin_amdgcn_exp2f(x)
#define AUNIT(Sc, Sn, qn0, qn1, Oc, Op, LD0, LD1, LD2, LD3) do { \
    Sc[0] = EX(Sc[0]); Sc[1] = EX(Sc[1]); SB(); \
    Sn = MFMA32(k0, qn0, zero16); SB(); LD0; SB(); \
    Sc[2] = EX(Sc[2]); Sc[3] = EX(Sc[3]); SB(); p1[2] = cvtpk(e12, e13); p1[3] = cvtpk(e14, e15); SB(); \
    Sc[4] = EX(Sc[4]); Sc[5] = EX(Sc[5]); SB(); \
    Op = MFMA32(v1, p1, Op); SB(); LD1; SB(); \
    Sc[6] = EX(Sc[6]); Sc[7] = EX(Sc[7]); SB(); p0[0] = cvtpk(Sc[0], Sc[1]); p0[1] = cvtpk(Sc[2], Sc[3]); SB(); \
    Sc[8] = EX(Sc[8]); Sc[9] = EX(Sc[9]); SB(); \
    Sn = MFMA32(k1, qn1, Sn); SB(); LD2; SB(); \
    Sc[10] = EX(Sc[10]); Sc[11] = EX(Sc[11]); SB(); p0[2] = cvtpk(Sc[4], Sc[5]); p0[3] = cvtpk(Sc[6], Sc[7]); SB(); \
    e12 = EX(Sc[12]); e13 = EX(Sc[13]); SB(); \
    Oc = MFMA32(v0, p0, Oc); SB(); LD3; SB(); \
    e14 = EX(Sc[14]); e15 = EX(Sc[15]); SB(); p1[0] = cvtpk(Sc[8], Sc[9]); p1[1] = cvtpk(Sc[10], Sc[11]); SB(); \
  } while (0)
#define ATILE(kn, v1c, v0n) do { \
    AUNIT(sA, sB, qB0, qB1, oA, oB, k0 = LDS128(kn), v1 = LDS128(v1c), k1 = LDS128((kn) + k1d), (void)0); \
    AUNIT(sB, sA, qA0, qA1, oB, oA, (void)0, (void)0, (void)0, v0 = LDS128(v0n)); \
  } while (0)
DI void load_q(const char* qb, int h, u32x4& q0, u32x4& q1) {
  u32x2 ga = *(const u32x2*)(qb + (2 * h) * 256), gb = *(const u32x2*)(qb + (2 * h + 1) * 256), gc = *(const u32x2*)(qb + 4 * 256);
  q0 = u32x4{ga[0], ga[1], gb[0], gb[1]};
  q1 = h ? u32x4{0u, 0u, 0u, 0u} : u32x4{gc[0], gc[1], 0u, 0u};
}
DI f32x16 attn_exact(const char* kv, u32x4 q0, u32x4 q1, int r, int h) {
  const f32x16 zero16 = {};
  q1[2] = 0u;
  f32x16 o = {}; float m = -3.0e38f;
  const int rr = r < 21 ? r : 21;
#pragma unroll 1
  for (int t = 0; t < 64; t++) {
    const char* cb = kv + (size_t)(t >> 2) * KV_CHUNK;
    const char* kb = cb + (t & 3) * 1536 + r * 16;
    const u32x4 k0 = *(const u32x4*)(kb + h * 512), k1 = *(const u32x4*)(kb + 1024);
    const char* vb = cb + KV_VOFF + (t & 3) * 1536 + h * 384 + rr * 16;
    const u32x4 v0 = *(const u32x4*)(vb), v1 = *(const u32x4*)(vb + 768);
    f32x16 s = MFMA32(k0, q0, zero16); s = MFMA32(k1, q1, s);
    float mx = s[0];
#pragma unroll
    for (int i = 1; i < 16; i++) mx = fmaxf(mx, s[i]);
    const float mn = fmaxf(m, half_max(mx)), a = EX(m - mn);
    m = mn;
#pragma unroll
    for (int i = 0; i < 16; i++) { o[i] *= a; s[i] = EX(s[i] - mn); }
    const u32x4 p0 = {cvtpk(s[0], s[1]), cvtpk(s[2], s[3]), cvtpk(s[4], s[5]), cvtpk(s[6], s[7])};
    const u32x4 p1 = {cvtpk(s[8], s[9]), cvtpk(s[10], s[11]), cvtpk(s[12], s[13]), cvtpk(s[14], s[15])};
    o = MFMA32(v0, p0, o); o = MFMA32(v1, p1, o);
  }
  return o;
}
DI void attn_role(const Params& p, char* lds_, int wg) {
  const int tid = threadIdx.x, lane = tid & 63, r = lane & 31, h = lane >> 5;
  const int wid = RFL(tid >> 6);
  const int pi = wid & 3, kh = wid >> 2;
  const int tb = wg >> 2, headA = (wg & 3) * 8 + 2 * pi;
  const unsigned l0 = (unsigned)(uintptr_t)lds_;
  LAS char* lds = (LAS char*)lds_;
  const char* kvg = p.ws + WS_KV;
  const char* kvsrc = kvg + (size_t)kh * 8 * KV_CHUNK + pi * 3072;
  const unsigned voff = lane * 16;
  const unsigned ring = l0 + kh * 3 * KV_CHUNK + pi * 3072;
#define APRIO 2
  __builtin_amdgcn_s_setprio(APRIO);
  glds3(kvsrc, voff, ring);
  glds3(kvsrc + KV_CHUNK, voff, ring + KV_CHUNK);
  u32x4 qA0, qA1, qB0, qB1;
  const char* qb = p.ws + WS_QI + ((size_t)(tb * 160 + headA * 5) * 32 + r) * 8;
  load_q(qb, h, qA0, qA1); load_q(qb + 5 * 256, h, qB0, qB1);
  asm volatile("s_waitcnt vmcnt(0)\n\ts_barrier" ::: "memory");
  const f32x16 zero16 = {};
  const int k1d = 1024 - h * 512;
  const int rr = r < 21 ? r : 21;
  u32x4 k0, k1; f32x16 sA, sB;
  {
    LAS char* kb0 = lds + h * 512 + r * 16;
    k0 = LDS128(kb0); k1 = LDS128(kb0 + k1d);
    sA = MFMA32(k0, qA0, zero16); sA = MFMA32(k1, qA1, sA); sB = MFMA32(k0, qB0, zero16); sB = MFMA32(k1, qB1, sB);
    float mA = sA[0], mB = sB[0];
#pragma unroll
    for (int i = 1; i < 16; i++) { mA = fmaxf(mA, sA[i]); mB = fmaxf(mB, sB[i]); }
    k0 = LDS128(kb0 + 1536); k1 = LDS128(kb0 + 1536 + k1d);
    sA = MFMA32(k0, qA0, zero16); sA = MFMA32(k1, qA1, sA); sB = MFMA32(k0, qB0, zero16); sB = MFMA32(k1, qB1, sB);
#pragma unroll
    for (int i = 0; i < 16; i++) { mA = fmaxf(mA, sA[i]); mB = fmaxf(mB, sB[i]); }
    mA = half_max(mA); mB = half_max(mB);
    if (h == 0) { qA1[2] = cvtpk(-(mA + SM_OFF), 0.f); qB1[2] = cvtpk(-(mB + SM_OFF), 0.f); }
  }
  LAS char* kbase = lds + kh * 3 * KV_CHUNK + h * 512 + r * 16;
  LAS char* vbase = lds + kh * 3 * KV_CHUNK + KV_VOFF + h * 384 + rr * 16;
  k0 = LDS128(kbase); k1 = LDS128(kbase + k1d);
  sA = MFMA32(k0, qA0, zero16); sA = MFMA32(k1, qA1, sA);
  u32x4 v0 = LDS128(vbase), v1 = v0, p0 = {0u, 0u, 0u, 0u}, p1 = {0u, 0u, 0u, 0u};
  f32x16 oA = {}, oB = {};
  float e12 = 0.f, e13 = 0.f, e14 = 0.f, e15 = 0.f;
  int sc = 0;
#pragma unroll 1
  for (int c = 0; c < 8; c++) {
    const int sn = sc == 2 * KV_CHUNK ? 0 : sc + KV_CHUNK, sp = sn == 2 * KV_CHUNK ? 0 : sn + KV_CHUNK;
    if (c + 2 < 8) glds3(kvsrc + (size_t)(c + 2) * KV_CHUNK, voff, ring + sp);
    LAS char* kc = kbase + sc; LAS char* kn = kbase + sn; LAS char* vc = vbase + sc; LAS char* vn = vbase + sn;
    ATILE(kc + 1 * 1536, vc + 1 * 768, vc + 2 * 768);
    ATILE(kc + 2 * 1536, vc + 3 * 768, vc + 4 * 768);
    ATILE(kc + 3 * 1536, vc + 5 * 768, vc + 6 * 768);
    ATILE(kn, vc + 7 * 768, vn);
    asm volatile("s_waitcnt vmcnt(0) lgkmcnt(0)\n\ts_barrier" ::: "memory");
    sc = sn;
  }
  p1[2] = cvtpk(e12, e13); p1[3] = cvtpk(e14, e15);
  oB = MFMA32(v1, p1, oB);
  LAS f32x4* ex = (LAS f32x4*)(lds + (pi * 2 + kh) * 3072 + lane * 16);
  LAS const f32x4* exr = (LAS const f32x4*)(lds + (pi * 2 + (kh ^ 1)) * 3072 + lane * 16);
  f32x16 o;
  if (kh == 0) { o = oA;
#pragma unroll
    for (int g = 0; g < 3; g++) ex[g * 64] = f32x4{oB[4 * g], oB[4 * g + 1], oB[4 * g + 2], oB[4 * g + 3]};
  } else { o = oB;
#pragma unroll
    for (int g = 0; g < 3; g++) ex[g * 64] = f32x4{oA[4 * g], oA[4 * g + 1], oA[4 * g + 2], oA[4 * g + 3]};
  }
  BARRIER();
#pragma unroll
  for (int g = 0; g < 3; g++) { f32x4 t = exr[g * 64]; o[4 * g] += t[0]; o[4 * g + 1] += t[1]; o[4 * g + 2] += t[2]; o[4 * g + 3] += t[3]; }
  {
    float big = 0.f;
#pragma unroll
    for (int i = 0; i < 12; i++) big = fmaxf(big, fabsf(o[i]));
    float chk = 0.f;
#pragma unroll
    for (int i = 0; i < 12; i++) chk += o[i] * 0.f;
    const bool bad = !(big < 1.0e36f) || !(chk == 0.f);
    if (__builtin_expect(__any(bad), 0)) o = attn_exact(kvg, kh ? qB0 : qA0, kh ? qB1 : qA1, r, h);
  }
  auto lr = __builtin_amdgcn_permlane32_swap(__float_as_uint(o[8]), __float_as_uint(o[8]), false, false);
  const float il = __builtin_amdgcn_rcpf(__uint_as_float(lr[1]));
  const int tok = tb * 32 + r, head = headA + kh;
#pragma unroll
  for (int g = 0; g < 3; g++) {
    if (g < 2 || h == 0) {
      const int kk = head * 20 + (2 * g + h) * 4;
      u32x2 w = {cvtpk(o[4 * g] * il, o[4 * g + 1] * il), cvtpk(o[4 * g + 2] * il, o[4 * g + 3] * il)};
      *(u32x2*)(p.ws + WS_AO + ((size_t)(kk >> 3) * 2048 + tok) * 16 + ((kk >> 2) & 1) * 8) = w;
    }
  }
}

DI int fb_start(int fb) { return (fb >> 2) * 216 + (fb & 3) * 56; }
constexpr int SP_STAGE = 24576, SP_ZOFF = 16384;
typedef __attribute__((address_space(1))) unsigned gu32;
DI void sp_role(const Params& p, char* lds_, int b) {
  const int wid = RFL(threadIdx.x >> 6);
  __builtin_amdgcn_s_setprio(3);
  const int tid = threadIdx.x, lane = tid & 63, r = lane & 31, h = lane >> 5;
  const unsigned l0 = (unsigned)(uintptr_t)lds_;
  LAS char* lds = (LAS char*)lds_;
  const int x = b & 7, jj = b >> 3;
  const int tbk = (x >> 1) * 4 + (jj & 3), fb = (x & 1) * 8 + (jj >> 2);
  gu32* cnt = (gu32*)(p.ws + WS_FLAG);
  if (wid >= 4) {
    const int lw = wid - 4;
    const char* wb = p.wsp + (size_t)tbk * 128 * 4096;
    unsigned wv4[4];
#pragma unroll
    for (int i = 0; i < 4; i++) { const unsigned row = 8 * (4 * lw + i) + (lane >> 3); wv4[i] = row * 4096 + (((lane & 7) ^ ((row >> 1) & 7)) << 4); }
    const char* zb = p.ws + WS_Z2N + (size_t)fb * 32 * 8192 + lw * 2048;
    const unsigned zv = lane * 16;
    auto issue_w = [&](int st) {
#pragma unroll
      for (int i = 0; i < 4; i++) gldsn(wb + st * 128, wv4[i], l0 + (st % 3) * SP_STAGE + (4 * lw + i) * 1024);
    };
    auto issue_z = [&](int st) {
      asm volatile("s_mov_b32 m0, %2\n\ts_nop 0\n\tglobal_load_lds_dwordx4 %0, %1\n\tglobal_load_lds_dwordx4 %0, %1 offset:1024"
                   :: "v"(zv), "s"(zb + (size_t)st * 8192), "s"(l0 + (st % 3) * SP_STAGE + SP_ZOFF + lw * 2048) : "memory");
    };
    {
      const int u = b * 3 + lw;
      if (lw < 3 && u < 752) {
        const int ks = u >> 3, nb = u & 7;
        const char* src = (ks < 40 ? p.wo + (size_t)ks * 16 * 1280 : p.wout + (size_t)(ks - 40) * 16 * 1280) + nb * 160;
        LAS char* sc = lds + lw * 4096;
#pragma unroll
        for (int i = 0; i < 3; i++) {
          const int c = lane + 64 * i, rw = c / 10, cc = c - rw * 10;
          if (c < 160) *(LAS u32x4*)(sc + c * 16) = *(const u32x4*)(src + rw * 1280 + cc * 16);
        }
        asm volatile("s_waitcnt vmcnt(0) lgkmcnt(0)" ::: "memory");
        const int blk = (lane >> 4) & 1, q = (lane & 15) >> 2, pp = lane & 3;
        LAS char* ta = sc + (8 * h + q) * 160 + 32 * blk + 8 * pp;
        char* dst = p.ws + WS_W3 + ((size_t)nb * 94 + ks) * 2560;
        const u32x4 f0 = ldtr(ta, 0, 640), f1 = ldtr(ta, 64, 64 + 640), f2 = ldtr(ta, 128, 128 + 640);
        *(u32x4*)(dst + lane * 16) = f0;
        *(u32x4*)(dst + 1024 + lane * 16) = f1;
        if (r < 16) *(u32x4*)(dst + 2048 + (h * 16 + r) * 16) = f2;
        asm volatile("s_waitcnt vmcnt(0) lgkmcnt(0)" ::: "memory");
      }
    }
    BARRIER();
    issue_w(0); issue_w(1);
    BARRIER(); BARRIER();
    if (lw == 0) {
      __builtin_amdgcn_s_sleep(24);
      unsigned spins = 0;
      for (;;) {
        const unsigned c = __hip_atomic_load(cnt + (lane & 31) * 16, __ATOMIC_RELAXED, __HIP_MEMORY_SCOPE_AGENT);
        if (__all(c >= 8u)) break;
        if (++spins > (1u << 21)) break;
        __builtin_amdgcn_s_sleep(6);
      }
      __builtin_amdgcn_fence(__ATOMIC_ACQUIRE, "agent");
      const char* z00 = p.ws + WS_Z2N + (size_t)fb * 32 * 8192;
#pragma unroll
      for (int i = 0; i < 4; i++)
        asm volatile("s_mov_b32 m0, %2\n\ts_nop 0\n\tglobal_load_lds_dwordx4 %0, %1\n\tglobal_load_lds_dwordx4 %0, %1 offset:1024\n\tglobal_load_lds_dwordx4 %0, %1 offset:2048\n\tglobal_load_lds_dwordx4 %0, %1 offset:3072"
                     :: "v"(zv), "s"(z00 + i * 4096), "s"(l0 + (i >> 1) * SP_STAGE + SP_ZOFF + (i & 1) * 4096) : "memory");
      WAITV(8);
    }
#pragma unroll 1
    for (int st = 0; st < 32; st++) {
      if (st == 0) { } else if (st == 1) WAITV(6); else if (st < 31) WAITV(6); else WAITV(0);
      BARRIER();
      if (st + 2 < 32) { issue_w(st + 2); issue_z(st + 2); }
    }
    return;
  }
  {
    LAS float* red = (LAS float*)(lds + K2_LDS - 256);
    const int fg = tid < 216 ? tid : 215;
    const char* src = p.ws + WS_Z2R + ((size_t)((b >> 2) * 216 + fg) * 32 + (b & 3) * 8) * 8;
    u32x4 raw[4];
#pragma unroll
    for (int i = 0; i < 4; i++) raw[i] = *(const u32x4*)(src + i * 16);
    const unsigned ONES = tid < 216 ? 0x3F803F80u : 0u;
    LAS float* part = (LAS float*)(lds + 2 * SP_STAGE);
#pragma unroll
    for (int t = 0; t < 8; t++) {
      const unsigned a = raw[t >> 1][2 * (t & 1)], c = raw[t >> 1][2 * (t & 1) + 1];
      const unsigned am = tid < 216 ? a : 0u, cm = tid < 216 ? c : 0u;
      part[(2 * t) * 256 + tid] = dot2(c, ONES, dot2(a, ONES, 0.f));
      part[(2 * t + 1) * 256 + tid] = dot2(cm, cm, dot2(am, am, 0.f));
    }
    BARRIER();
    {
      float s4[4];
#pragma unroll
      for (int k = 0; k < 4; k++) { const f32x4 a = *(LAS f32x4*)(part + (4 * wid + k) * 256 + lane * 4); s4[k] = (a[0] + a[1]) + (a[2] + a[3]); }
#pragma unroll
      for (int k = 0; k < 4; k++) s4[k] = wave_sum(s4[k]);
      if (lane < 2) {
        const float su = lane ? s4[2] : s4[0], sq = lane ? s4[3] : s4[1];
        const float mu = su * (1.f / 864.f), var = fmaxf(sq * (1.f / 864.f) - mu * mu, 0.f), rs = __builtin_amdgcn_rsqf(var + 1e-5f);
        ((LAS f32x2*)red)[2 * wid + lane] = f32x2{rs, -mu * rs};
      }
    }
    BARRIER();
    LAS char* tr = lds + 2 * SP_STAGE + 16384;
    {
      float v[8][4];
#pragma unroll
      for (int t = 0; t < 8; t++) {
        const f32x2 c = ((LAS f32x2*)red)[t];
        const unsigned a = raw[t >> 1][2 * (t & 1)], d = raw[t >> 1][2 * (t & 1) + 1];
        v[t][0] = __builtin_fmaf(bflo(a), c[0], c[1]); v[t][1] = __builtin_fmaf(bfhi(a), c[0], c[1]);
        v[t][2] = __builtin_fmaf(bflo(d), c[0], c[1]); v[t][3] = __builtin_fmaf(bfhi(d), c[0], c[1]);
      }
      if (tid < 216) {
#pragma unroll
        for (int i = 0; i < 4; i++)
          *(LAS u32x4*)(tr + (4 * fg + i) * 16) = u32x4{cvtpk(v[0][i], v[1][i]), cvtpk(v[2][i], v[3][i]), cvtpk(v[4][i], v[5][i]), cvtpk(v[6][i], v[7][i])};
      }
    }
    asm volatile("s_waitcnt lgkmcnt(0)" ::: "memory");
    {
      const __amdgpu_buffer_rsrc_t rsrc = __builtin_amdgcn_make_buffer_rsrc((void*)(p.ws + WS_Z2N), 0, 4194304, 0x00020000);
      const unsigned sbase = (((b >> 3) * 4 + ((b >> 1) & 3)) * 2 + (b & 1)) * 1024;
#pragma unroll
      for (int i = 0; i < 4; i++) {
        const int f = 256 * wid + 64 * i + lane;
        if (f < 864) {
          const u32x4 w = *(LAS u32x4*)(tr + f * 16);
          const int q4 = (f >= 216) + (f >= 432) + (f >= 648), rem = f - q4 * 216, sub = (rem >= 56) + (rem >= 112) + (rem >= 168);
          const unsigned off = (unsigned)(q4 * 4 + sub) * (32u * 8192u) + sbase + (rem - sub * 56) * 16;
          __builtin_amdgcn_raw_buffer_store_b128(w, rsrc, off, 0, 16);
        }
      }
    }
    asm volatile("s_waitcnt vmcnt(0)" ::: "memory");
    BARRIER();
    if (tid == 0) __hip_atomic_fetch_add(cnt + (b >> 3) * 16, 1u, __ATOMIC_RELAXED, __HIP_MEMORY_SCOPE_AGENT);
  }
  const unsigned row = 32 * wid + r;
  unsigned wa[4];
#pragma unroll
  for (int ks = 0; ks < 4; ks++) wa[ks] = row * 128 + ((32 * ks + 16 * h) ^ (16 * ((row >> 1) & 7)));
  const unsigned za = SP_ZOFF + h * 1024 + r * 16;
  f32x16 acc0 = {}, acc1 = {};
#pragma unroll 1
  for (int st = 0; st < 32; st++) {
    BARRIER();
    LAS char* sl = lds + (st % 3) * SP_STAGE;
    u32x4 wf[4], z0[4], z1[4];
#pragma unroll
    for (int ks = 0; ks < 4; ks++) { wf[ks] = LDS128(sl + wa[ks]); z0[ks] = LDS128(sl + za + ks * 2048); z1[ks] = LDS128(sl + za + ks * 2048 + 512); }
#pragma unroll
    for (int ks = 0; ks < 4; ks++) { acc0 = MFMA32(z0[ks], wf[ks], acc0); acc1 = MFMA32(z1[ks], wf[ks], acc1); }
  }
  const int tok = tbk * 128 + row, f0 = fb_start(fb), width = (fb & 3) == 3 ? 48 : 56;
  const float bias = bflo(*(const unsigned short*)(p.bsp + tok * 2));
  const char* z1b = p.ws + WS_Z1 + ((size_t)((tok >> 5) * 216 + (f0 >> 2) + h) * 32 + r) * 8;
#pragma unroll
  for (int j = 0; j < 2; j++) {
    const f32x16& a = j ? acc1 : acc0;
    u32x2 w[4];
#pragma unroll
    for (int g = 0; g < 4; g++) {
      const int fl = 32 * j + 8 * g;
      u32x2 z = {0u, 0u};
      if (fl < width) z = *(const u32x2*)(z1b + (8 * j + 2 * g) * 256);
      w[g] = u32x2{cvtpk(bflo(z[0]) * (a[4 * g] + bias), bfhi(z[0]) * (a[4 * g + 1] + bias)), cvtpk(bflo(z[1]) * (a[4 * g + 2] + bias), bfhi(z[1]) * (a[4 * g + 3] + bias))};
    }
#pragma unroll
    for (int g = 0; g < 4; g += 2) {
      auto s0 = __builtin_amdgcn_permlane32_swap(w[g][0], w[g + 1][0], false, false);
      auto s1 = __builtin_amdgcn_permlane32_swap(w[g][1], w[g + 1][1], false, false);
      const int fl = 32 * j + 8 * (g + h);
      if (fl < width) *(u32x4*)(p.ws + WS_AO + ((size_t)(80 + ((f0 + fl) >> 3)) * 2048 + tok) * 16) = u32x4{s0[0], s1[0], s0[1], s1[1]};
    }
  }
}
__global__ void __launch_bounds__(512, 4) k2_mix(Params p) {
  extern __shared__ __attribute__((aligned(16))) char lds[];
  if (blockIdx.x < 256) sp_role(p, lds, blockIdx.x); else attn_role(p, lds, blockIdx.x - 256);
}

constexpr int K3_ACT = 16384, K3_W = 20480, K3_STAGE = K3_ACT + K3_W, K3_NS = 4;
constexpr int K3_LDS = K3_NS * K3_STAGE + 256;
__global__ void __launch_bounds__(640, 2) k3_out(Params p) {
  extern __shared__ __attribute__((aligned(16))) char lds_[];
  const int b = blockIdx.x, x = b & 7, jj = b >> 3;
  const int tb = x * 4 + (jj & 3), nb = jj >> 2;
  const int tid = threadIdx.x, lane = tid & 63, r = lane & 31, h = lane >> 5;
  const int wid = RFL(tid >> 6);
  const unsigned l0 = (unsigned)(uintptr_t)lds_;
  LAS char* lds = (LAS char*)lds_;
  if (wid >= 6) {
    const int lw = wid - 6;
    const char* ab = p.ws + WS_AO + (size_t)tb * 64 * 16;
    const char* wb = p.ws + WS_W3 + (size_t)nb * 94 * 2560;
    const unsigned lv = lane * 16;
    auto issue = [&](int st) {
      const unsigned slot = l0 + (st % K3_NS) * K3_STAGE;
#pragma unroll
      for (int i = 0; i < 4; i++) {
        const int pc = lw * 4 + i;
        gldsn(ab + ((size_t)st * 16 + (st == 11 && pc >= 12 ? 0 : pc)) * 32768, lv, slot + pc * 1024);
      }
      const char* ws_ = wb + (size_t)st * 20480 + (st == 11 && lw == 3 ? 0 : lw * 5120);
      asm volatile("s_mov_b32 m0, %2\n\ts_nop 0\n\tglobal_load_lds_dwordx4 %0, %1\n\tglobal_load_lds_dwordx4 %0, %1 offset:1024\n\tglobal_load_lds_dwordx4 %0, %1 offset:2048\n\tglobal_load_lds_dwordx4 %0, %1 offset:3072"
                   :: "v"(lv), "s"(ws_), "s"(slot + K3_ACT + lw * 5120) : "memory");
      gldsn(ws_ + 4096, lv, slot + K3_ACT + lw * 5120 + 4096);
    };
    issue(0); issue(1); issue(2);
#pragma unroll 1
    for (int st = 0; st < 12; st++) {
      if (st < 10) WAITV(18); else if (st == 10) WAITV(9); else WAITV(0);
      BARRIER();
      if (st + 3 < 12) issue(st + 3);
    }
    return;
  }
  const int nt = wid % 3, tt = wid / 3;
  u32x4 xr[2];
#pragma unroll
  for (int k = 0; k < 2; k++) { const int i = tid + 384 * k < 640 ? tid + 384 * k : 0, row = i / 10, c = i - row * 10;
    xr[k] = *(const u32x4*)(p.x + ((size_t)(tb * 64 + row) * 640 + nb * 80 + c * 8) * 2); }
  const unsigned wa = K3_ACT + nt * 1024 + (nt < 2 ? lane : h * 16 + (r & 15)) * 16;
  const unsigned aa = h * 1024 + (32 * tt + r) * 16;
  f32x16 acc = {};
#pragma unroll 1
  for (int st = 0; st < 12; st++) {
    BARRIER();

    LAS char* sl = lds + (st % K3_NS) * K3_STAGE;
    u32x4 af[8], wf[8];
#pragma unroll
    for (int ks = 0; ks < 6; ks++) { af[ks] = LDS128(sl + aa + ks * 2048); wf[ks] = LDS128(sl + wa + ks * 2560); }
    SB();
#pragma unroll
    for (int ks = 0; ks < 2; ks++) acc = MFMA32(wf[ks], af[ks], acc);
    SB();
    if (st < 11) {
#pragma unroll
      for (int ks = 6; ks < 8; ks++) { af[ks] = LDS128(sl + aa + ks * 2048); wf[ks] = LDS128(sl + wa + ks * 2560); }
    }
    SB();
#pragma unroll
    for (int ks = 2; ks < 6; ks++) acc = MFMA32(wf[ks], af[ks], acc);
    if (st < 11) {
#pragma unroll
      for (int ks = 6; ks < 8; ks++) acc = MFMA32(wf[ks], af[ks], acc);
    }
  }
  BARRIER();
  LAS float* ot = (LAS float*)lds;
  constexpr int OP = 84;
#pragma unroll
  for (int g = 0; g < 4; g++) {
    const int n = nt * 32 + 8 * g + 4 * h;
    if (n < 80) *(LAS f32x4*)(ot + (32 * tt + r) * OP + n) = f32x4{acc[4 * g], acc[4 * g + 1], acc[4 * g + 2], acc[4 * g + 3]};
  }
  BARRIER();
#pragma unroll
  for (int k = 0; k < 2; k++) {
    const int i = tid + 384 * k;
    if (i < 640) {
      const int row = i / 10, c = i - row * 10;
      const size_t go = ((size_t)(tb * 64 + row) * 640 + nb * 80 + c * 8) * 2;
      const f32x4 a = *(LAS f32x4*)(ot + row * OP + c * 8), a2 = *(LAS f32x4*)(ot + row * OP + c * 8 + 4);
      const u32x4 w = {cvtpk(a[0] + bflo(xr[k][0]), a[1] + bfhi(xr[k][0])), cvtpk(a[2] + bflo(xr[k][1]), a[3] + bfhi(xr[k][1])),
                       cvtpk(a2[0] + bflo(xr[k][2]), a2[1] + bfhi(xr[k][2])), cvtpk(a2[2] + bflo(xr[k][3]), a2[3] + bfhi(xr[k][3]))};
      *(u32x4*)(p.out + go) = w;
    }
  }
}

extern "C" void kernel_launch(void* const* d_in, const int* in_sizes, int n_in, void* d_out, int out_size, void* d_ws, size_t ws_size, hipStream_t stream) {
  Params p;
  p.x = (const char*)d_in[0]; p.wq = (const char*)d_in[1]; p.wk = (const char*)d_in[2]; p.wv = (const char*)d_in[3]; p.wo = (const char*)d_in[4];
  p.win = (const char*)d_in[6]; p.wsp = (const char*)d_in[7]; p.bsp = (const char*)d_in[8]; p.wout = (const char*)d_in[9];
  p.ws = (char*)d_ws; p.out = (char*)d_out;
  (void)hipFuncSetAttribute((const void*)k1_proj, hipFuncAttributeMaxDynamicSharedMemorySize, K1_LDS);
  (void)hipFuncSetAttribute((const void*)k2_mix, hipFuncAttributeMaxDynamicSharedMemorySize, K2_LDS);
  (void)hipFuncSetAttribute((const void*)k3_out, hipFuncAttributeMaxDynamicSharedMemorySize, K3_LDS);
  k1_proj<<<256, 768, K1_LDS, stream>>>(p);
  k2_mix<<<512, 512, K2_LDS, stream>>>(p);
  k3_out<<<256, 640, K3_LDS, stream>>>(p);
}
```

```cpp
#include <hip/hip_runtime.h>
#include <stdint.h>
#include <type_traits>
typedef __bf16 bf16x8 __attribute__((ext_vector_type(8)));
typedef __bf16 bf16x2 __attribute__((ext_vector_type(2)));
typedef short s16x4 __attribute__((ext_vector_type(4)));
typedef float f32x16 __attribute__((ext_vector_type(16)));
typedef float f32x4 __attribute__((ext_vector_type(4)));
typedef float f32x2 __attribute__((ext_vector_type(2)));
typedef unsigned u32x4 __attribute__((ext_vector_type(4)));
typedef unsigned u32x2 __attribute__((ext_vector_type(2)));
#define DI __device__ __forceinline__
#define LAS __attribute__((address_space(3)))
#define SB() __builtin_amdgcn_sched_barrier(0)
#define RFL(x) __builtin_amdgcn_readfirstlane(x)
#define MFMA32(a, b, c) __builtin_amdgcn_mfma_f32_32x32x16_bf16(__builtin_bit_cast(bf16x8, a), __builtin_bit_cast(bf16x8, b), c, 0, 0, 0)
#define MFMA16(a, b, c) __builtin_amdgcn_mfma_f32_16x16x32_bf16(__builtin_bit_cast(bf16x8, a), __builtin_bit_cast(bf16x8, b), c, 0, 0, 0)
DI f32x4 mfma4sq(unsigned x0, unsigned x1, f32x4 c) { u32x2 v = {x0, x1}; s16x4 a = __builtin_bit_cast(s16x4, v); return __builtin_amdgcn_mfma_f32_4x4x4bf16_1k(a, a, c, 0, 0, 0); }
#define MFMA4(x0, x1, c) mfma4sq(x0, x1, c)
#define LDS128(p) (*(const LAS u32x4*)(p))
DI unsigned cvtpk(float lo, float hi) { f32x2 v = {lo, hi}; bf16x2 b = __builtin_convertvector(v, bf16x2); return __builtin_bit_cast(unsigned, b); }
DI float bflo(unsigned u) { return __uint_as_float(u << 16); }
DI float bfhi(unsigned u) { return __uint_as_float(u & 0xffff0000u); }
DI void sqacc(float& a, float x) { asm("v_fmac_f32_e32 %0, %1, %1" : "+v"(a) : "v"(x)); }
DI float dot2(unsigned a, unsigned b, float c) { return __builtin_amdgcn_fdot2_f32_bf16(__builtin_bit_cast(bf16x2, a), __builtin_bit_cast(bf16x2, b), c, false); }
DI float half_sum(float m) { auto r = __builtin_amdgcn_permlane32_swap(__float_as_uint(m), __float_as_uint(m), false, false); return __uint_as_float(r[0]) + __uint_as_float(r[1]); }
DI float half_max(float m) { auto r = __builtin_amdgcn_permlane32_swap(__float_as_uint(m), __float_as_uint(m), false, false); return fmaxf(__uint_as_float(r[0]), __uint_as_float(r[1])); }
template <int CTRL, int RMASK> DI float dpp_add(float v) { return v + __uint_as_float(__builtin_amdgcn_update_dpp(0u, __float_as_uint(v), CTRL, RMASK, 0xf, true)); }
DI float wave_sum(float v) {
  v = dpp_add<0x111, 0xf>(v); v = dpp_add<0x112, 0xf>(v); v = dpp_add<0x114, 0xf>(v); v = dpp_add<0x118, 0xf>(v);
  v = dpp_add<0x142, 0xa>(v); v = dpp_add<0x143, 0xc>(v);
  return __uint_as_float(__builtin_amdgcn_readlane(__float_as_uint(v), 63));
}
DI void glds1(const void* sbase, unsigned voff, unsigned lds_base) {
  unsigned sv;
  asm volatile("s_mov_b32 %0, m0\n\ts_mov_b32 m0, %3\n\ts_nop 0\n\tglobal_load_lds_dwordx4 %1, %2\n\ts_mov_b32 m0, %0"
               : "=&s"(sv) : "v"(voff), "s"(sbase), "s"(lds_base) : "memory"); }
DI void glds3(const void* sbase, unsigned voff, unsigned lds_base) {
  unsigned sv;
  asm volatile("s_mov_b32 %0, m0\n\ts_mov_b32 m0, %3\n\ts_nop 0\n\tglobal_load_lds_dwordx4 %1, %2\n\tglobal_load_lds_dwordx4 %1, %2 offset:1024\n\tglobal_load_lds_dwordx4 %1, %2 offset:2048\n\ts_mov_b32 m0, %0"
               : "=&s"(sv) : "v"(voff), "s"(sbase), "s"(lds_base) : "memory"); }
DI void gldsn(const void* sbase, unsigned voff, unsigned lds_base) {
  asm volatile("s_mov_b32 m0, %2\n\ts_nop 0\n\tglobal_load_lds_dwordx4 %0, %1" :: "v"(voff), "s"(sbase), "s"(lds_base) : "memory"); }
DI u32x4 ldtr(const LAS char* p, int off0, int off1) {
  typedef LAS s16x4* lp;
  s16x4 a = __builtin_amdgcn_ds_read_tr16_b64_v4i16((lp)(p + off0)), b = __builtin_amdgcn_ds_read_tr16_b64_v4i16((lp)(p + off1));
  u32x2 x = __builtin_bit_cast(u32x2, a), y = __builtin_bit_cast(u32x2, b);
  return u32x4{x[0], x[1], y[0], y[1]};
}
DI void st16_wt(char* base, unsigned off, u32x4 w, unsigned bytes) {
  const __amdgpu_buffer_rsrc_t rs = __builtin_amdgcn_make_buffer_rsrc((void*)base, 0, bytes, 0x00020000);
  __builtin_amdgcn_raw_buffer_store_b128(w, rs, off, 0, 16);
}
DI u32x4 pair8(u32x2 a, u32x2 b) {
  auto s0 = __builtin_amdgcn_permlane32_swap(a[0], b[0], false, false);
  auto s1 = __builtin_amdgcn_permlane32_swap(a[1], b[1], false, false);
  return u32x4{s0[0], s1[0], s0[1], s1[1]};
}
#define WAITV(n) asm volatile("s_waitcnt vmcnt(" #n ")" ::: "memory")
#define BARRIER() asm volatile("s_waitcnt lgkmcnt(0)\n\ts_barrier" ::: "memory")

constexpr int SEQ = 2048, DM = 640, NFH = 864;
constexpr size_t WS_FLAG = 0;
constexpr size_t WS_QI  = 4096;
constexpr size_t WS_KV  = WS_QI + 2621440;
constexpr size_t WS_Z1  = WS_KV + 196608;
constexpr size_t WS_Z2R = WS_Z1 + 3538944;
constexpr size_t WS_Z2N = WS_Z2R + 3538944;
constexpr size_t WS_AO  = WS_Z2N + 4194304;
constexpr size_t WS_W3  = WS_AO + 6160384;
constexpr size_t WS_END = WS_W3 + 8 * 94 * 2560;
constexpr int KV_CHUNK = 12288, KV_VOFF = 6144;
struct Params {
  const char *x, *wq, *wk, *wv, *wo, *win, *wsp, *bsp, *wout;
  char* ws; char* out;
};

constexpr int K1_STAGE = 36864, K1_NS = 4, K1_XT = 16384;
constexpr int K1_CSP = K1_NS * K1_STAGE;
constexpr int K1_CSF = K1_CSP + 2560;
constexpr int K1_STP = K1_CSF + 640;
constexpr int K1_LDS = K1_STP + 1024;
constexpr float QSCALE = 0.22360679774997896f * 1.4426950408889634f;
enum { T_Q = 0, T_K = 1, T_V = 2, T_Z1 = 3, T_Z2 = 4, T_E = 5 };
DI void k1_slot(int cg, int j, int& type, int& idx) {
  if (j < 3 || (j == 3 && cg < 6)) { const int z = cg < 6 ? cg * 4 + j : 24 + (cg - 6) * 3 + j; type = z < 27 ? T_Z1 : T_Z2; idx = z < 27 ? z : z - 27; }
  else if (cg == 15) { type = j == 3 ? T_K : T_V; idx = 0; }
  else if (cg < 4) { type = T_E; idx = 0; }
  else if (cg < 6) { type = T_Q; idx = cg - 4; }
  else { type = T_Q; idx = 2 + (cg - 6) * 2 + (j - 3); }
}
DI float gelu_t(float x) {
  const float p = __builtin_fmaf(x * x, -0.10294324f, -2.3022082f);
  const float e = __builtin_amdgcn_exp2f(x * p);
  return x * __builtin_amdgcn_rcpf(1.f + e);
}
DI void k1_loader(const Params& p, char* lds_, int rb, int cg, int lw) {
  const int lane = threadIdx.x & 63;
  __builtin_amdgcn_s_setprio(3);
  const unsigned l0 = (unsigned)(uintptr_t)lds_;
  const char* base[5]; unsigned stride[5], voff[5];
  const uintptr_t pwq = (uintptr_t)p.wq, pwin = (uintptr_t)p.win, pwk = (uintptr_t)p.wk, pwv = (uintptr_t)p.wv;
#pragma unroll
  for (int j = 0; j < 5; j++) {
    int type, idx; k1_slot(cg, j, type, idx);
    const unsigned rowp = 16 * lw + (lane >> 2), ch = (lane & 3) * 16;
    const bool kvs = j >= 3 && cg == 15, isq = type == T_Q || type == T_E;
    const uintptr_t a = kvs ? (j == 3 ? pwk : pwv) : isq ? pwq + idx * 64 : pwin + (type == T_Z2 ? 1728 : 0) + idx * 64;
    base[j] = (const char*)a;
    stride[j] = kvs ? 2560u : isq ? 64u * 1280u : 64u * 3456u;
    const unsigned o = (lw < 2 ? lw : 2) * 1024 + lane * 16;
    voff[j] = kvs ? (o < 2544u ? o : 2544u) : rowp * (isq ? 1280u : 3456u) + ch;
  }
  const char* xb = p.x + (size_t)rb * 128 * 1280;
  unsigned xv[4];
#pragma unroll
  for (int i = 0; i < 4; i++) { const unsigned row = 8 * (4 * lw + i) + (lane >> 3); xv[i] = row * 1280 + (((lane & 7) ^ ((row >> 1) & 7)) << 4); }
  auto issue = [&](int st) {
    const unsigned slot = l0 + (st & 3) * K1_STAGE;
#pragma unroll
    for (int i = 0; i < 4; i++) gldsn(xb + st * 128, xv[i], slot + (4 * lw + i) * 1024);
#pragma unroll
    for (int j = 0; j < 5; j++) gldsn(base[j] + (size_t)st * stride[j], voff[j], slot + K1_XT + j * 4096 + lw * 1024);
  };
  issue(0); issue(1); issue(2);
  WAITV(18);
  BARRIER();
#pragma unroll 1
  for (int st = 0; st < 10; st++) {
    if (st < 8) WAITV(9); else WAITV(0);
    BARRIER();
    if (st + 3 < 10) issue(st + 3);
  }
}
template <int ROLE>
DI void k1_body(const Params& p, char* lds_, int rb, int cg, int tt) {
  constexpr int NJ = ROLE == 0 ? 3 : 2, J0 = ROLE == 0 ? 0 : 3;
  const int lane = threadIdx.x & 63, r = lane & 31, h = lane >> 5;
  LAS char* lds = (LAS char*)lds_;
  const bool kv = ROLE == 1 && cg == 15;
  int stype[NJ], sidx[NJ];
#pragma unroll
  for (int j = 0; j < NJ; j++) k1_slot(cg, J0 + j, stype[j], sidx[j]);
#define K1_BPRIO 1
  if (ROLE == 1) __builtin_amdgcn_s_setprio(K1_BPRIO);
  const int row = 32 * tt + r;
  unsigned xa[4], wa[4];
  const unsigned pitch = kv ? 40 : 64;
  const int blk = (lane >> 4) & 1, q = (lane & 15) >> 2, pp = lane & 3;
#pragma unroll
  for (int i = 0; i < 4; i++) {
    const unsigned ks = (i + tt) & 3;
    xa[i] = row * 128 + ((32 * ks + 16 * h) ^ (16 * ((row >> 1) & 7)));
    wa[i] = K1_XT + J0 * 4096 + (16 * ks + 8 * h + q) * pitch + 32 * blk + 8 * pp;
  }
  f32x16 acc[NJ];
#pragma unroll
  for (int j = 0; j < NJ; j++) acc[j] = f32x16{};
  f32x4 cs[NJ], s1 = {};
  float sq[4] = {0.f, 0.f, 0.f, 0.f};
#pragma unroll
  for (int j = 0; j < NJ; j++) cs[j] = f32x4{};
  const unsigned ONES = 0x3F803F80u;
  const unsigned selw = ((lane & 15) == ((lane >> 4) & 1)) ? ONES : 0u;
  const u32x4 SEL = {selw, selw, selw, selw};
  auto mainloop = [&](auto KVC) {
    constexpr bool KV = decltype(KVC)::value;
    u32x4 xf[4] = {}, wf[4][NJ] = {};
    auto rd = [&](LAS char* sl, int i) {
      xf[i] = LDS128(sl + xa[i]);
#pragma unroll
      for (int j = 0; j < NJ; j++) wf[i][j] = ldtr(sl + wa[i], j * 4096, j * 4096 + (KV ? 160 : 256));
    };
    auto mm = [&](int i, LAS char* nsl, int ni) {
#pragma unroll
      for (int j = 0; j < NJ; j++) {
        if (KV && j == 1) acc[j] = MFMA32(xf[i], wf[i][j], acc[j]);
        else acc[j] = MFMA32(wf[i][j], xf[i], acc[j]);
        SB();
        if (j == 0) { xf[ni] = LDS128(nsl + xa[ni]); wf[ni][0] = ldtr(nsl + wa[ni], 0, KV ? 160 : 256); }
        else { wf[ni][j] = ldtr(nsl + wa[ni], j * 4096, j * 4096 + (KV ? 160 : 256)); }
        SB();
      }
      if (ROLE == 1) {
#pragma unroll
        for (int e = 0; e < 4; e++) { sqacc(sq[(2 * e) & 3], bflo(xf[i][e])); sqacc(sq[(2 * e + 1) & 3], bfhi(xf[i][e])); }
        SB();
      }
    };
    BARRIER();
    rd(lds, 0); rd(lds, 1);
#pragma unroll 1
    for (int st = 0; st < 10; st++) {
      LAS char* sl = lds + (st & 3) * K1_STAGE;
      LAS char* sn = lds + ((st + 1) & 3) * K1_STAGE;
      mm(0, sl, 2);
      mm(1, sl, 3);
#pragma unroll
      for (int j = 0; j < NJ; j++) cs[j] = MFMA16(SEL, wf[0][j], cs[j]);
      if (ROLE == 1) { s1 = MFMA16(SEL, xf[0], s1); s1 = MFMA16(SEL, xf[1], s1); }
      SB();
      BARRIER();
      mm(2, sn, 0);
      mm(3, sn, 1);
      if (ROLE == 1) { s1 = MFMA16(SEL, xf[2], s1); s1 = MFMA16(SEL, xf[3], s1); }
      SB();
    }
  };
  if (kv) mainloop(std::true_type{}); else mainloop(std::false_type{});
  LAS float* csp = (LAS float*)(lds + K1_CSP);
  LAS float* csf = (LAS float*)(lds + K1_CSF);
  LAS f32x2* stt = (LAS f32x2*)(lds + K1_STP);
  if (lane < 16) {
#pragma unroll
    for (int j = 0; j < NJ; j++) { csp[tt * 160 + (J0 + j) * 32 + lane] = cs[j][0]; csp[tt * 160 + (J0 + j) * 32 + 16 + lane] = cs[j][1]; }
  }
  if (ROLE == 1) {
    const float sqs = half_sum((sq[0] + sq[1]) + (sq[2] + sq[3]));
    const float a0 = __shfl(s1[0], r & 15), a1 = __shfl(s1[1], r & 15), su = r < 16 ? a0 : a1;
    const float mu = su * (1.f / 640.f), var = fmaxf(sqs * (1.f / 640.f) - mu * mu, 0.f), rs = __builtin_amdgcn_rsqf(var + 1e-5f);
    if (h == 0) stt[row] = f32x2{mu * rs, rs};
  }
  BARRIER();
  { const int c = threadIdx.x; if (c < 160) csf[c] = (csp[c] + csp[160 + c]) + (csp[320 + c] + csp[480 + c]); }
  const f32x2 st_ = stt[row];
  BARRIER();
  const int tb = rb * 4 + tt;
#pragma unroll
  for (int j = 0; j < NJ; j++) {
    const int type = stype[j];
    if (type == T_E) continue;
    if (ROLE == 1 && type == T_V) {
      const float csum = csf[(J0 + j) * 32 + r];
      char* vb = p.ws + WS_KV + (size_t)rb * KV_CHUNK + KV_VOFF + tt * 1536 + h * 384 + r * 16;
#pragma unroll
      for (int s = 0; s < 2; s++) {
        float v[8];
#pragma unroll
        for (int i = 0; i < 8; i++) { const f32x2 t = stt[32 * tt + 16 * s + 8 * (i >> 2) + 4 * h + (i & 3)]; v[i] = t[1] * acc[j][8 * s + i] - t[0] * csum; }
        u32x4 w = {cvtpk(v[0], v[1]), cvtpk(v[2], v[3]), cvtpk(v[4], v[5]), cvtpk(v[6], v[7])};
        if (r == 20) w = u32x4{ONES, ONES, ONES, ONES};
        if (r > 20) w = u32x4{0u, 0u, 0u, 0u};
        if (r < 24) *(u32x4*)(vb + s * 768) = w;
      }
      continue;
    }
    float v[16];
#pragma unroll
    for (int g = 0; g < 4; g++) {
      const f32x4 c4 = *(LAS f32x4*)(csf + (J0 + j) * 32 + 8 * g + 4 * h);
#pragma unroll
      for (int i = 0; i < 4; i++) v[4 * g + i] = st_[1] * acc[j][4 * g + i] - st_[0] * c4[i];
    }
    if (ROLE == 1 && type == T_K) {
      char* kb = p.ws + WS_KV + (size_t)rb * KV_CHUNK + tt * 1536 + r * 16 + h * 8;
      *(u32x2*)(kb) = u32x2{cvtpk(v[0], v[1]), cvtpk(v[2], v[3])};
      *(u32x2*)(kb + 512) = u32x2{cvtpk(v[4], v[5]), cvtpk(v[6], v[7])};
      *(u32x2*)(kb + 1024) = h ? u32x2{0x00003F80u, 0u} : u32x2{cvtpk(v[8], v[9]), cvtpk(v[10], v[11])};
      continue;
    }
    unsigned off; char* base;
    if (type == T_Q) {
      base = p.ws + WS_QI; off = ((tb * 80 + sidx[j] * 4 + h) * 32 + r) * 16;
#pragma unroll
      for (int i = 0; i < 16; i++) v[i] *= QSCALE;
    } else {
      base = p.ws + (type == T_Z1 ? WS_Z1 : WS_Z2R); off = ((tb * 108 + sidx[j] * 4 + h) * 32 + r) * 16;
#pragma unroll
      for (int i = 0; i < 16; i++) v[i] = gelu_t(v[i]);
    }
#pragma unroll
    for (int g = 0; g < 4; g += 2)
      st16_wt(base, off + g * 512, pair8(u32x2{cvtpk(v[4 * g], v[4 * g + 1]), cvtpk(v[4 * g + 2], v[4 * g + 3])}, u32x2{cvtpk(v[4 * g + 4], v[4 * g + 5]), cvtpk(v[4 * g + 6], v[4 * g + 7])}), 3538944);
  }
}
__global__ void __launch_bounds__(768, 3) k1_proj(Params p) {
  extern __shared__ __attribute__((aligned(16))) char lds[];
  const int b = blockIdx.x, x = b & 7, j = b >> 3;
  const int rb = (x & 1) * 8 + (j & 7), cg = (x >> 1) * 4 + (j >> 3);
  const int wid = RFL(threadIdx.x >> 6);
  if (b == 0 && threadIdx.x < 256) ((u32x4*)(p.ws + WS_FLAG))[threadIdx.x] = u32x4{0u, 0u, 0u, 0u};
  if (wid < 4) k1_body<0>(p, lds, rb, cg, wid); else if (wid < 8) k1_body<1>(p, lds, rb, cg, wid - 4); else k1_loader(p, lds, rb, cg, wid - 8);
}

constexpr int K2_LDS = 81920;
constexpr float SM_OFF = 90.f;
#define EX(x) __builtin_amdgcn_exp2f(x)
#define AUNIT(Sc, Sn, qn0, qn1, Oc, Op, LD0, LD1, LD2, LD3) do { \
    Sc[0] = EX(Sc[0]); Sc[1] = EX(Sc[1]); SB(); \
    Sn = MFMA32(k0, qn0, zero16); SB(); LD0; SB(); \
    Sc[2] = EX(Sc[2]); Sc[3] = EX(Sc[3]); SB(); p1[2] = cvtpk(e12, e13); p1[3] = cvtpk(e14, e15); SB(); \
    Sc[4] = EX(Sc[4]); Sc[5] = EX(Sc[5]); SB(); \
    Op = MFMA32(v1, p1, Op); SB(); LD1; SB(); \
    Sc[6] = EX(Sc[6]); Sc[7] = EX(Sc[7]); SB(); p0[0] = cvtpk(Sc[0], Sc[1]); p0[1] = cvtpk(Sc[2], Sc[3]); SB(); \
    Sc[8] = EX(Sc[8]); Sc[9] = EX(Sc[9]); SB(); \
    Sn = MFMA32(k1, qn1, Sn); SB(); LD2; SB(); \
    Sc[10] = EX(Sc[10]); Sc[11] = EX(Sc[11]); SB(); p0[2] = cvtpk(Sc[4], Sc[5]); p0[3] = cvtpk(Sc[6], Sc[7]); SB(); \
    e12 = EX(Sc[12]); e13 = EX(Sc[13]); SB(); \
    Oc = MFMA32(v0, p0, Oc); SB(); LD3; SB(); \
    e14 = EX(Sc[14]); e15 = EX(Sc[15]); SB(); p1[0] = cvtpk(Sc[8], Sc[9]); p1[1] = cvtpk(Sc[10], Sc[11]); SB(); \
  } while (0)
#define ATILE(kn, v1c, v0n) do { \
    AUNIT(sA, sB, qB0, qB1, oA, oB, k0 = LDS128(kn), v1 = LDS128(v1c), k1 = LDS128((kn) + k1d), (void)0); \
    AUNIT(sB, sA, qA0, qA1, oB, oA, (void)0, (void)0, (void)0, v0 = LDS128(v0n)); \
  } while (0)
DI void load_q(const char* qb, int c0, int h, u32x4& q0, u32x4& q1) {
  auto piece = [&](int c4) { return *(const u32x2*)(qb + (c4 >> 1) * 512 + (c4 & 1) * 8); };
  u32x2 ga = piece(c0 + 2 * h), gb = piece(c0 + 2 * h + 1), gc = piece(c0 + 4);
  q0 = u32x4{ga[0], ga[1], gb[0], gb[1]};
  q1 = h ? u32x4{0u, 0u, 0u, 0u} : u32x4{gc[0], gc[1], 0u, 0u};
}
DI float max3f(float a, float b, float c) { return __builtin_fmaxf(__builtin_fmaxf(a, b), c); }
DI float max16(const f32x16& s) {
  const float a = max3f(max3f(s[0], s[1], s[2]), s[3], s[4]), b = max3f(max3f(s[5], s[6], s[7]), s[8], s[9]), c = max3f(max3f(s[10], s[11], s[12]), s[13], s[14]);
  return max3f(max3f(a, b, c), s[15], s[15]);
}
DI f32x16 attn_exact(const char* kv, u32x4 q0, u32x4 q1, int r, int h) {
  const f32x16 zero16 = {};
  q1[2] = 0u;
  f32x16 o = {}; float m = -3.0e38f;
  const int rr = r < 21 ? r : 21;
#pragma unroll 1
  for (int t = 0; t < 64; t++) {
    const char* cb = kv + (size_t)(t >> 2) * KV_CHUNK;
    const char* kb = cb + (t & 3) * 1536 + r * 16;
    const u32x4 k0 = *(const u32x4*)(kb + h * 512), k1 = *(const u32x4*)(kb + 1024);
    const char* vb = cb + KV_VOFF + (t & 3) * 1536 + h * 384 + rr * 16;
    const u32x4 v0 = *(const u32x4*)(vb), v1 = *(const u32x4*)(vb + 768);
    f32x16 s = MFMA32(k0, q0, zero16); s = MFMA32(k1, q1, s);
    float mx = s[0];
#pragma unroll
    for (int i = 1; i < 16; i++) mx = fmaxf(mx, s[i]);
    const float mn = fmaxf(m, half_max(mx)), a = EX(m - mn);
    m = mn;
#pragma unroll
    for (int i = 0; i < 16; i++) { o[i] *= a; s[i] = EX(s[i] - mn); }
    const u32x4 p0 = {cvtpk(s[0], s[1]), cvtpk(s[2], s[3]), cvtpk(s[4], s[5]), cvtpk(s[6], s[7])};
    const u32x4 p1 = {cvtpk(s[8], s[9]), cvtpk(s[10], s[11]), cvtpk(s[12], s[13]), cvtpk(s[14], s[15])};
    o = MFMA32(v0, p0, o); o = MFMA32(v1, p1, o);
  }
  return o;
}
DI void attn_role(const Params& p, char* lds_, int wg) {
  const int tid = threadIdx.x, lane = tid & 63, r = lane & 31, h = lane >> 5;
  const int wid = RFL(tid >> 6);
  const int pi = wid & 3, kh = wid >> 2;
  const int tb = wg >> 2, headA = (wg & 3) * 8 + 2 * pi;
  const unsigned l0 = (unsigned)(uintptr_t)lds_;
  LAS char* lds = (LAS char*)lds_;
  const char* kvg = p.ws + WS_KV;
  const char* kvsrc = kvg + (size_t)kh * 8 * KV_CHUNK + pi * 3072;
  const unsigned voff = lane * 16;
  const unsigned ring = l0 + kh * 3 * KV_CHUNK + pi * 3072;
#define APRIO 2
  __builtin_amdgcn_s_setprio(APRIO);
  glds3(kvsrc, voff, ring);
  u32x4 qA0, qA1, qB0, qB1;
  const char* qb = p.ws + WS_QI + ((size_t)tb * 80 * 32 + r) * 16;
  load_q(qb, headA * 5, h, qA0, qA1); load_q(qb, headA * 5 + 5, h, qB0, qB1);
  asm volatile("" : "+v"(qA0), "+v"(qA1), "+v"(qB0), "+v"(qB1));
  glds3(kvsrc + KV_CHUNK, voff, ring + KV_CHUNK);
  asm volatile("s_waitcnt vmcnt(3)\n\ts_barrier" ::: "memory");
  const f32x16 zero16 = {};
  const int k1d = 1024 - h * 512;
  const int rr = r < 21 ? r : 21;
  u32x4 k0, k1; f32x16 sA, sB;
  {
    LAS char* kb0 = lds + h * 512 + r * 16;
    k0 = LDS128(kb0); k1 = LDS128(kb0 + k1d);
    sA = MFMA32(k0, qA0, zero16); sA = MFMA32(k1, qA1, sA); sB = MFMA32(k0, qB0, zero16); sB = MFMA32(k1, qB1, sB);
    float mA = max16(sA), mB = max16(sB);
    k0 = LDS128(kb0 + 1536); k1 = LDS128(kb0 + 1536 + k1d);
    sA = MFMA32(k0, qA0, zero16); sA = MFMA32(k1, qA1, sA); sB = MFMA32(k0, qB0, zero16); sB = MFMA32(k1, qB1, sB);
    mA = half_max(fmaxf(mA, max16(sA))); mB = half_max(fmaxf(mB, max16(sB)));
    if (h == 0) { qA1[2] = cvtpk(-(mA + SM_OFF), 0.f); qB1[2] = cvtpk(-(mB + SM_OFF), 0.f); }
  }
  LAS char* kbase = lds + kh * 3 * KV_CHUNK + h * 512 + r * 16;
  LAS char* vbase = lds + kh * 3 * KV_CHUNK + KV_VOFF + h * 384 + rr * 16;
  k0 = LDS128(kbase); k1 = LDS128(kbase + k1d);
  sA = MFMA32(k0, qA0, zero16); sA = MFMA32(k1, qA1, sA);
  u32x4 v0 = LDS128(vbase), v1 = v0, p0 = {0u, 0u, 0u, 0u}, p1 = {0u, 0u, 0u, 0u};
  f32x16 oA = {}, oB = {};
  float e12 = 0.f, e13 = 0.f, e14 = 0.f, e15 = 0.f;
  asm volatile("s_waitcnt vmcnt(0) lgkmcnt(0)\n\ts_barrier" ::: "memory");
  int sc = 0;
#pragma unroll 1
  for (int c = 0; c < 8; c++) {
    const int sn = sc == 2 * KV_CHUNK ? 0 : sc + KV_CHUNK, sp = sn == 2 * KV_CHUNK ? 0 : sn + KV_CHUNK;
    if (c + 2 < 8) glds3(kvsrc + (size_t)(c + 2) * KV_CHUNK, voff, ring + sp);
    LAS char* kc = kbase + sc; LAS char* kn = kbase + sn; LAS char* vc = vbase + sc; LAS char* vn = vbase + sn;
    ATILE(kc + 1 * 1536, vc + 1 * 768, vc + 2 * 768);
    ATILE(kc + 2 * 1536, vc + 3 * 768, vc + 4 * 768);
    ATILE(kc + 3 * 1536, vc + 5 * 768, vc + 6 * 768);
    ATILE(kn, vc + 7 * 768, vn);
    asm volatile("s_waitcnt vmcnt(0) lgkmcnt(0)\n\ts_barrier" ::: "memory");
    sc = sn;
  }
  p1[2] = cvtpk(e12, e13); p1[3] = cvtpk(e14, e15);
  oB = MFMA32(v1, p1, oB);
  LAS f32x4* ex = (LAS f32x4*)(lds + (pi * 2 + kh) * 3072 + lane * 16);
  LAS const f32x4* exr = (LAS const f32x4*)(lds + (pi * 2 + (kh ^ 1)) * 3072 + lane * 16);
  f32x16 o;
  if (kh == 0) { o = oA;
#pragma unroll
    for (int g = 0; g < 3; g++) ex[g * 64] = f32x4{oB[4 * g], oB[4 * g + 1], oB[4 * g + 2], oB[4 * g + 3]};
  } else { o = oB;
#pragma unroll
    for (int g = 0; g < 3; g++) ex[g * 64] = f32x4{oA[4 * g], oA[4 * g + 1], oA[4 * g + 2], oA[4 * g + 3]};
  }
  BARRIER();
#pragma unroll
  for (int g = 0; g < 3; g++) { f32x4 t = exr[g * 64]; o[4 * g] += t[0]; o[4 * g + 1] += t[1]; o[4 * g + 2] += t[2]; o[4 * g + 3] += t[3]; }
  {
    float big = 0.f;
#pragma unroll
    for (int i = 0; i < 12; i++) big = fmaxf(big, fabsf(o[i]));
    float chk = 0.f;
#pragma unroll
    for (int i = 0; i < 12; i++) chk += o[i] * 0.f;
    const bool bad = !(big < 1.0e36f) || !(chk == 0.f);
    if (__builtin_expect(__any(bad), 0)) o = attn_exact(kvg, kh ? qB0 : qA0, kh ? qB1 : qA1, r, h);
  }
  auto lr = __builtin_amdgcn_permlane32_swap(__float_as_uint(o[8]), __float_as_uint(o[8]), false, false);
  const float il = __builtin_amdgcn_rcpf(__uint_as_float(lr[1]));
  const int tok = tb * 32 + r, head = headA + kh;
#pragma unroll
  for (int g = 0; g < 3; g++) {
    if (g < 2 || h == 0) {
      const int kk = head * 20 + (2 * g + h) * 4;
      u32x2 w = {cvtpk(o[4 * g] * il, o[4 * g + 1] * il), cvtpk(o[4 * g + 2] * il, o[4 * g + 3] * il)};
      *(u32x2*)(p.ws + WS_AO + ((size_t)(kk >> 3) * 2048 + tok) * 16 + ((kk >> 2) & 1) * 8) = w;
    }
  }
}

DI int fb_start(int fb) { return (fb >> 2) * 216 + (fb & 3) * 56; }
constexpr int SP_STAGE = 24576, SP_ZOFF = 16384;
typedef __attribute__((address_space(1))) unsigned gu32;
DI void sp_role(const Params& p, char* lds_, int b) {
  const int wid = RFL(threadIdx.x >> 6);
  __builtin_amdgcn_s_setprio(3);
  const int tid = threadIdx.x, lane = tid & 63, r = lane & 31, h = lane >> 5;
  const unsigned l0 = (unsigned)(uintptr_t)lds_;
  LAS char* lds = (LAS char*)lds_;
  const int x = b & 7, jj = b >> 3;
  const int tbk = (x >> 1) * 4 + (jj & 3), fb = (x & 1) * 8 + (jj >> 2);
  gu32* cnt = (gu32*)(p.ws + WS_FLAG);
  if (wid >= 4) {
    const int lw = wid - 4;
    const char* wb = p.wsp + (size_t)tbk * 128 * 4096;
    unsigned wv4[4];
#pragma unroll
    for (int i = 0; i < 4; i++) { const unsigned row = 8 * (4 * lw + i) + (lane >> 3); wv4[i] = row * 4096 + (((lane & 7) ^ ((row >> 1) & 7)) << 4); }
    const char* zb = p.ws + WS_Z2N + (size_t)fb * 32 * 8192 + lw * 2048;
    const unsigned zv = lane * 16;
    auto issue_w = [&](int st) {
#pragma unroll
      for (int i = 0; i < 4; i++) gldsn(wb + st * 128, wv4[i], l0 + (st % 3) * SP_STAGE + (4 * lw + i) * 1024);
    };
    auto issue_z = [&](int st) {
      asm volatile("s_mov_b32 m0, %2\n\ts_nop 0\n\tglobal_load_lds_dwordx4 %0, %1\n\tglobal_load_lds_dwordx4 %0, %1 offset:1024"
                   :: "v"(zv), "s"(zb + (size_t)st * 8192), "s"(l0 + (st % 3) * SP_STAGE + SP_ZOFF + lw * 2048) : "memory");
    };
    issue_w(0); issue_w(1);
    BARRIER(); BARRIER(); BARRIER();
    {
      const int u = b * 3 + lw - 1;
      if (lw > 0 && u < 752) {
        const int ks = u >> 3, nb = u & 7;
        const char* src = (ks < 40 ? p.wo + (size_t)ks * 16 * 1280 : p.wout + (size_t)(ks - 40) * 16 * 1280) + nb * 160;
        LAS char* sc = lds + 3 * SP_STAGE + (lw - 1) * 2560;
#pragma unroll
        for (int i = 0; i < 3; i++) {
          const int c = lane + 64 * i, rw = c / 10, cc = c - rw * 10;
          if (c < 160) *(LAS u32x4*)(sc + c * 16) = *(const u32x4*)(src + rw * 1280 + cc * 16);
        }
        asm volatile("s_waitcnt vmcnt(0) lgkmcnt(0)" ::: "memory");
        const int blk = (lane >> 4) & 1, q = (lane & 15) >> 2, pp = lane & 3;
        LAS char* ta = sc + (8 * h + q) * 160 + 32 * blk + 8 * pp;
        char* dst = p.ws + WS_W3 + ((size_t)nb * 94 + ks) * 2560;
        const u32x4 f0 = ldtr(ta, 0, 640), f1 = ldtr(ta, 64, 64 + 640), f2 = ldtr(ta, 128, 128 + 640);
        *(u32x4*)(dst + lane * 16) = f0;
        *(u32x4*)(dst + 1024 + lane * 16) = f1;
        if (r < 16) *(u32x4*)(dst + 2048 + (h * 16 + r) * 16) = f2;
        asm volatile("s_waitcnt vmcnt(0) lgkmcnt(0)" ::: "memory");
      }
    }
    if (lw > 0) {
      const char* src = p.wsp + (unsigned)RFL(b * 32768 + (lw - 1) * 12288);
      const int n2 = lw == 3 ? 4 : 6;
      for (int i = 0; i < n2; i++)
        asm volatile("s_mov_b32 m0, %2\n\ts_nop 0\n\tglobal_load_lds_dwordx4 %0, %1\n\tglobal_load_lds_dwordx4 %0, %1 offset:1024"
                     :: "v"(zv), "s"(src + i * 2048), "s"(l0 + 3 * SP_STAGE - 2048) : "memory");
    }
    if (lw == 0) {
      {
      __builtin_amdgcn_s_sleep(24);
      unsigned spins = 0;
      for (;;) {
        const unsigned c = __hip_atomic_load(cnt + (lane & 31) * 16, __ATOMIC_RELAXED, __HIP_MEMORY_SCOPE_AGENT);
        if (__all(c >= 8u)) break;
        if (++spins > (1u << 21)) break;
        __builtin_amdgcn_s_sleep(6);
      }
      }
      __builtin_amdgcn_fence(__ATOMIC_ACQUIRE, "agent");
      const char* z00 = p.ws + WS_Z2N + (size_t)fb * 32 * 8192;
      constexpr int NZ0 = 4;
#pragma unroll
      for (int i = 0; i < NZ0; i++)
        asm volatile("s_mov_b32 m0, %2\n\ts_nop 0\n\tglobal_load_lds_dwordx4 %0, %1\n\tglobal_load_lds_dwordx4 %0, %1 offset:1024\n\tglobal_load_lds_dwordx4 %0, %1 offset:2048\n\tglobal_load_lds_dwordx4 %0, %1 offset:3072"
                     :: "v"(zv), "s"(z00 + i * 4096), "s"(l0 + (i >> 1) * SP_STAGE + SP_ZOFF + (i & 1) * 4096) : "memory");
      WAITV(8);
    }
#pragma unroll 1
    for (int st = 0; st < 32; st++) {
      if (st == 0) { if (lw) WAITV(0); } else if (st < 31) WAITV(6); else WAITV(0);
      BARRIER();
      if (st + 2 < 32) { issue_w(st + 2); issue_z(st + 2); }
    }
    return;
  }
  {
    LAS float* red = (LAS float*)(lds + K2_LDS - 256);
    const bool act = tid < 216;
    const int g8 = act ? tid >> 1 : 107, half = tid & 1;
    const char* src = p.ws + WS_Z2R + ((size_t)((b >> 2) * 108 + g8) * 32 + (b & 3) * 8 + 4 * half) * 16;
    u32x4 raw[4];
#pragma unroll
    for (int i = 0; i < 4; i++) raw[i] = *(const u32x4*)(src + i * 16);
    const unsigned ONES = act ? 0x3F803F80u : 0u;
    LAS float* part = (LAS float*)(lds + 2 * SP_STAGE);
#pragma unroll
    for (int t = 0; t < 4; t++) {
      float su = 0.f, sq = 0.f;
#pragma unroll
      for (int e = 0; e < 4; e++) { const unsigned a = act ? raw[t][e] : 0u; su = dot2(a, ONES, su); sq = dot2(a, a, sq); }
      part[(2 * (4 * half + t)) * 128 + (tid >> 1)] = su;
      part[(2 * (4 * half + t) + 1) * 128 + (tid >> 1)] = sq;
    }
    BARRIER();
    {
      float s4[4];
#pragma unroll
      for (int k = 0; k < 4; k++) { const f32x2 a = *(LAS f32x2*)(part + (4 * wid + k) * 128 + lane * 2); s4[k] = a[0] + a[1]; }
#pragma unroll
      for (int k = 0; k < 4; k++) s4[k] = wave_sum(s4[k]);
      if (lane < 2) {
        const float su = lane ? s4[2] : s4[0], sq = lane ? s4[3] : s4[1];
        const float mu = su * (1.f / 864.f), var = fmaxf(sq * (1.f / 864.f) - mu * mu, 0.f), rs = __builtin_amdgcn_rsqf(var + 1e-5f);
        ((LAS f32x2*)red)[2 * wid + lane] = f32x2{rs, -mu * rs};
      }
    }
    BARRIER();
    LAS char* tr = lds + 2 * SP_STAGE + 8192;
    {
      float v[4][8];
#pragma unroll
      for (int t = 0; t < 4; t++) {
        const f32x2 c = ((LAS f32x2*)red)[4 * half + t];
#pragma unroll
        for (int e = 0; e < 4; e++) { v[t][2 * e] = __builtin_fmaf(bflo(raw[t][e]), c[0], c[1]); v[t][2 * e + 1] = __builtin_fmaf(bfhi(raw[t][e]), c[0], c[1]); }
      }
      if (act) {
#pragma unroll
        for (int i = 0; i < 8; i++) *(LAS u32x2*)(tr + (8 * g8 + i) * 16 + half * 8) = u32x2{cvtpk(v[0][i], v[1][i]), cvtpk(v[2][i], v[3][i])};
      }
    }
    asm volatile("s_waitcnt lgkmcnt(0)" ::: "memory");
    {
      const __amdgpu_buffer_rsrc_t rsrc = __builtin_amdgcn_make_buffer_rsrc((void*)(p.ws + WS_Z2N), 0, 4194304, 0x00020000);
      const unsigned sbase = (((b >> 3) * 4 + ((b >> 1) & 3)) * 2 + (b & 1)) * 1024;
#pragma unroll
      for (int i = 0; i < 4; i++) {
        const int f = 256 * wid + 64 * i + lane;
        if (f < 864) {
          const u32x4 w = *(LAS u32x4*)(tr + f * 16);
          const int q4 = (f >= 216) + (f >= 432) + (f >= 648), rem = f - q4 * 216, sub = (rem >= 56) + (rem >= 112) + (rem >= 168);
          const unsigned off = (unsigned)(q4 * 4 + sub) * (32u * 8192u) + sbase + (rem - sub * 56) * 16;
          __builtin_amdgcn_raw_buffer_store_b128(w, rsrc, off, 0, 16);
        }
      }
    }
    asm volatile("s_waitcnt vmcnt(0)" ::: "memory");
    BARRIER();
    if (tid == 0) __hip_atomic_fetch_add(cnt + (b >> 3) * 16, 1u, __ATOMIC_RELAXED, __HIP_MEMORY_SCOPE_AGENT);
  }
  const unsigned row = 32 * wid + r;
  unsigned wa[4];
#pragma unroll
  for (int ks = 0; ks < 4; ks++) wa[ks] = row * 128 + ((32 * ks + 16 * h) ^ (16 * ((row >> 1) & 7)));
  const unsigned za = SP_ZOFF + h * 1024 + r * 16;
  f32x16 acc0 = {}, acc1 = {};
#pragma unroll 1
  for (int st = 0; st < 32; st++) {
    BARRIER();
    LAS char* sl = lds + (st % 3) * SP_STAGE;
    u32x4 wf[4], z0[4], z1[4];
#pragma unroll
    for (int ks = 0; ks < 4; ks++) { wf[ks] = LDS128(sl + wa[ks]); z0[ks] = LDS128(sl + za + ks * 2048); z1[ks] = LDS128(sl + za + ks * 2048 + 512); }
#pragma unroll
    for (int ks = 0; ks < 4; ks++) { acc0 = MFMA32(z0[ks], wf[ks], acc0); acc1 = MFMA32(z1[ks], wf[ks], acc1); }
  }
  const int tok = tbk * 128 + row, f0 = fb_start(fb), width = (fb & 3) == 3 ? 48 : 56;
  const float bias = bflo(*(const unsigned short*)(p.bsp + tok * 2));
  const char* z1b = p.ws + WS_Z1 + ((size_t)((tok >> 5) * 108 + (f0 >> 3)) * 32 + r) * 16 + h * 8;
#pragma unroll
  for (int j = 0; j < 2; j++) {
    const f32x16& a = j ? acc1 : acc0;
    u32x2 w[4];
#pragma unroll
    for (int g = 0; g < 4; g++) {
      const int fl = 32 * j + 8 * g;
      u32x2 z = {0u, 0u};
      if (fl < width) z = *(const u32x2*)(z1b + (4 * j + g) * 512);
      w[g] = u32x2{cvtpk(bflo(z[0]) * (a[4 * g] + bias), bfhi(z[0]) * (a[4 * g + 1] + bias)), cvtpk(bflo(z[1]) * (a[4 * g + 2] + bias), bfhi(z[1]) * (a[4 * g + 3] + bias))};
    }
#pragma unroll
    for (int g = 0; g < 4; g += 2) {
      const u32x4 w8 = pair8(w[g], w[g + 1]);
      const int fl = 32 * j + 8 * (g + h);
      if (fl < width) st16_wt(p.ws + WS_AO, ((80 + ((f0 + fl) >> 3)) * 2048 + tok) * 16, w8, 6160384);
    }
  }
}
__global__ void __launch_bounds__(512, 4) k2_mix(Params p) {
  extern __shared__ __attribute__((aligned(16))) char lds[];
  if (blockIdx.x >= 256) sp_role(p, lds, blockIdx.x - 256); else attn_role(p, lds, blockIdx.x);
}

constexpr int K3_ACT = 16384, K3_W = 20480, K3_STAGE = K3_ACT + K3_W, K3_NS = 4;
constexpr int K3_LDS = K3_NS * K3_STAGE + 256;
__global__ void __launch_bounds__(640, 2) k3_out(Params p) {
  extern __shared__ __attribute__((aligned(16))) char lds_[];
  const int b = blockIdx.x, x = b & 7, jj = b >> 3;
  const int tb = x * 4 + (jj & 3), nb = jj >> 2;
  const int tid = threadIdx.x, lane = tid & 63, r = lane & 31, h = lane >> 5;
  const int wid = RFL(tid >> 6);
  const unsigned l0 = (unsigned)(uintptr_t)lds_;
  LAS char* lds = (LAS char*)lds_;
  if (wid >= 6) {
    const int lw = wid - 6;
    const char* ab = p.ws + WS_AO + (size_t)tb * 64 * 16;
    const char* wb = p.ws + WS_W3 + (size_t)nb * 94 * 2560;
    const unsigned lv = lane * 16;
    auto issue = [&](int st) {
      const unsigned slot = l0 + (st % K3_NS) * K3_STAGE;
#pragma unroll
      for (int i = 0; i < 4; i++) {
        const int pc = lw * 4 + i;
        gldsn(ab + ((size_t)st * 16 + (st == 11 && pc >= 12 ? 0 : pc)) * 32768, lv, slot + pc * 1024);
      }
      const char* ws_ = wb + (size_t)st * 20480 + (st == 11 && lw == 3 ? 0 : lw * 5120);
      asm volatile("s_mov_b32 m0, %2\n\ts_nop 0\n\tglobal_load_lds_dwordx4 %0, %1\n\tglobal_load_lds_dwordx4 %0, %1 offset:1024\n\tglobal_load_lds_dwordx4 %0, %1 offset:2048\n\tglobal_load_lds_dwordx4 %0, %1 offset:3072"
                   :: "v"(lv), "s"(ws_), "s"(slot + K3_ACT + lw * 5120) : "memory");
      gldsn(ws_ + 4096, lv, slot + K3_ACT + lw * 5120 + 4096);
    };
    issue(0); issue(1); issue(2);
#pragma unroll 1
    for (int st = 0; st < 12; st++) {
      if (st < 10) WAITV(18); else if (st == 10) WAITV(9); else WAITV(0);
      BARRIER();
      if (st + 3 < 12) issue(st + 3);
    }
    return;
  }
  const int nt = wid % 3, tt = wid / 3;
  u32x4 xr[2];
#pragma unroll
  for (int k = 0; k < 2; k++) { const int i = tid + 384 * k < 640 ? tid + 384 * k : 0, row = i / 10, c = i - row * 10;
    xr[k] = *(const u32x4*)(p.x + ((size_t)(tb * 64 + row) * 640 + nb * 80 + c * 8) * 2); }
  const unsigned wa = K3_ACT + nt * 1024 + (nt < 2 ? lane : h * 16 + (r & 15)) * 16;
  const unsigned aa = h * 1024 + (32 * tt + r) * 16;
  f32x16 acc = {};
#pragma unroll 1
  for (int st = 0; st < 12; st++) {
    BARRIER();

    LAS char* sl = lds + (st % K3_NS) * K3_STAGE;
    u32x4 af[8], wf[8];
#pragma unroll
    for (int ks = 0; ks < 6; ks++) { af[ks] = LDS128(sl + aa + ks * 2048); wf[ks] = LDS128(sl + wa + ks * 2560); }
    SB();
#pragma unroll
    for (int ks = 0; ks < 2; ks++) acc = MFMA32(wf[ks], af[ks], acc);
    SB();
    if (st < 11) {
#pragma unroll
      for (int ks = 6; ks < 8; ks++) { af[ks] = LDS128(sl + aa + ks * 2048); wf[ks] = LDS128(sl + wa + ks * 2560); }
    }
    SB();
#pragma unroll
    for (int ks = 2; ks < 6; ks++) acc = MFMA32(wf[ks], af[ks], acc);
    if (st < 11) {
#pragma unroll
      for (int ks = 6; ks < 8; ks++) acc = MFMA32(wf[ks], af[ks], acc);
    }
  }
  BARRIER();
  LAS float* ot = (LAS float*)lds;
  constexpr int OP = 84;
#pragma unroll
  for (int g = 0; g < 4; g++) {
    const int n = nt * 32 + 8 * g + 4 * h;
    if (n < 80) *(LAS f32x4*)(ot + (32 * tt + r) * OP + n) = f32x4{acc[4 * g], acc[4 * g + 1], acc[4 * g + 2], acc[4 * g + 3]};
  }
  BARRIER();
#pragma unroll
  for (int k = 0; k < 2; k++) {
    const int i = tid + 384 * k;
    if (i < 640) {
      const int row = i / 10, c = i - row * 10;
      const size_t go = ((size_t)(tb * 64 + row) * 640 + nb * 80 + c * 8) * 2;
      const f32x4 a = *(LAS f32x4*)(ot + row * OP + c * 8), a2 = *(LAS f32x4*)(ot + row * OP + c * 8 + 4);
      const u32x4 w = {cvtpk(a[0] + bflo(xr[k][0]), a[1] + bfhi(xr[k][0])), cvtpk(a[2] + bflo(xr[k][1]), a[3] + bfhi(xr[k][1])),
                       cvtpk(a2[0] + bflo(xr[k][2]), a2[1] + bfhi(xr[k][2])), cvtpk(a2[2] + bflo(xr[k][3]), a2[3] + bfhi(xr[k][3]))};
      *(u32x4*)(p.out + go) = w;
    }
  }
}

extern "C" void kernel_launch(void* const* d_in, const int* in_sizes, int n_in, void* d_out, int out_size, void* d_ws, size_t ws_size, hipStream_t stream) {
  Params p;
  p.x = (const char*)d_in[0]; p.wq = (const char*)d_in[1]; p.wk = (const char*)d_in[2]; p.wv = (const char*)d_in[3]; p.wo = (const char*)d_in[4];
  p.win = (const char*)d_in[6]; p.wsp = (const char*)d_in[7]; p.bsp = (const char*)d_in[8]; p.wout = (const char*)d_in[9];
  p.ws = (char*)d_ws; p.out = (char*)d_out;
  (void)hipFuncSetAttribute((const void*)k1_proj, hipFuncAttributeMaxDynamicSharedMemorySize, K1_LDS);
  (void)hipFuncSetAttribute((const void*)k2_mix, hipFuncAttributeMaxDynamicSharedMemorySize, K2_LDS);
  (void)hipFuncSetAttribute((const void*)k3_out, hipFuncAttributeMaxDynamicSharedMemorySize, K3_LDS);
  k1_proj<<<256, 768, K1_LDS, stream>>>(p);
  k2_mix<<<512, 512, K2_LDS, stream>>>(p);
  k3_out<<<256, 640, K3_LDS, stream>>>(p);
}
```

```cpp
#include <hip/hip_runtime.h>
#include <stdint.h>
#include <type_traits>
typedef __bf16 bf16x8 __attribute__((ext_vector_type(8)));
typedef __bf16 bf16x2 __attribute__((ext_vector_type(2)));
typedef short s16x4 __attribute__((ext_vector_type(4)));
typedef float f32x16 __attribute__((ext_vector_type(16)));
typedef float f32x4 __attribute__((ext_vector_type(4)));
typedef float f32x2 __attribute__((ext_vector_type(2)));
typedef unsigned u32x4 __attribute__((ext_vector_type(4)));
typedef unsigned u32x2 __attribute__((ext_vector_type(2)));
#define DI __device__ __forceinline__
#define LAS __attribute__((address_space(3)))
#define SB() __builtin_amdgcn_sched_barrier(0)
#define RFL(x) __builtin_amdgcn_readfirstlane(x)
#define MFMA32(a, b, c) __builtin_amdgcn_mfma_f32_32x32x16_bf16(__builtin_bit_cast(bf16x8, a), __builtin_bit_cast(bf16x8, b), c, 0, 0, 0)
#define MFMA16(a, b, c) __builtin_amdgcn_mfma_f32_16x16x32_bf16(__builtin_bit_cast(bf16x8, a), __builtin_bit_cast(bf16x8, b), c, 0, 0, 0)
DI f32x4 mfma4sq(unsigned x0, unsigned x1, f32x4 c) { u32x2 v = {x0, x1}; s16x4 a = __builtin_bit_cast(s16x4, v); return __builtin_amdgcn_mfma_f32_4x4x4bf16_1k(a, a, c, 0, 0, 0); }
#define MFMA4(x0, x1, c) mfma4sq(x0, x1, c)
#define LDS128(p) (*(const LAS u32x4*)(p))
DI unsigned cvtpk(float lo, float hi) { f32x2 v = {lo, hi}; bf16x2 b = __builtin_convertvector(v, bf16x2); return __builtin_bit_cast(unsigned, b); }
DI float bflo(unsigned u) { return __uint_as_float(u << 16); }
DI float bfhi(unsigned u) { return __uint_as_float(u & 0xffff0000u); }
DI void sqacc(float& a, float x) { asm("v_fmac_f32_e32 %0, %1, %1" : "+v"(a) : "v"(x)); }
DI void addacc(float& a, float x) { asm("v_add_f32_e32 %0, %0, %1" : "+v"(a) : "v"(x)); }
DI float dot2(unsigned a, unsigned b, float c) { return __builtin_amdgcn_fdot2_f32_bf16(__builtin_bit_cast(bf16x2, a), __builtin_bit_cast(bf16x2, b), c, false); }
DI float half_sum(float m) { auto r = __builtin_amdgcn_permlane32_swap(__float_as_uint(m), __float_as_uint(m), false, false); return __uint_as_float(r[0]) + __uint_as_float(r[1]); }
DI float half_max(float m) { auto r = __builtin_amdgcn_permlane32_swap(__float_as_uint(m), __float_as_uint(m), false, false); return fmaxf(__uint_as_float(r[0]), __uint_as_float(r[1])); }
template <int CTRL, int RMASK> DI float dpp_add(float v) { return v + __uint_as_float(__builtin_amdgcn_update_dpp(0u, __float_as_uint(v), CTRL, RMASK, 0xf, true)); }
DI float wave_sum(float v) {
  v = dpp_add<0x111, 0xf>(v); v = dpp_add<0x112, 0xf>(v); v = dpp_add<0x114, 0xf>(v); v = dpp_add<0x118, 0xf>(v);
  v = dpp_add<0x142, 0xa>(v); v = dpp_add<0x143, 0xc>(v);
  return __uint_as_float(__builtin_amdgcn_readlane(__float_as_uint(v), 63));
}
DI void glds1(const void* sbase, unsigned voff, unsigned lds_base) {
  unsigned sv;
  asm volatile("s_mov_b32 %0, m0\n\ts_mov_b32 m0, %3\n\ts_nop 0\n\tglobal_load_lds_dwordx4 %1, %2\n\ts_mov_b32 m0, %0"
               : "=&s"(sv) : "v"(voff), "s"(sbase), "s"(lds_base) : "memory"); }
DI void glds3(const void* sbase, unsigned voff, unsigned lds_base) {
  unsigned sv;
  asm volatile("s_mov_b32 %0, m0\n\ts_mov_b32 m0, %3\n\ts_nop 0\n\tglobal_load_lds_dwordx4 %1, %2\n\tglobal_load_lds_dwordx4 %1, %2 offset:1024\n\tglobal_load_lds_dwordx4 %1, %2 offset:2048\n\ts_mov_b32 m0, %0"
               : "=&s"(sv) : "v"(voff), "s"(sbase), "s"(lds_base) : "memory"); }
DI void gldsn(const void* sbase, unsigned voff, unsigned lds_base) {
  asm volatile("s_mov_b32 m0, %2\n\ts_nop 0\n\tglobal_load_lds_dwordx4 %0, %1" :: "v"(voff), "s"(sbase), "s"(lds_base) : "memory"); }
DI u32x4 ldtr(const LAS char* p, int off0, int off1) {
  typedef LAS s16x4* lp;
  s16x4 a = __builtin_amdgcn_ds_read_tr16_b64_v4i16((lp)(p + off0)), b = __builtin_amdgcn_ds_read_tr16_b64_v4i16((lp)(p + off1));
  u32x2 x = __builtin_bit_cast(u32x2, a), y = __builtin_bit_cast(u32x2, b);
  return u32x4{x[0], x[1], y[0], y[1]};
}
DI void st16_wt(char* base, unsigned off, u32x4 w, unsigned bytes) {
  const __amdgpu_buffer_rsrc_t rs = __builtin_amdgcn_make_buffer_rsrc((void*)base, 0, bytes, 0x00020000);
  __builtin_amdgcn_raw_buffer_store_b128(w, rs, off, 0, 16);
}
DI u32x4 pair8(u32x2 a, u32x2 b) {
  auto s0 = __builtin_amdgcn_permlane32_swap(a[0], b[0], false, false);
  auto s1 = __builtin_amdgcn_permlane32_swap(a[1], b[1], false, false);
  return u32x4{s0[0], s1[0], s0[1], s1[1]};
}
DI void touch_lines(const void* base, int lines, unsigned& junk, int lane) {
  for (int l0 = 0; l0 < lines; l0 += 64) { const int l = l0 + lane < lines ? l0 + lane : lines - 1;
    asm volatile("global_load_dword %0, %1, %2" : "+v"(junk) : "v"((unsigned)l * 128u), "s"(base) : "memory"); }
}
DI const char* code_ptr() { unsigned long long pc; asm volatile("s_getpc_b64 %0" : "=s"(pc)); return (const char*)(pc & ~127ull); }
#define WAITV(n) asm volatile("s_waitcnt vmcnt(" #n ")" ::: "memory")
#define BARRIER() asm volatile("s_waitcnt lgkmcnt(0)\n\ts_barrier" ::: "memory")

constexpr int SEQ = 2048, DM = 640, NFH = 864;
constexpr size_t WS_FLAG = 0;
constexpr size_t WS_QI  = 4096;
constexpr size_t WS_KV  = WS_QI + 2621440;
constexpr size_t WS_Z1  = WS_KV + 196608;
constexpr size_t WS_Z2R = WS_Z1 + 3538944;
constexpr size_t WS_Z2N = WS_Z2R + 3538944;
constexpr size_t WS_AO  = WS_Z2N + 4194304;
constexpr size_t WS_W3  = WS_AO + 6160384;
constexpr size_t WS_END = WS_W3 + 8 * 94 * 2560;
constexpr int KV_CHUNK = 12288, KV_VOFF = 6144;
struct Params {
  const char *x, *wq, *wk, *wv, *wo, *win, *wsp, *bsp, *wout;
  char* ws; char* out;
};

constexpr int K1_STAGE = 36864, K1_NS = 4, K1_XT = 16384;
constexpr int K1_CSP = K1_NS * K1_STAGE;
constexpr int K1_CSF = K1_CSP + 2560;
constexpr int K1_STP = K1_CSF + 640;
constexpr int K1_JUNK = K1_STP + 1024;
constexpr int K1_LDS = K1_JUNK + 4096;
#define CODE_PF_LINES 200
constexpr float QSCALE = 0.22360679774997896f * 1.4426950408889634f;
enum { T_Q = 0, T_K = 1, T_V = 2, T_Z1 = 3, T_Z2 = 4, T_E = 5 };
DI void k1_slot(int cg, int j, int& type, int& idx) {
  if (j < 3 || (j == 3 && cg < 6)) { const int z = cg < 6 ? cg * 4 + j : 24 + (cg - 6) * 3 + j; type = z < 27 ? T_Z1 : T_Z2; idx = z < 27 ? z : z - 27; }
  else if (cg == 15) { type = j == 3 ? T_K : T_V; idx = 0; }
  else if (cg < 4) { type = T_E; idx = 0; }
  else if (cg < 6) { type = T_Q; idx = cg - 4; }
  else { type = T_Q; idx = 2 + (cg - 6) * 2 + (j - 3); }
}
DI f32x2 gelu2(f32x2 x) {
  const f32x2 c1 = {-0.10294324f, -0.10294324f}, c0 = {-2.3022082f, -2.3022082f}, one = {1.f, 1.f};
  const f32x2 a = x * __builtin_elementwise_fma(x * x, c1, c0);
  const f32x2 d = f32x2{__builtin_amdgcn_exp2f(a[0]), __builtin_amdgcn_exp2f(a[1])} + one;
  return x * f32x2{__builtin_amdgcn_rcpf(d[0]), __builtin_amdgcn_rcpf(d[1])};
}
DI void k1_loader(const Params& p, char* lds_, int rb, int cg, int lw) {
  const int lane = threadIdx.x & 63;
  __builtin_amdgcn_s_setprio(3);
  const unsigned l0 = (unsigned)(uintptr_t)lds_;
  const char* base[5]; unsigned stride[5], voff[5];
  const uintptr_t pwq = (uintptr_t)p.wq, pwin = (uintptr_t)p.win, pwk = (uintptr_t)p.wk, pwv = (uintptr_t)p.wv;
#pragma unroll
  for (int j = 0; j < 5; j++) {
    int type, idx; k1_slot(cg, j, type, idx);
    const unsigned rowp = 16 * lw + (lane >> 2), ch = (lane & 3) * 16;
    const bool kvs = j >= 3 && cg == 15, isq = type == T_Q || type == T_E;
    const uintptr_t a = kvs ? (j == 3 ? pwk : pwv) : isq ? pwq + idx * 64 : pwin + (type == T_Z2 ? 1728 : 0) + idx * 64;
    base[j] = (const char*)a;
    stride[j] = kvs ? 2560u : isq ? 64u * 1280u : 64u * 3456u;
    const unsigned o = (lw < 2 ? lw : 2) * 1024 + lane * 16;
    voff[j] = kvs ? (o < 2544u ? o : 2544u) : rowp * (isq ? 1280u : 3456u) + ch;
  }
  const char* xb = p.x + (size_t)rb * 128 * 1280;
  unsigned xv[4];
#pragma unroll
  for (int i = 0; i < 4; i++) { const unsigned row = 8 * (4 * lw + i) + (lane >> 3); xv[i] = row * 1280 + (((lane & 7) ^ ((row >> 1) & 7)) << 4); }
  auto issue = [&](int st) {
    const unsigned slot = l0 + (st & 3) * K1_STAGE;
#pragma unroll
    for (int i = 0; i < 4; i++) gldsn(xb + st * 128, xv[i], slot + (4 * lw + i) * 1024);
#pragma unroll
    for (int j = 0; j < 5; j++) gldsn(base[j] + (size_t)st * stride[j], voff[j], slot + K1_XT + j * 4096 + lw * 1024);
  };
  issue(0); issue(1); issue(2);
  WAITV(18);
  BARRIER();
#pragma unroll 1
  for (int st = 0; st < 10; st++) {
    if (st < 8) WAITV(9); else WAITV(0);
    BARRIER();
    if (st + 3 < 10) issue(st + 3);
  }
}
template <int ROLE>
DI void k1_body(const Params& p, char* lds_, int rb, int cg, int tt, const char* codep) {
  constexpr int NJ = ROLE == 0 ? 3 : 2, J0 = ROLE == 0 ? 0 : 3;
  const int lane = threadIdx.x & 63, r = lane & 31, h = lane >> 5;
  LAS char* lds = (LAS char*)lds_;
  const bool kv = ROLE == 1 && cg == 15;
  int stype[NJ], sidx[NJ];
#pragma unroll
  for (int j = 0; j < NJ; j++) k1_slot(cg, J0 + j, stype[j], sidx[j]);
#define K1_BPRIO 1
  if (ROLE == 1) __builtin_amdgcn_s_setprio(K1_BPRIO);
  unsigned junk = 0;
  if (ROLE == 0 && tt == 0) touch_lines(codep, CODE_PF_LINES, junk, lane);
  const int row = 32 * tt + r;
  unsigned xa[4], wa[4];
  const unsigned pitch = kv ? 40 : 64;
  const int blk = (lane >> 4) & 1, q = (lane & 15) >> 2, pp = lane & 3;
#pragma unroll
  for (int i = 0; i < 4; i++) {
    const unsigned ks = (i + tt) & 3;
    xa[i] = row * 128 + ((32 * ks + 16 * h) ^ (16 * ((row >> 1) & 7)));
    wa[i] = K1_XT + J0 * 4096 + (16 * ks + 8 * h + q) * pitch + 32 * blk + 8 * pp;
  }
  f32x16 acc[NJ];
#pragma unroll
  for (int j = 0; j < NJ; j++) acc[j] = f32x16{};
  f32x4 cs[NJ];
  float sq[4] = {0.f, 0.f, 0.f, 0.f}, sm[4] = {0.f, 0.f, 0.f, 0.f};
#pragma unroll
  for (int j = 0; j < NJ; j++) cs[j] = f32x4{};
  const unsigned ONES = 0x3F803F80u;
  const unsigned selw = ((lane & 15) == ((lane >> 4) & 1)) ? ONES : 0u;
  const u32x4 SEL = {selw, selw, selw, selw};
  auto mainloop = [&](auto KVC) {
    constexpr bool KV = decltype(KVC)::value;
    u32x4 xf[4] = {}, wf[4][NJ] = {};
    auto rd = [&](LAS char* sl, int i) {
      xf[i] = LDS128(sl + xa[i]);
#pragma unroll
      for (int j = 0; j < NJ; j++) wf[i][j] = ldtr(sl + wa[i], j * 4096, j * 4096 + (KV ? 160 : 256));
    };
    auto mm = [&](int i, LAS char* nsl, int ni) {
#pragma unroll
      for (int j = 0; j < NJ; j++) {
        if (KV && j == 1) acc[j] = MFMA32(xf[i], wf[i][j], acc[j]);
        else acc[j] = MFMA32(wf[i][j], xf[i], acc[j]);
        SB();
        if (j == 0) { xf[ni] = LDS128(nsl + xa[ni]); wf[ni][0] = ldtr(nsl + wa[ni], 0, KV ? 160 : 256); }
        else { wf[ni][j] = ldtr(nsl + wa[ni], j * 4096, j * 4096 + (KV ? 160 : 256)); }
        SB();
      }
      if (ROLE == 1) {
#pragma unroll
        for (int e = 0; e < 4; e++) { const float lo = bflo(xf[i][e]), hi = bfhi(xf[i][e]); sqacc(sq[(2 * e) & 3], lo); sqacc(sq[(2 * e + 1) & 3], hi); addacc(sm[e & 1], lo); addacc(sm[2 + (e & 1)], hi); }
        SB();
      }
    };
    BARRIER();
    rd(lds, 0); rd(lds, 1);
#pragma unroll 1
    for (int st = 0; st < 10; st++) {
      LAS char* sl = lds + (st & 3) * K1_STAGE;
      LAS char* sn = lds + ((st + 1) & 3) * K1_STAGE;
      mm(0, sl, 2);
      mm(1, sl, 3);
#pragma unroll
      for (int j = 0; j < NJ; j++) cs[j] = MFMA16(SEL, wf[0][j], cs[j]);
      SB();
      BARRIER();
      mm(2, sn, 0);
      mm(3, sn, 1);
      SB();
    }
  };
  if (kv) mainloop(std::true_type{}); else mainloop(std::false_type{});
  LAS float* csp = (LAS float*)(lds + K1_CSP);
  LAS float* csf = (LAS float*)(lds + K1_CSF);
  LAS f32x2* stt = (LAS f32x2*)(lds + K1_STP);
  if (lane < 16) {
#pragma unroll
    for (int j = 0; j < NJ; j++) { csp[tt * 160 + (J0 + j) * 32 + lane] = cs[j][0]; csp[tt * 160 + (J0 + j) * 32 + 16 + lane] = cs[j][1]; }
  }
  if (ROLE == 1) {
    const float sqs = half_sum((sq[0] + sq[1]) + (sq[2] + sq[3]));
    const float su = half_sum((sm[0] + sm[1]) + (sm[2] + sm[3]));
    const float mu = su * (1.f / 640.f), var = fmaxf(sqs * (1.f / 640.f) - mu * mu, 0.f), rs = __builtin_amdgcn_rsqf(var + 1e-5f);
    if (h == 0) stt[row] = f32x2{mu * rs, rs};
  }
  BARRIER();
  { const int c = threadIdx.x; if (c < 160) csf[c] = (csp[c] + csp[160 + c]) + (csp[320 + c] + csp[480 + c]); }
  const f32x2 st_ = stt[row];
  BARRIER();
  const int tb = rb * 4 + tt;
#pragma unroll
  for (int j = 0; j < NJ; j++) {
    const int type = stype[j];
    if (type == T_E) continue;
    if (ROLE == 1 && type == T_V) {
      const float csum = csf[(J0 + j) * 32 + r];
      char* vb = p.ws + WS_KV + (size_t)rb * KV_CHUNK + KV_VOFF + tt * 1536 + h * 384 + r * 16;
#pragma unroll
      for (int s = 0; s < 2; s++) {
        float v[8];
#pragma unroll
        for (int i = 0; i < 8; i++) { const f32x2 t = stt[32 * tt + 16 * s + 8 * (i >> 2) + 4 * h + (i & 3)]; v[i] = t[1] * acc[j][8 * s + i] - t[0] * csum; }
        u32x4 w = {cvtpk(v[0], v[1]), cvtpk(v[2], v[3]), cvtpk(v[4], v[5]), cvtpk(v[6], v[7])};
        if (r == 20) w = u32x4{ONES, ONES, ONES, ONES};
        if (r > 20) w = u32x4{0u, 0u, 0u, 0u};
        if (r < 24) *(u32x4*)(vb + s * 768) = w;
      }
      continue;
    }
    f32x2 v[8];
    const f32x2 rs2 = {st_[1], st_[1]}, nm2 = {-st_[0], -st_[0]};
#pragma unroll
    for (int g = 0; g < 4; g++) {
      const f32x4 c4 = *(LAS f32x4*)(csf + (J0 + j) * 32 + 8 * g + 4 * h);
      v[2 * g] = __builtin_elementwise_fma(f32x2{c4[0], c4[1]}, nm2, f32x2{acc[j][4 * g], acc[j][4 * g + 1]} * rs2);
      v[2 * g + 1] = __builtin_elementwise_fma(f32x2{c4[2], c4[3]}, nm2, f32x2{acc[j][4 * g + 2], acc[j][4 * g + 3]} * rs2);
    }
    if (ROLE == 1 && type == T_K) {
      char* kb = p.ws + WS_KV + (size_t)rb * KV_CHUNK + tt * 1536 + r * 16 + h * 8;
      *(u32x2*)(kb) = u32x2{cvtpk(v[0][0], v[0][1]), cvtpk(v[1][0], v[1][1])};
      *(u32x2*)(kb + 512) = u32x2{cvtpk(v[2][0], v[2][1]), cvtpk(v[3][0], v[3][1])};
      *(u32x2*)(kb + 1024) = h ? u32x2{0x00003F80u, 0u} : u32x2{cvtpk(v[4][0], v[4][1]), cvtpk(v[5][0], v[5][1])};
      continue;
    }
    unsigned off; char* base;
    if (type == T_Q) {
      base = p.ws + WS_QI; off = ((tb * 80 + sidx[j] * 4 + h) * 32 + r) * 16;
#pragma unroll
      for (int i = 0; i < 8; i++) v[i] *= f32x2{QSCALE, QSCALE};
    } else {
      base = p.ws + (type == T_Z1 ? WS_Z1 : WS_Z2R); off = ((tb * 108 + sidx[j] * 4 + h) * 32 + r) * 16;
#pragma unroll
      for (int i = 0; i < 8; i++) v[i] = gelu2(v[i]);
    }
#pragma unroll
    for (int g = 0; g < 4; g += 2)
      st16_wt(base, off + g * 512, pair8(u32x2{cvtpk(v[2 * g][0], v[2 * g][1]), cvtpk(v[2 * g + 1][0], v[2 * g + 1][1])}, u32x2{cvtpk(v[2 * g + 2][0], v[2 * g + 2][1]), cvtpk(v[2 * g + 3][0], v[2 * g + 3][1])}), 3538944);
  }
  asm volatile("" :: "v"(junk));
}
__global__ void __launch_bounds__(768, 3) k1_proj(Params p) {
  extern __shared__ __attribute__((aligned(16))) char lds[];
  const char* codep = code_ptr();
  const int b = blockIdx.x, x = b & 7, j = b >> 3;
  const int rb = (x & 1) * 8 + (j & 7), cg = (x >> 1) * 4 + (j >> 3);
  const int wid = RFL(threadIdx.x >> 6);
  if (b == 0 && threadIdx.x < 256) ((u32x4*)(p.ws + WS_FLAG))[threadIdx.x] = u32x4{0u, 0u, 0u, 0u};
  if (wid < 4) k1_body<0>(p, lds, rb, cg, wid, codep); else if (wid < 8) k1_body<1>(p, lds, rb, cg, wid - 4, codep); else k1_loader(p, lds, rb, cg, wid - 8);
}

constexpr int K2_LDS = 81920;
constexpr float SM_OFF = 90.f;
#define EX(x) __builtin_amdgcn_exp2f(x)
#define AUNIT(Sc, Sn, qn0, qn1, Oc, Op, LD0, LD1, LD2, LD3) do { \
    Sc[0] = EX(Sc[0]); Sc[1] = EX(Sc[1]); SB(); \
    Sn = MFMA32(k0, qn0, zero16); SB(); LD0; SB(); \
    Sc[2] = EX(Sc[2]); Sc[3] = EX(Sc[3]); SB(); p1[2] = cvtpk(e12, e13); p1[3] = cvtpk(e14, e15); SB(); \
    Sc[4] = EX(Sc[4]); Sc[5] = EX(Sc[5]); SB(); \
    Op = MFMA32(v1, p1, Op); SB(); LD1; SB(); \
    Sc[6] = EX(Sc[6]); Sc[7] = EX(Sc[7]); SB(); p0[0] = cvtpk(Sc[0], Sc[1]); p0[1] = cvtpk(Sc[2], Sc[3]); SB(); \
    Sc[8] = EX(Sc[8]); Sc[9] = EX(Sc[9]); SB(); \
    Sn = MFMA32(k1, qn1, Sn); SB(); LD2; SB(); \
    Sc[10] = EX(Sc[10]); Sc[11] = EX(Sc[11]); SB(); p0[2] = cvtpk(Sc[4], Sc[5]); p0[3] = cvtpk(Sc[6], Sc[7]); SB(); \
    e12 = EX(Sc[12]); e13 = EX(Sc[13]); SB(); \
    Oc = MFMA32(v0, p0, Oc); SB(); LD3; SB(); \
    e14 = EX(Sc[14]); e15 = EX(Sc[15]); SB(); p1[0] = cvtpk(Sc[8], Sc[9]); p1[1] = cvtpk(Sc[10], Sc[11]); SB(); \
  } while (0)
#define ATILE(kn, v1c, v0n) do { \
    AUNIT(sA, sB, qB0, qB1, oA, oB, k0 = LDS128(kn), v1 = LDS128(v1c), k1 = LDS128((kn) + k1d), (void)0); \
    AUNIT(sB, sA, qA0, qA1, oB, oA, (void)0, (void)0, (void)0, v0 = LDS128(v0n)); \
  } while (0)
DI float max3f(float a, float b, float c) { return __builtin_fmaxf(__builtin_fmaxf(a, b), c); }
DI float max16(const f32x16& s) {
  const float a = max3f(max3f(s[0], s[1], s[2]), s[3], s[4]), b = max3f(max3f(s[5], s[6], s[7]), s[8], s[9]), c = max3f(max3f(s[10], s[11], s[12]), s[13], s[14]);
  return max3f(max3f(a, b, c), s[15], s[15]);
}
DI f32x16 attn_exact(const char* kv, u32x4 q0, u32x4 q1, int r, int h) {
  const f32x16 zero16 = {};
  q1[2] = 0u;
  f32x16 o = {}; float m = -3.0e38f;
  const int rr = r < 21 ? r : 21;
#pragma unroll 1
  for (int t = 0; t < 64; t++) {
    const char* cb = kv + (size_t)(t >> 2) * KV_CHUNK;
    const char* kb = cb + (t & 3) * 1536 + r * 16;
    const u32x4 k0 = *(const u32x4*)(kb + h * 512), k1 = *(const u32x4*)(kb + 1024);
    const char* vb = cb + KV_VOFF + (t & 3) * 1536 + h * 384 + rr * 16;
    const u32x4 v0 = *(const u32x4*)(vb), v1 = *(const u32x4*)(vb + 768);
    f32x16 s = MFMA32(k0, q0, zero16); s = MFMA32(k1, q1, s);
    float mx = s[0];
#pragma unroll
    for (int i = 1; i < 16; i++) mx = fmaxf(mx, s[i]);
    const float mn = fmaxf(m, half_max(mx)), a = EX(m - mn);
    m = mn;
#pragma unroll
    for (int i = 0; i < 16; i++) { o[i] *= a; s[i] = EX(s[i] - mn); }
    const u32x4 p0 = {cvtpk(s[0], s[1]), cvtpk(s[2], s[3]), cvtpk(s[4], s[5]), cvtpk(s[6], s[7])};
    const u32x4 p1 = {cvtpk(s[8], s[9]), cvtpk(s[10], s[11]), cvtpk(s[12], s[13]), cvtpk(s[14], s[15])};
    o = MFMA32(v0, p0, o); o = MFMA32(v1, p1, o);
  }
  return o;
}
DI void attn_role(const Params& p, char* lds_, int wg) {
  const int tid = threadIdx.x, lane = tid & 63, r = lane & 31, h = lane >> 5;
  const int wid = RFL(tid >> 6);
  const int pi = wid & 3, kh = wid >> 2;
  const int tb = wg >> 2, headA = (wg & 3) * 8 + 2 * pi;
  const unsigned l0 = (unsigned)(uintptr_t)lds_;
  LAS char* lds = (LAS char*)lds_;
  const char* kvg = p.ws + WS_KV;
  const char* kvsrc = kvg + (size_t)kh * 8 * KV_CHUNK + pi * 3072;
  const unsigned voff = lane * 16;
  const unsigned ring = l0 + kh * 3 * KV_CHUNK + pi * 3072;
#define APRIO 2
  __builtin_amdgcn_s_setprio(APRIO);
  glds3(kvsrc, voff, ring);
  u32x4 qA0, qA1, qB0, qB1;
  {
    const char* qrow = p.ws + WS_QI + (size_t)tb * 80 * 32 * 16;
    auto poff = [&](int c4) { return (unsigned)(r * 16 + (c4 >> 1) * 512 + (c4 & 1) * 8); };
    u32x2 a0, a1, a2, b0, b1, b2;
    const int cA = headA * 5, cB = cA + 5;
    asm volatile("global_load_dwordx2 %0, %6, %12\n\tglobal_load_dwordx2 %1, %7, %12\n\tglobal_load_dwordx2 %2, %8, %12\n\t"
                 "global_load_dwordx2 %3, %9, %12\n\tglobal_load_dwordx2 %4, %10, %12\n\tglobal_load_dwordx2 %5, %11, %12"
                 : "=&v"(a0), "=&v"(a1), "=&v"(a2), "=&v"(b0), "=&v"(b1), "=&v"(b2)
                 : "v"(poff(cA + 2 * h)), "v"(poff(cA + 2 * h + 1)), "v"(poff(cA + 4)), "v"(poff(cB + 2 * h)), "v"(poff(cB + 2 * h + 1)), "v"(poff(cB + 4)), "s"(qrow) : "memory");
    glds3(kvsrc + KV_CHUNK, voff, ring + KV_CHUNK);
    asm volatile("s_waitcnt vmcnt(3)\n\ts_barrier" : "+v"(a0), "+v"(a1), "+v"(a2), "+v"(b0), "+v"(b1), "+v"(b2) :: "memory");
    qA0 = u32x4{a0[0], a0[1], a1[0], a1[1]}; qB0 = u32x4{b0[0], b0[1], b1[0], b1[1]};
    qA1 = h ? u32x4{0u, 0u, 0u, 0u} : u32x4{a2[0], a2[1], 0u, 0u};
    qB1 = h ? u32x4{0u, 0u, 0u, 0u} : u32x4{b2[0], b2[1], 0u, 0u};
  }
  const f32x16 zero16 = {};
  const int k1d = 1024 - h * 512;
  const int rr = r < 21 ? r : 21;
  u32x4 k0, k1; f32x16 sA, sB;
  {
    LAS char* kb0 = lds + h * 512 + r * 16;
    k0 = LDS128(kb0); k1 = LDS128(kb0 + k1d);
    sA = MFMA32(k0, qA0, zero16); sA = MFMA32(k1, qA1, sA); sB = MFMA32(k0, qB0, zero16); sB = MFMA32(k1, qB1, sB);
    float mA = max16(sA), mB = max16(sB);
    k0 = LDS128(kb0 + 1536); k1 = LDS128(kb0 + 1536 + k1d);
    sA = MFMA32(k0, qA0, zero16); sA = MFMA32(k1, qA1, sA); sB = MFMA32(k0, qB0, zero16); sB = MFMA32(k1, qB1, sB);
    mA = half_max(fmaxf(mA, max16(sA))); mB = half_max(fmaxf(mB, max16(sB)));
    if (h == 0) { qA1[2] = cvtpk(-(mA + SM_OFF), 0.f); qB1[2] = cvtpk(-(mB + SM_OFF), 0.f); }
  }
  LAS char* kbase = lds + kh * 3 * KV_CHUNK + h * 512 + r * 16;
  LAS char* vbase = lds + kh * 3 * KV_CHUNK + KV_VOFF + h * 384 + rr * 16;
  k0 = LDS128(kbase); k1 = LDS128(kbase + k1d);
  sA = MFMA32(k0, qA0, zero16); sA = MFMA32(k1, qA1, sA);
  u32x4 v0 = LDS128(vbase), v1 = v0, p0 = {0u, 0u, 0u, 0u}, p1 = {0u, 0u, 0u, 0u};
  f32x16 oA = {}, oB = {};
  float e12 = 0.f, e13 = 0.f, e14 = 0.f, e15 = 0.f;
  asm volatile("s_waitcnt vmcnt(0) lgkmcnt(0)\n\ts_barrier" ::: "memory");
  int sc = 0;
#pragma unroll 1
  for (int c = 0; c < 8; c++) {
    const int sn = sc == 2 * KV_CHUNK ? 0 : sc + KV_CHUNK, sp = sn == 2 * KV_CHUNK ? 0 : sn + KV_CHUNK;
    if (c + 2 < 8) glds3(kvsrc + (size_t)(c + 2) * KV_CHUNK, voff, ring + sp);
    LAS char* kc = kbase + sc; LAS char* kn = kbase + sn; LAS char* vc = vbase + sc; LAS char* vn = vbase + sn;
    ATILE(kc + 1 * 1536, vc + 1 * 768, vc + 2 * 768);
    ATILE(kc + 2 * 1536, vc + 3 * 768, vc + 4 * 768);
    ATILE(kc + 3 * 1536, vc + 5 * 768, vc + 6 * 768);
    ATILE(kn, vc + 7 * 768, vn);
    asm volatile("s_waitcnt vmcnt(0) lgkmcnt(0)\n\ts_barrier" ::: "memory");
    sc = sn;
  }
  p1[2] = cvtpk(e12, e13); p1[3] = cvtpk(e14, e15);
  oB = MFMA32(v1, p1, oB);
  LAS f32x4* ex = (LAS f32x4*)(lds + (pi * 2 + kh) * 3072 + lane * 16);
  LAS const f32x4* exr = (LAS const f32x4*)(lds + (pi * 2 + (kh ^ 1)) * 3072 + lane * 16);
  f32x16 o;
  if (kh == 0) { o = oA;
#pragma unroll
    for (int g = 0; g < 3; g++) ex[g * 64] = f32x4{oB[4 * g], oB[4 * g + 1], oB[4 * g + 2], oB[4 * g + 3]};
  } else { o = oB;
#pragma unroll
    for (int g = 0; g < 3; g++) ex[g * 64] = f32x4{oA[4 * g], oA[4 * g + 1], oA[4 * g + 2], oA[4 * g + 3]};
  }
  BARRIER();
#pragma unroll
  for (int g = 0; g < 3; g++) { f32x4 t = exr[g * 64]; o[4 * g] += t[0]; o[4 * g + 1] += t[1]; o[4 * g + 2] += t[2]; o[4 * g + 3] += t[3]; }
  {
    float big = 0.f;
#pragma unroll
    for (int i = 0; i < 12; i++) big = fmaxf(big, fabsf(o[i]));
    float chk = 0.f;
#pragma unroll
    for (int i = 0; i < 12; i++) chk += o[i] * 0.f;
    const bool bad = !(big < 1.0e36f) || !(chk == 0.f);
    if (__builtin_expect(__any(bad), 0)) o = attn_exact(kvg, kh ? qB0 : qA0, kh ? qB1 : qA1, r, h);
  }
  auto lr = __builtin_amdgcn_permlane32_swap(__float_as_uint(o[8]), __float_as_uint(o[8]), false, false);
  const float il = __builtin_amdgcn_rcpf(__uint_as_float(lr[1]));
  const int tok = tb * 32 + r, head = headA + kh;
#pragma unroll
  for (int g = 0; g < 3; g++) {
    if (g < 2 || h == 0) {
      const int kk = head * 20 + (2 * g + h) * 4;
      u32x2 w = {cvtpk(o[4 * g] * il, o[4 * g + 1] * il), cvtpk(o[4 * g + 2] * il, o[4 * g + 3] * il)};
      *(u32x2*)(p.ws + WS_AO + ((size_t)(kk >> 3) * 2048 + tok) * 16 + ((kk >> 2) & 1) * 8) = w;
    }
  }
}

DI int fb_start(int fb) { return (fb >> 2) * 216 + (fb & 3) * 56; }
constexpr int SP_STAGE = 24576, SP_ZOFF = 16384;
typedef __attribute__((address_space(1))) unsigned gu32;
DI void sp_role(const Params& p, char* lds_, int b) {
  const int wid = RFL(threadIdx.x >> 6);
  __builtin_amdgcn_s_setprio(3);
  const int tid = threadIdx.x, lane = tid & 63, r = lane & 31, h = lane >> 5;
  const unsigned l0 = (unsigned)(uintptr_t)lds_;
  LAS char* lds = (LAS char*)lds_;
  const int x = b & 7, jj = b >> 3;
  const int tbk = (x >> 1) * 4 + (jj & 3), fb = (x & 1) * 8 + (jj >> 2);
  gu32* cnt = (gu32*)(p.ws + WS_FLAG);
  if (wid >= 4) {
    const int lw = wid - 4;
    const char* wb = p.wsp + (size_t)tbk * 128 * 4096;
    unsigned wv4[4];
#pragma unroll
    for (int i = 0; i < 4; i++) { const unsigned row = 8 * (4 * lw + i) + (lane >> 3); wv4[i] = row * 4096 + (((lane & 7) ^ ((row >> 1) & 7)) << 4); }
    const char* zb = p.ws + WS_Z2N + (size_t)fb * 32 * 8192 + lw * 2048;
    const unsigned zv = lane * 16;
    auto issue_w = [&](int st) {
#pragma unroll
      for (int i = 0; i < 4; i++) gldsn(wb + st * 128, wv4[i], l0 + (st % 3) * SP_STAGE + (4 * lw + i) * 1024);
    };
    auto issue_z = [&](int st) {
      asm volatile("s_mov_b32 m0, %2\n\ts_nop 0\n\tglobal_load_lds_dwordx4 %0, %1\n\tglobal_load_lds_dwordx4 %0, %1 offset:1024"
                   :: "v"(zv), "s"(zb + (size_t)st * 8192), "s"(l0 + (st % 3) * SP_STAGE + SP_ZOFF + lw * 2048) : "memory");
    };
    BARRIER(); BARRIER();
    issue_w(0); issue_w(1);
    BARRIER();
    {
      const int u = b * 3 + lw - 1;
      if (lw > 0 && u < 752) {
        const int ks = u >> 3, nb = u & 7;
        const char* src = (ks < 40 ? p.wo + (size_t)ks * 16 * 1280 : p.wout + (size_t)(ks - 40) * 16 * 1280) + nb * 160;
        LAS char* sc = lds + 3 * SP_STAGE + (lw - 1) * 2560;
#pragma unroll
        for (int i = 0; i < 3; i++) {
          const int c = lane + 64 * i, rw = c / 10, cc = c - rw * 10;
          if (c < 160) *(LAS u32x4*)(sc + c * 16) = *(const u32x4*)(src + rw * 1280 + cc * 16);
        }
        asm volatile("s_waitcnt vmcnt(0) lgkmcnt(0)" ::: "memory");
        const int blk = (lane >> 4) & 1, q = (lane & 15) >> 2, pp = lane & 3;
        LAS char* ta = sc + (8 * h + q) * 160 + 32 * blk + 8 * pp;
        char* dst = p.ws + WS_W3 + ((size_t)nb * 94 + ks) * 2560;
        const u32x4 f0 = ldtr(ta, 0, 640), f1 = ldtr(ta, 64, 64 + 640), f2 = ldtr(ta, 128, 128 + 640);
        *(u32x4*)(dst + lane * 16) = f0;
        *(u32x4*)(dst + 1024 + lane * 16) = f1;
        if (r < 16) *(u32x4*)(dst + 2048 + (h * 16 + r) * 16) = f2;
        asm volatile("s_waitcnt vmcnt(0) lgkmcnt(0)" ::: "memory");
      }
    }
    if (lw > 0) {
      const char* src = p.wsp + (unsigned)RFL(b * 32768 + (lw - 1) * 12288);
      const int n2 = lw == 3 ? 4 : 6;
      for (int i = 0; i < n2; i++)
        asm volatile("s_mov_b32 m0, %2\n\ts_nop 0\n\tglobal_load_lds_dwordx4 %0, %1\n\tglobal_load_lds_dwordx4 %0, %1 offset:1024"
                     :: "v"(zv), "s"(src + i * 2048), "s"(l0 + 3 * SP_STAGE - 2048) : "memory");
    }
    if (lw == 0) {
      {
      __builtin_amdgcn_s_sleep(24);
      unsigned spins = 0;
      for (;;) {
        const unsigned c = __hip_atomic_load(cnt + (lane & 31) * 16, __ATOMIC_RELAXED, __HIP_MEMORY_SCOPE_AGENT);
        if (__all(c >= 8u)) break;
        if (++spins > (1u << 21)) break;
        __builtin_amdgcn_s_sleep(6);
      }
      }
      __builtin_amdgcn_fence(__ATOMIC_ACQUIRE, "agent");
      const char* z00 = p.ws + WS_Z2N + (size_t)fb * 32 * 8192;
      constexpr int NZ0 = 4;
#pragma unroll
      for (int i = 0; i < NZ0; i++)
        asm volatile("s_mov_b32 m0, %2\n\ts_nop 0\n\tglobal_load_lds_dwordx4 %0, %1\n\tglobal_load_lds_dwordx4 %0, %1 offset:1024\n\tglobal_load_lds_dwordx4 %0, %1 offset:2048\n\tglobal_load_lds_dwordx4 %0, %1 offset:3072"
                     :: "v"(zv), "s"(z00 + i * 4096), "s"(l0 + (i >> 1) * SP_STAGE + SP_ZOFF + (i & 1) * 4096) : "memory");
      WAITV(8);
    }
#pragma unroll 1
    for (int st = 0; st < 32; st++) {
      if (st == 0) { if (lw) WAITV(0); } else if (st < 31) WAITV(6); else WAITV(0);
      BARRIER();
      if (st + 2 < 32) { issue_w(st + 2); issue_z(st + 2); }
    }
    return;
  }
  {
    LAS float* red = (LAS float*)(lds + K2_LDS - 256);
    const bool act = tid < 216;
    const int g8 = act ? tid >> 1 : 107, half = tid & 1;
    const char* src = p.ws + WS_Z2R + ((size_t)((b >> 2) * 108 + g8) * 32 + (b & 3) * 8 + 4 * half) * 16;
    u32x4 raw[4];
#pragma unroll
    for (int i = 0; i < 4; i++) raw[i] = *(const u32x4*)(src + i * 16);
    const unsigned ONES = act ? 0x3F803F80u : 0u;
    LAS float* part = (LAS float*)(lds + 2 * SP_STAGE);
#pragma unroll
    for (int t = 0; t < 4; t++) {
      float su = 0.f, sq = 0.f;
#pragma unroll
      for (int e = 0; e < 4; e++) { const unsigned a = act ? raw[t][e] : 0u; su = dot2(a, ONES, su); sq = dot2(a, a, sq); }
      part[(2 * (4 * half + t)) * 128 + (tid >> 1)] = su;
      part[(2 * (4 * half + t) + 1) * 128 + (tid >> 1)] = sq;
    }
    BARRIER();
    {
      float s4[4];
#pragma unroll
      for (int k = 0; k < 4; k++) { const f32x2 a = *(LAS f32x2*)(part + (4 * wid + k) * 128 + lane * 2); s4[k] = a[0] + a[1]; }
#pragma unroll
      for (int k = 0; k < 4; k++) s4[k] = wave_sum(s4[k]);
      if (lane < 2) {
        const float su = lane ? s4[2] : s4[0], sq = lane ? s4[3] : s4[1];
        const float mu = su * (1.f / 864.f), var = fmaxf(sq * (1.f / 864.f) - mu * mu, 0.f), rs = __builtin_amdgcn_rsqf(var + 1e-5f);
        ((LAS f32x2*)red)[2 * wid + lane] = f32x2{rs, -mu * rs};
      }
    }
    BARRIER();
    LAS char* tr = lds + 2 * SP_STAGE + 8192;
    {
      float v[4][8];
#pragma unroll
      for (int t = 0; t < 4; t++) {
        const f32x2 c = ((LAS f32x2*)red)[4 * half + t];
#pragma unroll
        for (int e = 0; e < 4; e++) { v[t][2 * e] = __builtin_fmaf(bflo(raw[t][e]), c[0], c[1]); v[t][2 * e + 1] = __builtin_fmaf(bfhi(raw[t][e]), c[0], c[1]); }
      }
      if (act) {
#pragma unroll
        for (int i = 0; i < 8; i++) *(LAS u32x2*)(tr + (8 * g8 + i) * 16 + half * 8) = u32x2{cvtpk(v[0][i], v[1][i]), cvtpk(v[2][i], v[3][i])};
      }
    }
    asm volatile("s_waitcnt lgkmcnt(0)" ::: "memory");
    {
      const __amdgpu_buffer_rsrc_t rsrc = __builtin_amdgcn_make_buffer_rsrc((void*)(p.ws + WS_Z2N), 0, 4194304, 0x00020000);
      const unsigned sbase = (((b >> 3) * 4 + ((b >> 1) & 3)) * 2 + (b & 1)) * 1024;
#pragma unroll
      for (int i = 0; i < 4; i++) {
        const int f = 256 * wid + 64 * i + lane;
        if (f < 864) {
          const u32x4 w = *(LAS u32x4*)(tr + f * 16);
          const int q4 = (f >= 216) + (f >= 432) + (f >= 648), rem = f - q4 * 216, sub = (rem >= 56) + (rem >= 112) + (rem >= 168);
          const unsigned off = (unsigned)(q4 * 4 + sub) * (32u * 8192u) + sbase + (rem - sub * 56) * 16;
          __builtin_amdgcn_raw_buffer_store_b128(w, rsrc, off, 0, 16);
        }
      }
    }
    asm volatile("s_waitcnt vmcnt(0)" ::: "memory");
    BARRIER();
    if (tid == 0) __hip_atomic_fetch_add(cnt + (b >> 3) * 16, 1u, __ATOMIC_RELAXED, __HIP_MEMORY_SCOPE_AGENT);
  }
  const unsigned row = 32 * wid + r;
  unsigned wa[4];
#pragma unroll
  for (int ks = 0; ks < 4; ks++) wa[ks] = row * 128 + ((32 * ks + 16 * h) ^ (16 * ((row >> 1) & 7)));
  const unsigned za = SP_ZOFF + h * 1024 + r * 16;
  f32x16 acc0 = {}, acc1 = {};
#pragma unroll 1
  for (int st = 0; st < 32; st++) {
    BARRIER();
    LAS char* sl = lds + (st % 3) * SP_STAGE;
    u32x4 wf[4], z0[4], z1[4];
#pragma unroll
    for (int ks = 0; ks < 4; ks++) { wf[ks] = LDS128(sl + wa[ks]); z0[ks] = LDS128(sl + za + ks * 2048); z1[ks] = LDS128(sl + za + ks * 2048 + 512); }
#pragma unroll
    for (int ks = 0; ks < 4; ks++) { acc0 = MFMA32(z0[ks], wf[ks], acc0); acc1 = MFMA32(z1[ks], wf[ks], acc1); }
  }
  const int tok = tbk * 128 + row, f0 = fb_start(fb), width = (fb & 3) == 3 ? 48 : 56;
  const float bias = bflo(*(const unsigned short*)(p.bsp + tok * 2));
  const char* z1b = p.ws + WS_Z1 + ((size_t)((tok >> 5) * 108 + (f0 >> 3)) * 32 + r) * 16 + h * 8;
#pragma unroll
  for (int j = 0; j < 2; j++) {
    const f32x16& a = j ? acc1 : acc0;
    u32x2 w[4];
#pragma unroll
    for (int g = 0; g < 4; g++) {
      const int fl = 32 * j + 8 * g;
      u32x2 z = {0u, 0u};
      if (fl < width) z = *(const u32x2*)(z1b + (4 * j + g) * 512);
      w[g] = u32x2{cvtpk(bflo(z[0]) * (a[4 * g] + bias), bfhi(z[0]) * (a[4 * g + 1] + bias)), cvtpk(bflo(z[1]) * (a[4 * g + 2] + bias), bfhi(z[1]) * (a[4 * g + 3] + bias))};
    }
#pragma unroll
    for (int g = 0; g < 4; g += 2) {
      const u32x4 w8 = pair8(w[g], w[g + 1]);
      const int fl = 32 * j + 8 * (g + h);
      if (fl < width) st16_wt(p.ws + WS_AO, ((80 + ((f0 + fl) >> 3)) * 2048 + tok) * 16, w8, 6160384);
    }
  }
}
__global__ void __launch_bounds__(512, 4) k2_mix(Params p) {
  extern __shared__ __attribute__((aligned(16))) char lds[];
  if (blockIdx.x >= 256) sp_role(p, lds, blockIdx.x - 256); else attn_role(p, lds, blockIdx.x);
}

constexpr int K3_ACT = 16384, K3_W = 20480, K3_STAGE = K3_ACT + K3_W, K3_NS = 4;
constexpr int K3_LDS = K3_NS * K3_STAGE + 256;
__global__ void __launch_bounds__(640, 2) k3_out(Params p) {
  extern __shared__ __attribute__((aligned(16))) char lds_[];
  const int b = blockIdx.x, x = b & 7, jj = b >> 3;
  const int tb = x * 4 + (jj & 3), nb = jj >> 2;
  const int tid = threadIdx.x, lane = tid & 63, r = lane & 31, h = lane >> 5;
  const int wid = RFL(tid >> 6);
  const unsigned l0 = (unsigned)(uintptr_t)lds_;
  LAS char* lds = (LAS char*)lds_;
  if (wid >= 6) {
    const int lw = wid - 6;
    const char* ab = p.ws + WS_AO + (size_t)tb * 64 * 16;
    const char* wb = p.ws + WS_W3 + (size_t)nb * 94 * 2560;
    const unsigned lv = lane * 16;
    auto issue = [&](int st) {
      const unsigned slot = l0 + (st % K3_NS) * K3_STAGE;
#pragma unroll
      for (int i = 0; i < 4; i++) {
        const int pc = lw * 4 + i;
        gldsn(ab + ((size_t)st * 16 + (st == 11 && pc >= 12 ? 0 : pc)) * 32768, lv, slot + pc * 1024);
      }
      const char* ws_ = wb + (size_t)st * 20480 + (st == 11 && lw == 3 ? 0 : lw * 5120);
      asm volatile("s_mov_b32 m0, %2\n\ts_nop 0\n\tglobal_load_lds_dwordx4 %0, %1\n\tglobal_load_lds_dwordx4 %0, %1 offset:1024\n\tglobal_load_lds_dwordx4 %0, %1 offset:2048\n\tglobal_load_lds_dwordx4 %0, %1 offset:3072"
                   :: "v"(lv), "s"(ws_), "s"(slot + K3_ACT + lw * 5120) : "memory");
      gldsn(ws_ + 4096, lv, slot + K3_ACT + lw * 5120 + 4096);
    };
    issue(0); issue(1); issue(2);
#pragma unroll 1
    for (int st = 0; st < 12; st++) {
      if (st < 10) WAITV(18); else if (st == 10) WAITV(9); else WAITV(0);
      BARRIER();
      if (st + 3 < 12) issue(st + 3);
    }
    return;
  }
  const int nt = wid % 3, tt = wid / 3;
  u32x4 xr[2];
#pragma unroll
  for (int k = 0; k < 2; k++) { const int i = tid + 384 * k < 640 ? tid + 384 * k : 0, row = i / 10, c = i - row * 10;
    xr[k] = *(const u32x4*)(p.x + ((size_t)(tb * 64 + row) * 640 + nb * 80 + c * 8) * 2); }
  const unsigned wa = K3_ACT + nt * 1024 + (nt < 2 ? lane : h * 16 + (r & 15)) * 16;
  const unsigned aa = h * 1024 + (32 * tt + r) * 16;
  f32x16 acc = {};
#pragma unroll 1
  for (int st = 0; st < 12; st++) {
    BARRIER();

    LAS char* sl = lds + (st % K3_NS) * K3_STAGE;
    u32x4 af[8], wf[8];
#pragma unroll
    for (int ks = 0; ks < 6; ks++) { af[ks] = LDS128(sl + aa + ks * 2048); wf[ks] = LDS128(sl + wa + ks * 2560); }
    SB();
#pragma unroll
    for (int ks = 0; ks < 2; ks++) acc = MFMA32(wf[ks], af[ks], acc);
    SB();
    if (st < 11) {
#pragma unroll
      for (int ks = 6; ks < 8; ks++) { af[ks] = LDS128(sl + aa + ks * 2048); wf[ks] = LDS128(sl + wa + ks * 2560); }
    }
    SB();
#pragma unroll
    for (int ks = 2; ks < 6; ks++) acc = MFMA32(wf[ks], af[ks], acc);
    if (st < 11) {
#pragma unroll
      for (int ks = 6; ks < 8; ks++) acc = MFMA32(wf[ks], af[ks], acc);
    }
  }
  BARRIER();
  LAS float* ot = (LAS float*)lds;
  constexpr int OP = 84;
#pragma unroll
  for (int g = 0; g < 4; g++) {
    const int n = nt * 32 + 8 * g + 4 * h;
    if (n < 80) *(LAS f32x4*)(ot + (32 * tt + r) * OP + n) = f32x4{acc[4 * g], acc[4 * g + 1], acc[4 * g + 2], acc[4 * g + 3]};
  }
  BARRIER();
#pragma unroll
  for (int k = 0; k < 2; k++) {
    const int i = tid + 384 * k;
    if (i < 640) {
      const int row = i / 10, c = i - row * 10;
      const size_t go = ((size_t)(tb * 64 + row) * 640 + nb * 80 + c * 8) * 2;
      const f32x4 a = *(LAS f32x4*)(ot + row * OP + c * 8), a2 = *(LAS f32x4*)(ot + row * OP + c * 8 + 4);
      const u32x4 w = {cvtpk(a[0] + bflo(xr[k][0]), a[1] + bfhi(xr[k][0])), cvtpk(a[2] + bflo(xr[k][1]), a[3] + bfhi(xr[k][1])),
                       cvtpk(a2[0] + bflo(xr[k][2]), a2[1] + bfhi(xr[k][2])), cvtpk(a2[2] + bflo(xr[k][3]), a2[3] + bfhi(xr[k][3]))};
      st16_wt(p.out, (unsigned)go, w, 2621440);
    }
  }
}

extern "C" void kernel_launch(void* const* d_in, const int* in_sizes, int n_in, void* d_out, int out_size, void* d_ws, size_t ws_size, hipStream_t stream) {
  Params p;
  p.x = (const char*)d_in[0]; p.wq = (const char*)d_in[1]; p.wk = (const char*)d_in[2]; p.wv = (const char*)d_in[3]; p.wo = (const char*)d_in[4];
  p.win = (const char*)d_in[6]; p.wsp = (const char*)d_in[7]; p.bsp = (const char*)d_in[8]; p.wout = (const char*)d_in[9];
  p.ws = (char*)d_ws; p.out = (char*)d_out;
  (void)hipFuncSetAttribute((const void*)k1_proj, hipFuncAttributeMaxDynamicSharedMemorySize, K1_LDS);
  (void)hipFuncSetAttribute((const void*)k2_mix, hipFuncAttributeMaxDynamicSharedMemorySize, K2_LDS);
  (void)hipFuncSetAttribute((const void*)k3_out, hipFuncAttributeMaxDynamicSharedMemorySize, K3_LDS);
  k1_proj<<<256, 768, K1_LDS, stream>>>(p);
  k2_mix<<<512, 512, K2_LDS, stream>>>(p);
  k3_out<<<256, 640, K3_LDS, stream>>>(p);
}
```

```cpp
#include <hip/hip_runtime.h>
#include <stdint.h>
#include <type_traits>
typedef __bf16 bf16x8 __attribute__((ext_vector_type(8)));
typedef __bf16 bf16x2 __attribute__((ext_vector_type(2)));
typedef short s16x4 __attribute__((ext_vector_type(4)));
typedef float f32x16 __attribute__((ext_vector_type(16)));
typedef float f32x4 __attribute__((ext_vector_type(4)));
typedef float f32x2 __attribute__((ext_vector_type(2)));
typedef unsigned u32x4 __attribute__((ext_vector_type(4)));
typedef unsigned u32x2 __attribute__((ext_vector_type(2)));
#define DI __device__ __forceinline__
#define LAS __attribute__((address_space(3)))
#define GAS __attribute__((address_space(1)))
#define SB() __builtin_amdgcn_sched_barrier(0)
#define RFL(x) __builtin_amdgcn_readfirstlane(x)
#define MFMA32(a, b, c) __builtin_amdgcn_mfma_f32_32x32x16_bf16(__builtin_bit_cast(bf16x8, a), __builtin_bit_cast(bf16x8, b), c, 0, 0, 0)
#define MFMA16(a, b, c) __builtin_amdgcn_mfma_f32_16x16x32_bf16(__builtin_bit_cast(bf16x8, a), __builtin_bit_cast(bf16x8, b), c, 0, 0, 0)
DI f32x4 mfma4sq(unsigned x0, unsigned x1, f32x4 c) { u32x2 v = {x0, x1}; s16x4 a = __builtin_bit_cast(s16x4, v); return __builtin_amdgcn_mfma_f32_4x4x4bf16_1k(a, a, c, 0, 0, 0); }
#define MFMA4(x0, x1, c) mfma4sq(x0, x1, c)
#define LDS128(p) (*(const LAS u32x4*)(p))
DI unsigned cvtpk(float lo, float hi) { f32x2 v = {lo, hi}; bf16x2 b = __builtin_convertvector(v, bf16x2); return __builtin_bit_cast(unsigned, b); }
DI float bflo(unsigned u) { return __uint_as_float(u << 16); }
DI float bfhi(unsigned u) { return __uint_as_float(u & 0xffff0000u); }
DI void sqacc(float& a, float x) { asm("v_fmac_f32_e32 %0, %1, %1" : "+v"(a) : "v"(x)); }
DI void addacc(float& a, float x) { asm("v_add_f32_e32 %0, %0, %1" : "+v"(a) : "v"(x)); }
DI float dot2(unsigned a, unsigned b, float c) { return __builtin_amdgcn_fdot2_f32_bf16(__builtin_bit_cast(bf16x2, a), __builtin_bit_cast(bf16x2, b), c, false); }
DI float half_sum(float m) { auto r = __builtin_amdgcn_permlane32_swap(__float_as_uint(m), __float_as_uint(m), false, false); return __uint_as_float(r[0]) + __uint_as_float(r[1]); }
DI float half_max(float m) { auto r = __builtin_amdgcn_permlane32_swap(__float_as_uint(m), __float_as_uint(m), false, false); return fmaxf(__uint_as_float(r[0]), __uint_as_float(r[1])); }
template <int CTRL, int RMASK> DI float dpp_add(float v) { return v + __uint_as_float(__builtin_amdgcn_update_dpp(0u, __float_as_uint(v), CTRL, RMASK, 0xf, true)); }
DI float wave_sum(float v) {
  v = dpp_add<0x111, 0xf>(v); v = dpp_add<0x112, 0xf>(v); v = dpp_add<0x114, 0xf>(v); v = dpp_add<0x118, 0xf>(v);
  v = dpp_add<0x142, 0xa>(v); v = dpp_add<0x143, 0xc>(v);
  return __uint_as_float(__builtin_amdgcn_readlane(__float_as_uint(v), 63));
}
DI void glds1(const void* sbase, unsigned voff, unsigned lds_base) {
  unsigned sv;
  asm volatile("s_mov_b32 %0, m0\n\ts_mov_b32 m0, %3\n\ts_nop 0\n\tglobal_load_lds_dwordx4 %1, %2\n\ts_mov_b32 m0, %0"
               : "=&s"(sv) : "v"(voff), "s"(sbase), "s"(lds_base) : "memory"); }
DI void glds3(const void* sbase, unsigned voff, unsigned lds_base) {
  unsigned sv;
  asm volatile("s_mov_b32 %0, m0\n\ts_mov_b32 m0, %3\n\ts_nop 0\n\tglobal_load_lds_dwordx4 %1, %2\n\tglobal_load_lds_dwordx4 %1, %2 offset:1024\n\tglobal_load_lds_dwordx4 %1, %2 offset:2048\n\ts_mov_b32 m0, %0"
               : "=&s"(sv) : "v"(voff), "s"(sbase), "s"(lds_base) : "memory"); }
DI void gldsn(const void* sbase, unsigned voff, unsigned lds_base) {
  asm volatile("s_mov_b32 m0, %2\n\ts_nop 0\n\tglobal_load_lds_dwordx4 %0, %1" :: "v"(voff), "s"(sbase), "s"(lds_base) : "memory"); }
DI void gldsn_dev(const void* sbase, unsigned voff, unsigned lds_base) {
  asm volatile("s_mov_b32 m0, %2\n\ts_nop 0\n\tglobal_load_lds_dwordx4 %0, %1 sc1" :: "v"(voff), "s"(sbase), "s"(lds_base) : "memory"); }
DI u32x4 ldtr(const LAS char* p, int off0, int off1) {
  typedef LAS s16x4* lp;
  s16x4 a = __builtin_amdgcn_ds_read_tr16_b64_v4i16((lp)(p + off0)), b = __builtin_amdgcn_ds_read_tr16_b64_v4i16((lp)(p + off1));
  u32x2 x = __builtin_bit_cast(u32x2, a), y = __builtin_bit_cast(u32x2, b);
  return u32x4{x[0], x[1], y[0], y[1]};
}
DI void st16_wt(char* base, unsigned off, u32x4 w, unsigned bytes) {
  const __amdgpu_buffer_rsrc_t rs = __builtin_amdgcn_make_buffer_rsrc((void*)base, 0, bytes, 0x00020000);
  __builtin_amdgcn_raw_buffer_store_b128(w, rs, off, 0, 16);
}
DI u32x4 pair8(u32x2 a, u32x2 b) {
  auto s0 = __builtin_amdgcn_permlane32_swap(a[0], b[0], false, false);
  auto s1 = __builtin_amdgcn_permlane32_swap(a[1], b[1], false, false);
  return u32x4{s0[0], s1[0], s0[1], s1[1]};
}
DI void touch_lines(const void* base, int lines, unsigned& junk, int lane) {
  for (int l0 = 0; l0 < lines; l0 += 64) { const int l = l0 + lane < lines ? l0 + lane : lines - 1;
    asm volatile("global_load_dword %0, %1, %2" : "+v"(junk) : "v"((unsigned)l * 128u), "s"(base) : "memory"); }
}
DI const char* code_ptr() { unsigned long long pc; asm volatile("s_getpc_b64 %0" : "=s"(pc)); return (const char*)(pc & ~127ull); }
#define WAITV(n) asm volatile("s_waitcnt vmcnt(" #n ")" ::: "memory")
#define BARRIER() asm volatile("s_waitcnt lgkmcnt(0)\n\ts_barrier" ::: "memory")

constexpr int SEQ = 2048, DM = 640, NFH = 864;
constexpr size_t WS_FLAG = 0;
constexpr size_t WS_QI  = 8192;
constexpr size_t WS_KV  = WS_QI + 2621440;
constexpr size_t WS_Z1  = WS_KV + 196608;
constexpr size_t WS_Z2R = WS_Z1 + 3538944;
constexpr size_t WS_Z2N = WS_Z2R + 3538944;
constexpr size_t WS_AO  = WS_Z2N + 4194304;
constexpr size_t WS_W3  = WS_AO + 6160384;
constexpr size_t WS_END = WS_W3 + 8 * 94 * 2560;
constexpr int KV_CHUNK = 12288, KV_VOFF = 6144;
struct Params {
  const char *x, *wq, *wk, *wv, *wo, *win, *wsp, *bsp, *wout;
  char* ws; char* out;
};

constexpr int K1_STAGE = 36864, K1_NS = 4, K1_XT = 16384;
constexpr int K1_CSP = K1_NS * K1_STAGE;
constexpr int K1_CSF = K1_CSP + 2560;
constexpr int K1_STP = K1_CSF + 640;
constexpr int K1_LDS = K1_STP + 1024;
#define CODE_PF_LINES 200
constexpr float QSCALE = 0.22360679774997896f * 1.4426950408889634f;
enum { T_Q = 0, T_K = 1, T_V = 2, T_Z1 = 3, T_Z2 = 4, T_E = 5 };
DI void k1_slot(int cg, int j, int& type, int& idx) {
  if (j < 3 || (j == 3 && cg < 6)) { const int z = cg < 6 ? cg * 4 + j : 24 + (cg - 6) * 3 + j; type = z < 27 ? T_Z1 : T_Z2; idx = z < 27 ? z : z - 27; }
  else if (cg == 15) { type = j == 3 ? T_K : T_V; idx = 0; }
  else if (cg < 4) { type = T_E; idx = 0; }
  else if (cg < 6) { type = T_Q; idx = cg - 4; }
  else { type = T_Q; idx = 2 + (cg - 6) * 2 + (j - 3); }
}
DI f32x2 gelu2(f32x2 x) {
  const f32x2 c1 = {-0.10294324f, -0.10294324f}, c0 = {-2.3022082f, -2.3022082f}, one = {1.f, 1.f};
  const f32x2 a = x * __builtin_elementwise_fma(x * x, c1, c0);
  const f32x2 d = f32x2{__builtin_amdgcn_exp2f(a[0]), __builtin_amdgcn_exp2f(a[1])} + one;
  return x * f32x2{__builtin_amdgcn_rcpf(d[0]), __builtin_amdgcn_rcpf(d[1])};
}
DI void k1_loader(const Params& p, char* lds_, int rb, int cg, int lw) {
  const int lane = threadIdx.x & 63;
  __builtin_amdgcn_s_setprio(3);
  const unsigned l0 = (unsigned)(uintptr_t)lds_;
  const char* base[5]; unsigned stride[5], voff[5];
  const uintptr_t pwq = (uintptr_t)p.wq, pwin = (uintptr_t)p.win, pwk = (uintptr_t)p.wk, pwv = (uintptr_t)p.wv;
#pragma unroll
  for (int j = 0; j < 5; j++) {
    int type, idx; k1_slot(cg, j, type, idx);
    const unsigned rowp = 16 * lw + (lane >> 2), ch = (lane & 3) * 16;
    const bool kvs = j >= 3 && cg == 15, isq = type == T_Q || type == T_E;
    const uintptr_t a = kvs ? (j == 3 ? pwk : pwv) : isq ? pwq + idx * 64 : pwin + (type == T_Z2 ? 1728 : 0) + idx * 64;
    base[j] = (const char*)a;
    stride[j] = kvs ? 2560u : isq ? 64u * 1280u : 64u * 3456u;
    const unsigned o = (lw < 2 ? lw : 2) * 1024 + lane * 16;
    voff[j] = kvs ? (o < 2544u ? o : 2544u) : rowp * (isq ? 1280u : 3456u) + ch;
  }
  const char* xb = p.x + (size_t)rb * 128 * 1280;
  unsigned xv[4];
#pragma unroll
  for (int i = 0; i < 4; i++) { const unsigned row = 8 * (4 * lw + i) + (lane >> 3); xv[i] = row * 1280 + (((lane & 7) ^ ((row >> 1) & 7)) << 4); }
  auto issue = [&](int st) {
    const unsigned slot = l0 + (st & 3) * K1_STAGE;
#pragma unroll
    for (int i = 0; i < 4; i++) gldsn(xb + st * 128, xv[i], slot + (4 * lw + i) * 1024);
#pragma unroll
    for (int j = 0; j < 5; j++) gldsn(base[j] + (size_t)st * stride[j], voff[j], slot + K1_XT + j * 4096 + lw * 1024);
  };
  issue(0); issue(1); issue(2);
  WAITV(18);
  BARRIER();
#pragma unroll 1
  for (int st = 0; st < 10; st++) {
    if (st < 8) WAITV(9); else WAITV(0);
    BARRIER();
    if (st + 3 < 10) issue(st + 3);
  }
}
template <int ROLE>
DI void k1_body(const Params& p, char* lds_, int rb, int cg, int tt, const char* codep) {
  constexpr int NJ = ROLE == 0 ? 3 : 2, J0 = ROLE == 0 ? 0 : 3;
  const int lane = threadIdx.x & 63, r = lane & 31, h = lane >> 5;
  LAS char* lds = (LAS char*)lds_;
  const bool kv = ROLE == 1 && cg == 15;
  int stype[NJ], sidx[NJ];
#pragma unroll
  for (int j = 0; j < NJ; j++) k1_slot(cg, J0 + j, stype[j], sidx[j]);
#define K1_BPRIO 1
  if (ROLE == 1) __builtin_amdgcn_s_setprio(K1_BPRIO);
  unsigned junk = 0;
  if (ROLE == 0 && tt == 0) touch_lines(codep, CODE_PF_LINES, junk, lane);
  const int row = 32 * tt + r;
  unsigned xa[4], wa[4];
  const unsigned pitch = kv ? 40 : 64;
  const int blk = (lane >> 4) & 1, q = (lane & 15) >> 2, pp = lane & 3;
#pragma unroll
  for (int i = 0; i < 4; i++) {
    const unsigned ks = (i + tt) & 3;
    xa[i] = row * 128 + ((32 * ks + 16 * h) ^ (16 * ((row >> 1) & 7)));
    wa[i] = K1_XT + J0 * 4096 + (16 * ks + 8 * h + q) * pitch + 32 * blk + 8 * pp;
  }
  f32x16 acc[NJ];
#pragma unroll
  for (int j = 0; j < NJ; j++) acc[j] = f32x16{};
  f32x4 cs[NJ];
  float sq[4] = {0.f, 0.f, 0.f, 0.f}, sm[4] = {0.f, 0.f, 0.f, 0.f};
#pragma unroll
  for (int j = 0; j < NJ; j++) cs[j] = f32x4{};
  const unsigned ONES = 0x3F803F80u;
  const unsigned selw = ((lane & 15) == ((lane >> 4) & 1)) ? ONES : 0u;
  const u32x4 SEL = {selw, selw, selw, selw};
  auto mainloop = [&](auto KVC) {
    constexpr bool KV = decltype(KVC)::value;
    u32x4 xf[4] = {}, wf[4][NJ] = {};
    auto rd = [&](LAS char* sl, int i) {
      xf[i] = LDS128(sl + xa[i]);
#pragma unroll
      for (int j = 0; j < NJ; j++) wf[i][j] = ldtr(sl + wa[i], j * 4096, j * 4096 + (KV ? 160 : 256));
    };
    auto mm = [&](int i, LAS char* nsl, int ni) {
#pragma unroll
      for (int j = 0; j < NJ; j++) {
        if (KV && j == 1) acc[j] = MFMA32(xf[i], wf[i][j], acc[j]);
        else acc[j] = MFMA32(wf[i][j], xf[i], acc[j]);
        SB();
        if (j == 0) { xf[ni] = LDS128(nsl + xa[ni]); wf[ni][0] = ldtr(nsl + wa[ni], 0, KV ? 160 : 256); }
        else { wf[ni][j] = ldtr(nsl + wa[ni], j * 4096, j * 4096 + (KV ? 160 : 256)); }
        SB();
      }
      if (ROLE == 1) {
#pragma unroll
        for (int e = 0; e < 4; e++) { const float lo = bflo(xf[i][e]), hi = bfhi(xf[i][e]); sqacc(sq[(2 * e) & 3], lo); sqacc(sq[(2 * e + 1) & 3], hi); addacc(sm[e & 1], lo); addacc(sm[2 + (e & 1)], hi); }
        SB();
      }
    };
    BARRIER();
    rd(lds, 0); rd(lds, 1);
#pragma unroll 1
    for (int st = 0; st < 10; st++) {
      LAS char* sl = lds + (st & 3) * K1_STAGE;
      LAS char* sn = lds + ((st + 1) & 3) * K1_STAGE;
      mm(0, sl, 2);
      mm(1, sl, 3);
#pragma unroll
      for (int j = 0; j < NJ; j++) cs[j] = MFMA16(SEL, wf[0][j], cs[j]);
      SB();
      BARRIER();
      mm(2, sn, 0);
      mm(3, sn, 1);
      SB();
    }
  };
  if (kv) mainloop(std::true_type{}); else mainloop(std::false_type{});
  LAS float* csp = (LAS float*)(lds + K1_CSP);
  LAS float* csf = (LAS float*)(lds + K1_CSF);
  LAS f32x2* stt = (LAS f32x2*)(lds + K1_STP);
  if (lane < 16) {
#pragma unroll
    for (int j = 0; j < NJ; j++) { csp[tt * 160 + (J0 + j) * 32 + lane] = cs[j][0]; csp[tt * 160 + (J0 + j) * 32 + 16 + lane] = cs[j][1]; }
  }
  if (ROLE == 1) {
    const float sqs = half_sum((sq[0] + sq[1]) + (sq[2] + sq[3]));
    const float su = half_sum((sm[0] + sm[1]) + (sm[2] + sm[3]));
    const float mu = su * (1.f / 640.f), var = fmaxf(sqs * (1.f / 640.f) - mu * mu, 0.f), rs = __builtin_amdgcn_rsqf(var + 1e-5f);
    if (h == 0) stt[row] = f32x2{mu * rs, rs};
  }
  BARRIER();
  { const int c = threadIdx.x; if (c < 160) csf[c] = (csp[c] + csp[160 + c]) + (csp[320 + c] + csp[480 + c]); }
  const f32x2 st_ = stt[row];
  BARRIER();
  const int tb = rb * 4 + tt;
#pragma unroll
  for (int j = 0; j < NJ; j++) {
    const int type = stype[j];
    if (type == T_E) continue;
    if (ROLE == 1 && type == T_V) {
      const float csum = csf[(J0 + j) * 32 + r];
      char* vb = p.ws + WS_KV + (size_t)rb * KV_CHUNK + KV_VOFF + tt * 1536 + h * 384 + r * 16;
#pragma unroll
      for (int s = 0; s < 2; s++) {
        float v[8];
#pragma unroll
        for (int i = 0; i < 8; i++) { const f32x2 t = stt[32 * tt + 16 * s + 8 * (i >> 2) + 4 * h + (i & 3)]; v[i] = t[1] * acc[j][8 * s + i] - t[0] * csum; }
        u32x4 w = {cvtpk(v[0], v[1]), cvtpk(v[2], v[3]), cvtpk(v[4], v[5]), cvtpk(v[6], v[7])};
        if (r == 20) w = u32x4{ONES, ONES, ONES, ONES};
        if (r > 20) w = u32x4{0u, 0u, 0u, 0u};
        if (r < 24) *(u32x4*)(vb + s * 768) = w;
      }
      continue;
    }
    f32x2 v[8];
    const f32x2 rs2 = {st_[1], st_[1]}, nm2 = {-st_[0], -st_[0]};
#pragma unroll
    for (int g = 0; g < 4; g++) {
      const f32x4 c4 = *(LAS f32x4*)(csf + (J0 + j) * 32 + 8 * g + 4 * h);
      v[2 * g] = __builtin_elementwise_fma(f32x2{c4[0], c4[1]}, nm2, f32x2{acc[j][4 * g], acc[j][4 * g + 1]} * rs2);
      v[2 * g + 1] = __builtin_elementwise_fma(f32x2{c4[2], c4[3]}, nm2, f32x2{acc[j][4 * g + 2], acc[j][4 * g + 3]} * rs2);
    }
    if (ROLE == 1 && type == T_K) {
      char* kb = p.ws + WS_KV + (size_t)rb * KV_CHUNK + tt * 1536 + r * 16 + h * 8;
      *(u32x2*)(kb) = u32x2{cvtpk(v[0][0], v[0][1]), cvtpk(v[1][0], v[1][1])};
      *(u32x2*)(kb + 512) = u32x2{cvtpk(v[2][0], v[2][1]), cvtpk(v[3][0], v[3][1])};
      *(u32x2*)(kb + 1024) = h ? u32x2{0x00003F80u, 0u} : u32x2{cvtpk(v[4][0], v[4][1]), cvtpk(v[5][0], v[5][1])};
      continue;
    }
    unsigned off; char* base;
    if (type == T_Q) {
      base = p.ws + WS_QI; off = ((tb * 80 + sidx[j] * 4 + h) * 32 + r) * 16;
#pragma unroll
      for (int i = 0; i < 8; i++) v[i] *= f32x2{QSCALE, QSCALE};
    } else {
      base = p.ws + (type == T_Z1 ? WS_Z1 : WS_Z2R); off = ((tb * 108 + sidx[j] * 4 + h) * 32 + r) * 16;
#pragma unroll
      for (int i = 0; i < 8; i++) v[i] = gelu2(v[i]);
    }
#pragma unroll
    for (int g = 0; g < 4; g += 2)
      st16_wt(base, off + g * 512, pair8(u32x2{cvtpk(v[2 * g][0], v[2 * g][1]), cvtpk(v[2 * g + 1][0], v[2 * g + 1][1])}, u32x2{cvtpk(v[2 * g + 2][0], v[2 * g + 2][1]), cvtpk(v[2 * g + 3][0], v[2 * g + 3][1])}), 3538944);
  }
  asm volatile("" :: "v"(junk));
}
__global__ void __launch_bounds__(768, 3) k1_proj(Params p) {
  extern __shared__ __attribute__((aligned(16))) char lds[];
  const char* codep = code_ptr();
  const int b = blockIdx.x, x = b & 7, j = b >> 3;
  const int rb = (x & 1) * 8 + (j & 7), cg = (x >> 1) * 4 + (j >> 3);
  const int wid = RFL(threadIdx.x >> 6);
  if (b == 0 && threadIdx.x < 512) ((u32x4*)(p.ws + WS_FLAG))[threadIdx.x] = u32x4{0u, 0u, 0u, 0u};
  if (wid < 4) k1_body<0>(p, lds, rb, cg, wid, codep); else if (wid < 8) k1_body<1>(p, lds, rb, cg, wid - 4, codep); else k1_loader(p, lds, rb, cg, wid - 8);
}

typedef __attribute__((address_space(1))) unsigned gu32;
constexpr int K2_LDS = 81920;
constexpr float SM_OFF = 90.f;
#define EX(x) __builtin_amdgcn_exp2f(x)
#define AUNIT(Sc, Sn, qn0, qn1, Oc, Op, LD0, LD1, LD2, LD3) do { \
    Sc[0] = EX(Sc[0]); Sc[1] = EX(Sc[1]); SB(); \
    Sn = MFMA32(k0, qn0, zero16); SB(); LD0; SB(); \
    Sc[2] = EX(Sc[2]); Sc[3] = EX(Sc[3]); SB(); p1[2] = cvtpk(e12, e13); p1[3] = cvtpk(e14, e15); SB(); \
    Sc[4] = EX(Sc[4]); Sc[5] = EX(Sc[5]); SB(); \
    Op = MFMA32(v1, p1, Op); SB(); LD1; SB(); \
    Sc[6] = EX(Sc[6]); Sc[7] = EX(Sc[7]); SB(); p0[0] = cvtpk(Sc[0], Sc[1]); p0[1] = cvtpk(Sc[2], Sc[3]); SB(); \
    Sc[8] = EX(Sc[8]); Sc[9] = EX(Sc[9]); SB(); \
    Sn = MFMA32(k1, qn1, Sn); SB(); LD2; SB(); \
    Sc[10] = EX(Sc[10]); Sc[11] = EX(Sc[11]); SB(); p0[2] = cvtpk(Sc[4], Sc[5]); p0[3] = cvtpk(Sc[6], Sc[7]); SB(); \
    e12 = EX(Sc[12]); e13 = EX(Sc[13]); SB(); \
    Oc = MFMA32(v0, p0, Oc); SB(); LD3; SB(); \
    e14 = EX(Sc[14]); e15 = EX(Sc[15]); SB(); p1[0] = cvtpk(Sc[8], Sc[9]); p1[1] = cvtpk(Sc[10], Sc[11]); SB(); \
  } while (0)
#define ATILE(kn, v1c, v0n) do { \
    AUNIT(sA, sB, qB0, qB1, oA, oB, k0 = LDS128(kn), v1 = LDS128(v1c), k1 = LDS128((kn) + k1d), (void)0); \
    AUNIT(sB, sA, qA0, qA1, oB, oA, (void)0, (void)0, (void)0, v0 = LDS128(v0n)); \
  } while (0)
DI float max3f(float a, float b, float c) { return __builtin_fmaxf(__builtin_fmaxf(a, b), c); }
DI float max16(const f32x16& s) {
  const float a = max3f(max3f(s[0], s[1], s[2]), s[3], s[4]), b = max3f(max3f(s[5], s[6], s[7]), s[8], s[9]), c = max3f(max3f(s[10], s[11], s[12]), s[13], s[14]);
  return max3f(max3f(a, b, c), s[15], s[15]);
}
DI f32x16 attn_exact(const char* kv, u32x4 q0, u32x4 q1, int r, int h) {
  const f32x16 zero16 = {};
  q1[2] = 0u;
  f32x16 o = {}; float m = -3.0e38f;
  const int rr = r < 21 ? r : 21;
#pragma unroll 1
  for (int t = 0; t < 64; t++) {
    const char* cb = kv + (size_t)(t >> 2) * KV_CHUNK;
    const char* kb = cb + (t & 3) * 1536 + r * 16;
    const u32x4 k0 = *(const u32x4*)(kb + h * 512), k1 = *(const u32x4*)(kb + 1024);
    const char* vb = cb + KV_VOFF + (t & 3) * 1536 + h * 384 + rr * 16;
    const u32x4 v0 = *(const u32x4*)(vb), v1 = *(const u32x4*)(vb + 768);
    f32x16 s = MFMA32(k0, q0, zero16); s = MFMA32(k1, q1, s);
    float mx = s[0];
#pragma unroll
    for (int i = 1; i < 16; i++) mx = fmaxf(mx, s[i]);
    const float mn = fmaxf(m, half_max(mx)), a = EX(m - mn);
    m = mn;
#pragma unroll
    for (int i = 0; i < 16; i++) { o[i] *= a; s[i] = EX(s[i] - mn); }
    const u32x4 p0 = {cvtpk(s[0], s[1]), cvtpk(s[2], s[3]), cvtpk(s[4], s[5]), cvtpk(s[6], s[7])};
    const u32x4 p1 = {cvtpk(s[8], s[9]), cvtpk(s[10], s[11]), cvtpk(s[12], s[13]), cvtpk(s[14], s[15])};
    o = MFMA32(v0, p0, o); o = MFMA32(v1, p1, o);
  }
  return o;
}
DI void attn_role(const Params& p, char* lds_, int wg) {
  const int tid = threadIdx.x, lane = tid & 63, r = lane & 31, h = lane >> 5;
  const int wid = RFL(tid >> 6);
  const int pi = wid & 3, kh = wid >> 2;
  const int tb = wg >> 2, headA = (wg & 3) * 8 + 2 * pi;
  const unsigned l0 = (unsigned)(uintptr_t)lds_;
  LAS char* lds = (LAS char*)lds_;
  const char* kvg = p.ws + WS_KV;
  const char* kvsrc = kvg + (size_t)kh * 8 * KV_CHUNK + pi * 3072;
  const unsigned voff = lane * 16;
  const unsigned ring = l0 + kh * 3 * KV_CHUNK + pi * 3072;
#define APRIO 2
  __builtin_amdgcn_s_setprio(APRIO);
  glds3(kvsrc, voff, ring);
  u32x4 qA0, qA1, qB0, qB1;
  {
    const char* qrow = p.ws + WS_QI + (size_t)tb * 80 * 32 * 16;
    auto poff = [&](int c4) { return (unsigned)(r * 16 + (c4 >> 1) * 512 + (c4 & 1) * 8); };
    u32x2 a0, a1, a2, b0, b1, b2;
    const int cA = headA * 5, cB = cA + 5;
    asm volatile("global_load_dwordx2 %0, %6, %12\n\tglobal_load_dwordx2 %1, %7, %12\n\tglobal_load_dwordx2 %2, %8, %12\n\t"
                 "global_load_dwordx2 %3, %9, %12\n\tglobal_load_dwordx2 %4, %10, %12\n\tglobal_load_dwordx2 %5, %11, %12"
                 : "=&v"(a0), "=&v"(a1), "=&v"(a2), "=&v"(b0), "=&v"(b1), "=&v"(b2)
                 : "v"(poff(cA + 2 * h)), "v"(poff(cA + 2 * h + 1)), "v"(poff(cA + 4)), "v"(poff(cB + 2 * h)), "v"(poff(cB + 2 * h + 1)), "v"(poff(cB + 4)), "s"(qrow) : "memory");
    glds3(kvsrc + KV_CHUNK, voff, ring + KV_CHUNK);
    asm volatile("s_waitcnt vmcnt(3)\n\ts_barrier" : "+v"(a0), "+v"(a1), "+v"(a2), "+v"(b0), "+v"(b1), "+v"(b2) :: "memory");
    qA0 = u32x4{a0[0], a0[1], a1[0], a1[1]}; qB0 = u32x4{b0[0], b0[1], b1[0], b1[1]};
    qA1 = h ? u32x4{0u, 0u, 0u, 0u} : u32x4{a2[0], a2[1], 0u, 0u};
    qB1 = h ? u32x4{0u, 0u, 0u, 0u} : u32x4{b2[0], b2[1], 0u, 0u};
  }
  const f32x16 zero16 = {};
  const int k1d = 1024 - h * 512;
  const int rr = r < 21 ? r : 21;
  u32x4 k0, k1; f32x16 sA, sB;
  {
    LAS char* kb0 = lds + h * 512 + r * 16;
    k0 = LDS128(kb0); k1 = LDS128(kb0 + k1d);
    sA = MFMA32(k0, qA0, zero16); sA = MFMA32(k1, qA1, sA); sB = MFMA32(k0, qB0, zero16); sB = MFMA32(k1, qB1, sB);
    float mA = max16(sA), mB = max16(sB);
    k0 = LDS128(kb0 + 1536); k1 = LDS128(kb0 + 1536 + k1d);
    sA = MFMA32(k0, qA0, zero16); sA = MFMA32(k1, qA1, sA); sB = MFMA32(k0, qB0, zero16); sB = MFMA32(k1, qB1, sB);
    mA = half_max(fmaxf(mA, max16(sA))); mB = half_max(fmaxf(mB, max16(sB)));
    if (h == 0) { qA1[2] = cvtpk(-(mA + SM_OFF), 0.f); qB1[2] = cvtpk(-(mB + SM_OFF), 0.f); }
  }
  LAS char* kbase = lds + kh * 3 * KV_CHUNK + h * 512 + r * 16;
  LAS char* vbase = lds + kh * 3 * KV_CHUNK + KV_VOFF + h * 384 + rr * 16;
  k0 = LDS128(kbase); k1 = LDS128(kbase + k1d);
  sA = MFMA32(k0, qA0, zero16); sA = MFMA32(k1, qA1, sA);
  u32x4 v0 = LDS128(vbase), v1 = v0, p0 = {0u, 0u, 0u, 0u}, p1 = {0u, 0u, 0u, 0u};
  f32x16 oA = {}, oB = {};
  float e12 = 0.f, e13 = 0.f, e14 = 0.f, e15 = 0.f;
  asm volatile("s_waitcnt vmcnt(0) lgkmcnt(0)\n\ts_barrier" ::: "memory");
  int sc = 0;
#pragma unroll 1
  for (int c = 0; c < 8; c++) {
    const int sn = sc == 2 * KV_CHUNK ? 0 : sc + KV_CHUNK, sp = sn == 2 * KV_CHUNK ? 0 : sn + KV_CHUNK;
    if (c + 2 < 8) glds3(kvsrc + (size_t)(c + 2) * KV_CHUNK, voff, ring + sp);
    LAS char* kc = kbase + sc; LAS char* kn = kbase + sn; LAS char* vc = vbase + sc; LAS char* vn = vbase + sn;
    ATILE(kc + 1 * 1536, vc + 1 * 768, vc + 2 * 768);
    ATILE(kc + 2 * 1536, vc + 3 * 768, vc + 4 * 768);
    ATILE(kc + 3 * 1536, vc + 5 * 768, vc + 6 * 768);
    ATILE(kn, vc + 7 * 768, vn);
    asm volatile("s_waitcnt vmcnt(0) lgkmcnt(0)\n\ts_barrier" ::: "memory");
    sc = sn;
  }
  p1[2] = cvtpk(e12, e13); p1[3] = cvtpk(e14, e15);
  oB = MFMA32(v1, p1, oB);
  LAS f32x4* ex = (LAS f32x4*)(lds + (pi * 2 + kh) * 3072 + lane * 16);
  LAS const f32x4* exr = (LAS const f32x4*)(lds + (pi * 2 + (kh ^ 1)) * 3072 + lane * 16);
  f32x16 o;
  if (kh == 0) { o = oA;
#pragma unroll
    for (int g = 0; g < 3; g++) ex[g * 64] = f32x4{oB[4 * g], oB[4 * g + 1], oB[4 * g + 2], oB[4 * g + 3]};
  } else { o = oB;
#pragma unroll
    for (int g = 0; g < 3; g++) ex[g * 64] = f32x4{oA[4 * g], oA[4 * g + 1], oA[4 * g + 2], oA[4 * g + 3]};
  }
  BARRIER();
#pragma unroll
  for (int g = 0; g < 3; g++) { f32x4 t = exr[g * 64]; o[4 * g] += t[0]; o[4 * g + 1] += t[1]; o[4 * g + 2] += t[2]; o[4 * g + 3] += t[3]; }
  {
    float big = 0.f;
#pragma unroll
    for (int i = 0; i < 12; i++) big = fmaxf(big, fabsf(o[i]));
    float chk = 0.f;
#pragma unroll
    for (int i = 0; i < 12; i++) chk += o[i] * 0.f;
    const bool bad = !(big < 1.0e36f) || !(chk == 0.f);
    if (__builtin_expect(__any(bad), 0)) o = attn_exact(kvg, kh ? qB0 : qA0, kh ? qB1 : qA1, r, h);
  }
  auto lr = __builtin_amdgcn_permlane32_swap(__float_as_uint(o[8]), __float_as_uint(o[8]), false, false);
  const float il = __builtin_amdgcn_rcpf(__uint_as_float(lr[1]));
  LAS char* ost = lds + 32768;
  const int hl = 2 * pi + kh;
#pragma unroll
  for (int g = 0; g < 3; g++) {
    if (g < 2 || h == 0) {
      const int kk = hl * 20 + (2 * g + h) * 4;
      *(LAS u32x2*)(ost + ((kk >> 3) * 32 + r) * 16 + ((kk >> 2) & 1) * 8) = u32x2{cvtpk(o[4 * g] * il, o[4 * g + 1] * il), cvtpk(o[4 * g + 2] * il, o[4 * g + 3] * il)};
    }
  }
  BARRIER();
#pragma unroll
  for (int k = 0; k < 2; k++) {
    const int i = tid + 512 * k;
    if (i < 640) st16_wt(p.ws + WS_AO, (((wg & 3) * 20 + (i >> 5)) * 2048 + tb * 32 + (i & 31)) * 16, *(LAS u32x4*)(ost + i * 16), 6160384);
  }
  asm volatile("s_waitcnt vmcnt(0)" ::: "memory");
  BARRIER();
  if (tid == 0) __hip_atomic_fetch_add((gu32*)(p.ws + WS_FLAG) + 1024 + (tb >> 1) * 16, 1u, __ATOMIC_RELAXED, __HIP_MEMORY_SCOPE_AGENT);
}

DI int fb_start(int fb) { return (fb >> 2) * 216 + (fb & 3) * 56; }
constexpr int SP_STAGE = 24576, SP_ZOFF = 16384;

constexpr int F_ACT = 8192, F_STAGE = F_ACT + 10240;
DI void fin_role(const Params& p, char* lds_, int b, int wid) {
  const int tid = threadIdx.x, lane = tid & 63, r = lane & 31, h = lane >> 5;
  const unsigned l0 = (unsigned)(uintptr_t)lds_;
  LAS char* lds = (LAS char*)lds_;
  const int x = b & 7, jj = b >> 3;
  const int tbk = (x >> 1) * 4 + (jj & 3), fb = (x & 1) * 8 + (jj >> 2);
  const int tb = 2 * tbk + (fb >> 3), nb = fb & 7;
  gu32* cnt = (gu32*)(p.ws + WS_FLAG);
  BARRIER();
  if (tid == 0) __hip_atomic_fetch_add(cnt + 512 + tbk * 16, 1u, __ATOMIC_RELAXED, __HIP_MEMORY_SCOPE_AGENT);
  if (wid >= 4) {
    const int lw = wid - 4;
    const char* ab = p.ws + WS_AO + (size_t)tb * 64 * 16;
    const char* wb = p.ws + WS_W3 + (size_t)nb * 94 * 2560;
    const unsigned lv = lane * 16;
    auto ks0 = [&](int s) { return s < 14 ? 40 + 4 * s : 4 * (s - 14); };
    auto issue_w = [&](int s) {
      const int p0 = lw < 2 ? 3 * lw : 2 + 2 * lw;
      const char* src = wb + (size_t)ks0(s) * 2560;
      const unsigned dst = l0 + (s & 3) * F_STAGE + F_ACT;
#pragma unroll
      for (int i = 0; i < 3; i++) { const int pc = (lw >= 2 && i == 2) ? p0 : p0 + i; gldsn_dev(src + (s == 13 && pc >= 5 ? 0 : pc * 1024), lv, dst + pc * 1024); }
    };
    auto issue_a = [&](int s) {
#pragma unroll
      for (int i = 0; i < 2; i++) gldsn_dev(ab + (size_t)(2 * ks0(s) + (s == 13 && lw >= 2 ? 0 : 2 * lw) + i) * 32768, lv, l0 + (s & 3) * F_STAGE + (2 * lw + i) * 1024);
    };
    auto await = [&](gu32* word, unsigned want) {
      unsigned spins = 0;
      while ((unsigned)RFL(__hip_atomic_load(word, __ATOMIC_RELAXED, __HIP_MEMORY_SCOPE_AGENT)) < want) { if (++spins > (1u << 21)) break; __builtin_amdgcn_s_sleep(4); }
      asm volatile("" ::: "memory");
    };
    await(cnt + 1536 + nb * 16, 94u);
    issue_w(0); issue_w(1); issue_w(2);
    await(cnt + 512 + tbk * 16, 16u);
    issue_a(0); issue_a(1); issue_a(2);
#pragma unroll 1
    for (int s = 0; s < 24; s++) {
      if (s == 0 || s == 14) WAITV(4); else if (s == 1 || s == 15) WAITV(7); else if (s == 12) WAITV(8); else if (s == 13) WAITV(6);
      else if (s == 22) WAITV(5); else if (s == 23) WAITV(0); else WAITV(10);
      BARRIER();
      if (s + 3 < 24) {
        if (s < 11 || s > 13) issue_a(s + 3);
        issue_w(s + 3);
      }
      if (s == 13) { await(cnt + 1024 + tb * 16, 8u); issue_a(14); issue_a(15); issue_a(16); }
    }
    return;
  }
  __builtin_amdgcn_s_setprio(1);
  const int tt = wid & 1, two = wid < 2;
  u32x4 xr[3];
#pragma unroll
  for (int k = 0; k < 3; k++) { const int i = tid + 256 * k < 640 ? tid + 256 * k : 0, row = i / 10, c = i - row * 10;
    xr[k] = *(const u32x4*)(p.x + ((size_t)(tb * 64 + row) * 640 + nb * 80 + c * 8) * 2); }
  const unsigned wa = F_ACT + (two ? lane : 128 + h * 16 + (r & 15)) * 16;
  const unsigned aa = h * 1024 + (32 * tt + r) * 16;
  f32x16 acc = {}, acc2 = {};
#pragma unroll 1
  for (int s = 0; s < 24; s++) {
    BARRIER();
    LAS char* sl = lds + (s & 3) * F_STAGE;
    u32x4 af[4], wf[4], wg[4];
#pragma unroll
    for (int ks = 0; ks < 4; ks++) { af[ks] = LDS128(sl + aa + ks * 2048); wf[ks] = LDS128(sl + wa + ks * 2560); }
    if (two) {
#pragma unroll
      for (int ks = 0; ks < 4; ks++) wg[ks] = LDS128(sl + wa + 1024 + ks * 2560);
      acc = MFMA32(wf[0], af[0], acc); acc2 = MFMA32(wg[0], af[0], acc2); acc = MFMA32(wf[1], af[1], acc); acc2 = MFMA32(wg[1], af[1], acc2);
      if (s != 13) { acc = MFMA32(wf[2], af[2], acc); acc2 = MFMA32(wg[2], af[2], acc2); acc = MFMA32(wf[3], af[3], acc); acc2 = MFMA32(wg[3], af[3], acc2); }
    } else {
      acc = MFMA32(wf[0], af[0], acc); acc = MFMA32(wf[1], af[1], acc);
      if (s != 13) { acc = MFMA32(wf[2], af[2], acc); acc = MFMA32(wf[3], af[3], acc); }
    }
  }
  BARRIER();
  LAS float* ot = (LAS float*)lds;
  constexpr int OP = 84;
  const int n0 = two ? 0 : 64;
#pragma unroll
  for (int g = 0; g < 4; g++) {
    const int n = n0 + 8 * g + 4 * h;
    if (n < 80) *(LAS f32x4*)(ot + (32 * tt + r) * OP + n) = f32x4{acc[4 * g], acc[4 * g + 1], acc[4 * g + 2], acc[4 * g + 3]};
    if (two) *(LAS f32x4*)(ot + (32 * tt + r) * OP + 32 + n) = f32x4{acc2[4 * g], acc2[4 * g + 1], acc2[4 * g + 2], acc2[4 * g + 3]};
  }
  BARRIER();
#pragma unroll
  for (int k = 0; k < 3; k++) {
    const int i = tid + 256 * k;
    if (i < 640) {
      const int row = i / 10, c = i - row * 10;
      const size_t go = ((size_t)(tb * 64 + row) * 640 + nb * 80 + c * 8) * 2;
      const f32x4 a = *(LAS f32x4*)(ot + row * OP + c * 8), a2 = *(LAS f32x4*)(ot + row * OP + c * 8 + 4);
      const u32x4 w = {cvtpk(a[0] + bflo(xr[k][0]), a[1] + bfhi(xr[k][0])), cvtpk(a[2] + bflo(xr[k][1]), a[3] + bfhi(xr[k][1])),
                       cvtpk(a2[0] + bflo(xr[k][2]), a2[1] + bfhi(xr[k][2])), cvtpk(a2[2] + bflo(xr[k][3]), a2[3] + bfhi(xr[k][3]))};
      st16_wt(p.out, (unsigned)go, w, 2621440);
    }
  }
}
DI void sp_role(const Params& p, char* lds_, int b) {
  const int wid = RFL(threadIdx.x >> 6);
  __builtin_amdgcn_s_setprio(3);
  const int tid = threadIdx.x, lane = tid & 63, r = lane & 31, h = lane >> 5;
  const unsigned l0 = (unsigned)(uintptr_t)lds_;
  LAS char* lds = (LAS char*)lds_;
  const int x = b & 7, jj = b >> 3;
  const int tbk = (x >> 1) * 4 + (jj & 3), fb = (x & 1) * 8 + (jj >> 2);
  gu32* cnt = (gu32*)(p.ws + WS_FLAG);
  if (wid >= 4) {
    const int lw = wid - 4;
    const char* wb = p.wsp + (size_t)tbk * 128 * 4096;
    unsigned wv4[4];
#pragma unroll
    for (int i = 0; i < 4; i++) { const unsigned row = 8 * (4 * lw + i) + (lane >> 3); wv4[i] = row * 4096 + (((lane & 7) ^ ((row >> 1) & 7)) << 4); }
    const char* zb = p.ws + WS_Z2N + (size_t)fb * 32 * 8192 + lw * 2048;
    const unsigned zv = lane * 16;
    auto issue_w = [&](int st) {
#pragma unroll
      for (int i = 0; i < 4; i++) gldsn(wb + st * 128, wv4[i], l0 + (st % 3) * SP_STAGE + (4 * lw + i) * 1024);
    };
    auto issue_z = [&](int st) {
      asm volatile("s_mov_b32 m0, %2\n\ts_nop 0\n\tglobal_load_lds_dwordx4 %0, %1 sc1\n\tglobal_load_lds_dwordx4 %0, %1 offset:1024 sc1"
                   :: "v"(zv), "s"(zb + (size_t)st * 8192), "s"(l0 + (st % 3) * SP_STAGE + SP_ZOFF + lw * 2048) : "memory");
    };
    BARRIER(); BARRIER();
    issue_w(0); issue_w(1);
    BARRIER();
    {
      const int u = b * 3 + lw - 1;
      if (lw > 0 && u < 752) {
        const int ks = u >> 3, nb = u & 7;
        const uintptr_t pwo = (uintptr_t)p.wo, pwout = (uintptr_t)p.wout;
        const GAS char* src = (const GAS char*)(ks < 40 ? pwo + (size_t)ks * 16 * 1280 : pwout + (size_t)(ks - 40) * 16 * 1280) + nb * 160;
        LAS char* sc = lds + 3 * SP_STAGE + (lw - 1) * 2560;
#pragma unroll
        for (int i = 0; i < 3; i++) {
          const int c = lane + 64 * i, rw = c / 10, cc = c - rw * 10;
          if (c < 160) *(LAS u32x4*)(sc + c * 16) = *(const GAS u32x4*)(src + rw * 1280 + cc * 16);
        }
        asm volatile("s_waitcnt vmcnt(0) lgkmcnt(0)" ::: "memory");
        LAS char* ta = sc + (8 * h + ((lane & 15) >> 2)) * 160 + 32 * ((lane >> 4) & 1) + 8 * (lane & 3);
        const unsigned dst = (nb * 94 + ks) * 2560;
        const u32x4 f0 = ldtr(ta, 0, 640), f1 = ldtr(ta, 64, 64 + 640), f2 = ldtr(ta, 128, 128 + 640);
        st16_wt(p.ws + WS_W3, dst + lane * 16, f0, 8 * 94 * 2560);
        st16_wt(p.ws + WS_W3, dst + 1024 + lane * 16, f1, 8 * 94 * 2560);
        if (r < 16) st16_wt(p.ws + WS_W3, dst + 2048 + (h * 16 + r) * 16, f2, 8 * 94 * 2560);
        asm volatile("s_waitcnt vmcnt(0) lgkmcnt(0)" ::: "memory");
        if (lane == 0) __hip_atomic_fetch_add(cnt + 1536 + nb * 16, 1u, __ATOMIC_RELAXED, __HIP_MEMORY_SCOPE_AGENT);
      }
    }
    if (lw > 0) {
      const char* src = p.wsp + (unsigned)RFL(b * 32768 + (lw - 1) * 12288);
      const int n2 = lw == 3 ? 4 : 6;
      for (int i = 0; i < n2; i++)
        asm volatile("s_mov_b32 m0, %2\n\ts_nop 0\n\tglobal_load_lds_dwordx4 %0, %1\n\tglobal_load_lds_dwordx4 %0, %1 offset:1024"
                     :: "v"(zv), "s"(src + i * 2048), "s"(l0 + 3 * SP_STAGE - 2048) : "memory");
    }
    if (lw == 0) {
      {
      __builtin_amdgcn_s_sleep(24);
      unsigned spins = 0;
      for (;;) {
        const unsigned c = __hip_atomic_load(cnt + (lane & 31) * 16, __ATOMIC_RELAXED, __HIP_MEMORY_SCOPE_AGENT);
        if (__all(c >= 8u)) break;
        if (++spins > (1u << 21)) break;
        __builtin_amdgcn_s_sleep(6);
      }
      }
      const char* z00 = p.ws + WS_Z2N + (size_t)fb * 32 * 8192;
#pragma unroll
      for (int i = 0; i < 4; i++)
        asm volatile("s_mov_b32 m0, %2\n\ts_nop 0\n\tglobal_load_lds_dwordx4 %0, %1 sc1\n\tglobal_load_lds_dwordx4 %0, %1 offset:1024 sc1\n\tglobal_load_lds_dwordx4 %0, %1 offset:2048 sc1\n\tglobal_load_lds_dwordx4 %0, %1 offset:3072 sc1"
                     :: "v"(zv), "s"(z00 + i * 4096), "s"(l0 + (i >> 1) * SP_STAGE + SP_ZOFF + (i & 1) * 4096) : "memory");
      WAITV(8);
    }
#pragma unroll 1
    for (int st = 0; st < 32; st++) {
      if (st == 0) { if (lw) WAITV(0); } else if (st < 31) WAITV(6); else WAITV(0);
      BARRIER();
      if (st + 2 < 32) { issue_w(st + 2); issue_z(st + 2); }
    }
    fin_role(p, lds_, b, wid);
    return;
  }
  {
    LAS float* red = (LAS float*)(lds + K2_LDS - 256);
    const bool act = tid < 216;
    const int g8 = act ? tid >> 1 : 107, half = tid & 1;
    const char* src = p.ws + WS_Z2R + ((size_t)((b >> 2) * 108 + g8) * 32 + (b & 3) * 8 + 4 * half) * 16;
    u32x4 raw[4];
#pragma unroll
    for (int i = 0; i < 4; i++) raw[i] = *(const u32x4*)(src + i * 16);
    const unsigned ONES = act ? 0x3F803F80u : 0u;
    LAS float* part = (LAS float*)(lds + 2 * SP_STAGE);
#pragma unroll
    for (int t = 0; t < 4; t++) {
      float su = 0.f, sq = 0.f;
#pragma unroll
      for (int e = 0; e < 4; e++) { const unsigned a = act ? raw[t][e] : 0u; su = dot2(a, ONES, su); sq = dot2(a, a, sq); }
      part[(2 * (4 * half + t)) * 128 + (tid >> 1)] = su;
      part[(2 * (4 * half + t) + 1) * 128 + (tid >> 1)] = sq;
    }
    BARRIER();
    {
      float s4[4];
#pragma unroll
      for (int k = 0; k < 4; k++) { const f32x2 a = *(LAS f32x2*)(part + (4 * wid + k) * 128 + lane * 2); s4[k] = a[0] + a[1]; }
#pragma unroll
      for (int k = 0; k < 4; k++) s4[k] = wave_sum(s4[k]);
      if (lane < 2) {
        const float su = lane ? s4[2] : s4[0], sq = lane ? s4[3] : s4[1];
        const float mu = su * (1.f / 864.f), var = fmaxf(sq * (1.f / 864.f) - mu * mu, 0.f), rs = __builtin_amdgcn_rsqf(var + 1e-5f);
        ((LAS f32x2*)red)[2 * wid + lane] = f32x2{rs, -mu * rs};
      }
    }
    BARRIER();
    LAS char* tr = lds + 2 * SP_STAGE + 8192;
    {
      float v[4][8];
#pragma unroll
      for (int t = 0; t < 4; t++) {
        const f32x2 c = ((LAS f32x2*)red)[4 * half + t];
#pragma unroll
        for (int e = 0; e < 4; e++) { v[t][2 * e] = __builtin_fmaf(bflo(raw[t][e]), c[0], c[1]); v[t][2 * e + 1] = __builtin_fmaf(bfhi(raw[t][e]), c[0], c[1]); }
      }
      if (act) {
#pragma unroll
        for (int i = 0; i < 8; i++) *(LAS u32x2*)(tr + (8 * g8 + i) * 16 + half * 8) = u32x2{cvtpk(v[0][i], v[1][i]), cvtpk(v[2][i], v[3][i])};
      }
    }
    asm volatile("s_waitcnt lgkmcnt(0)" ::: "memory");
    {
      const __amdgpu_buffer_rsrc_t rsrc = __builtin_amdgcn_make_buffer_rsrc((void*)(p.ws + WS_Z2N), 0, 4194304, 0x00020000);
      const unsigned sbase = (((b >> 3) * 4 + ((b >> 1) & 3)) * 2 + (b & 1)) * 1024;
#pragma unroll
      for (int i = 0; i < 4; i++) {
        const int f = 256 * wid + 64 * i + lane;
        if (f < 864) {
          const u32x4 w = *(LAS u32x4*)(tr + f * 16);
          const int q4 = (f >= 216) + (f >= 432) + (f >= 648), rem = f - q4 * 216, sub = (rem >= 56) + (rem >= 112) + (rem >= 168);
          const unsigned off = (unsigned)(q4 * 4 + sub) * (32u * 8192u) + sbase + (rem - sub * 56) * 16;
          __builtin_amdgcn_raw_buffer_store_b128(w, rsrc, off, 0, 16);
        }
      }
    }
    asm volatile("s_waitcnt vmcnt(0)" ::: "memory");
    BARRIER();
    if (tid == 0) __hip_atomic_fetch_add(cnt + (b >> 3) * 16, 1u, __ATOMIC_RELAXED, __HIP_MEMORY_SCOPE_AGENT);
  }
  const unsigned row = 32 * wid + r;
  unsigned wa[4];
#pragma unroll
  for (int ks = 0; ks < 4; ks++) wa[ks] = row * 128 + ((32 * ks + 16 * h) ^ (16 * ((row >> 1) & 7)));
  const unsigned za = SP_ZOFF + h * 1024 + r * 16;
  f32x16 acc0 = {}, acc1 = {};
#pragma unroll 1
  for (int st = 0; st < 32; st++) {
    BARRIER();
    LAS char* sl = lds + (st % 3) * SP_STAGE;
    u32x4 wf[4], z0[4], z1[4];
#pragma unroll
    for (int ks = 0; ks < 4; ks++) { wf[ks] = LDS128(sl + wa[ks]); z0[ks] = LDS128(sl + za + ks * 2048); z1[ks] = LDS128(sl + za + ks * 2048 + 512); }
#pragma unroll
    for (int ks = 0; ks < 4; ks++) { acc0 = MFMA32(z0[ks], wf[ks], acc0); acc1 = MFMA32(z1[ks], wf[ks], acc1); }
  }
  const int tok = tbk * 128 + row, f0 = fb_start(fb), width = (fb & 3) == 3 ? 48 : 56;
  const float bias = bflo(*(const unsigned short*)(p.bsp + tok * 2));
  const char* z1b = p.ws + WS_Z1 + ((size_t)((tok >> 5) * 108 + (f0 >> 3)) * 32 + r) * 16 + h * 8;
#pragma unroll
  for (int j = 0; j < 2; j++) {
    const f32x16& a = j ? acc1 : acc0;
    u32x2 w[4];
#pragma unroll
    for (int g = 0; g < 4; g++) {
      const int fl = 32 * j + 8 * g;
      u32x2 z = {0u, 0u};
      if (fl < width) z = *(const u32x2*)(z1b + (4 * j + g) * 512);
      w[g] = u32x2{cvtpk(bflo(z[0]) * (a[4 * g] + bias), bfhi(z[0]) * (a[4 * g + 1] + bias)), cvtpk(bflo(z[1]) * (a[4 * g + 2] + bias), bfhi(z[1]) * (a[4 * g + 3] + bias))};
    }
#pragma unroll
    for (int g = 0; g < 4; g += 2) {
      const u32x4 w8 = pair8(w[g], w[g + 1]);
      const int fl = 32 * j + 8 * (g + h);
      if (fl < width) st16_wt(p.ws + WS_AO, ((80 + ((f0 + fl) >> 3)) * 2048 + tok) * 16, w8, 6160384);
    }
  }
  asm volatile("s_waitcnt vmcnt(0)" ::: "memory");
  fin_role(p, lds_, b, wid);
}
__global__ void __launch_bounds__(512, 4) k2_mix(Params p) {
  extern __shared__ __attribute__((aligned(16))) char lds[];
  if (blockIdx.x >= 256) sp_role(p, lds, blockIdx.x - 256); else attn_role(p, lds, blockIdx.x);
}

extern "C" void kernel_launch(void* const* d_in, const int* in_sizes, int n_in, void* d_out, int out_size, void* d_ws, size_t ws_size, hipStream_t stream) {
  Params p;
  p.x = (const char*)d_in[0]; p.wq = (const char*)d_in[1]; p.wk = (const char*)d_in[2]; p.wv = (const char*)d_in[3]; p.wo = (const char*)d_in[4];
  p.win = (const char*)d_in[6]; p.wsp = (const char*)d_in[7]; p.bsp = (const char*)d_in[8]; p.wout = (const char*)d_in[9];
  p.ws = (char*)d_ws; p.out = (char*)d_out;
  (void)hipFuncSetAttribute((const void*)k1_proj, hipFuncAttributeMaxDynamicSharedMemorySize, K1_LDS);
  (void)hipFuncSetAttribute((const void*)k2_mix, hipFuncAttributeMaxDynamicSharedMemorySize, K2_LDS);
  k1_proj<<<256, 768, K1_LDS, stream>>>(p);
  k2_mix<<<512, 512, K2_LDS, stream>>>(p);
}
```

```cpp
#include <hip/hip_runtime.h>
#include <stdint.h>
#include <type_traits>
typedef __bf16 bf16x8 __attribute__((ext_vector_type(8)));
typedef __bf16 bf16x2 __attribute__((ext_vector_type(2)));
typedef short s16x4 __attribute__((ext_vector_type(4)));
typedef float f32x16 __attribute__((ext_vector_type(16)));
typedef float f32x4 __attribute__((ext_vector_type(4)));
typedef float f32x2 __attribute__((ext_vector_type(2)));
typedef unsigned u32x4 __attribute__((ext_vector_type(4)));
typedef unsigned u32x2 __attribute__((ext_vector_type(2)));
#define DI __device__ __forceinline__
#define LAS __attribute__((address_space(3)))
#define GAS __attribute__((address_space(1)))
#define SB() __builtin_amdgcn_sched_barrier(0)
#define RFL(x) __builtin_amdgcn_readfirstlane(x)
#define MFMA32(a, b, c) __builtin_amdgcn_mfma_f32_32x32x16_bf16(__builtin_bit_cast(bf16x8, a), __builtin_bit_cast(bf16x8, b), c, 0, 0, 0)
#define MFMA16(a, b, c) __builtin_amdgcn_mfma_f32_16x16x32_bf16(__builtin_bit_cast(bf16x8, a), __builtin_bit_cast(bf16x8, b), c, 0, 0, 0)
DI f32x4 mfma4sq(unsigned x0, unsigned x1, f32x4 c) { u32x2 v = {x0, x1}; s16x4 a = __builtin_bit_cast(s16x4, v); return __builtin_amdgcn_mfma_f32_4x4x4bf16_1k(a, a, c, 0, 0, 0); }
#define MFMA4(x0, x1, c) mfma4sq(x0, x1, c)
#define LDS128(p) (*(const LAS u32x4*)(p))
DI unsigned cvtpk(float lo, float hi) { f32x2 v = {lo, hi}; bf16x2 b = __builtin_convertvector(v, bf16x2); return __builtin_bit_cast(unsigned, b); }
DI float bflo(unsigned u) { return __uint_as_float(u << 16); }
DI float bfhi(unsigned u) { return __uint_as_float(u & 0xffff0000u); }
DI void sqacc(float& a, float x) { asm("v_fmac_f32_e32 %0, %1, %1" : "+v"(a) : "v"(x)); }
DI void addacc(float& a, float x) { asm("v_add_f32_e32 %0, %0, %1" : "+v"(a) : "v"(x)); }
DI float dot2(unsigned a, unsigned b, float c) { return __builtin_amdgcn_fdot2_f32_bf16(__builtin_bit_cast(bf16x2, a), __builtin_bit_cast(bf16x2, b), c, false); }
DI float half_sum(float m) { auto r = __builtin_amdgcn_permlane32_swap(__float_as_uint(m), __float_as_uint(m), false, false); return __uint_as_float(r[0]) + __uint_as_float(r[1]); }
DI float half_max(float m) { auto r = __builtin_amdgcn_permlane32_swap(__float_as_uint(m), __float_as_uint(m), false, false); return fmaxf(__uint_as_float(r[0]), __uint_as_float(r[1])); }
template <int CTRL, int RMASK> DI float dpp_add(float v) { return v + __uint_as_float(__builtin_amdgcn_update_dpp(0u, __float_as_uint(v), CTRL, RMASK, 0xf, true)); }
DI float wave_sum(float v) {
  v = dpp_add<0x111, 0xf>(v); v = dpp_add<0x112, 0xf>(v); v = dpp_add<0x114, 0xf>(v); v = dpp_add<0x118, 0xf>(v);
  v = dpp_add<0x142, 0xa>(v); v = dpp_add<0x143, 0xc>(v);
  return __uint_as_float(__builtin_amdgcn_readlane(__float_as_uint(v), 63));
}
DI void glds1(const void* sbase, unsigned voff, unsigned lds_base) {
  unsigned sv;
  asm volatile("s_mov_b32 %0, m0\n\ts_mov_b32 m0, %3\n\ts_nop 0\n\tglobal_load_lds_dwordx4 %1, %2\n\ts_mov_b32 m0, %0"
               : "=&s"(sv) : "v"(voff), "s"(sbase), "s"(lds_base) : "memory"); }
DI void glds3(const void* sbase, unsigned voff, unsigned lds_base) {
  unsigned sv;
  asm volatile("s_mov_b32 %0, m0\n\ts_mov_b32 m0, %3\n\ts_nop 0\n\tglobal_load_lds_dwordx4 %1, %2\n\tglobal_load_lds_dwordx4 %1, %2 offset:1024\n\tglobal_load_lds_dwordx4 %1, %2 offset:2048\n\ts_mov_b32 m0, %0"
               : "=&s"(sv) : "v"(voff), "s"(sbase), "s"(lds_base) : "memory"); }
DI void gldsn(const void* sbase, unsigned voff, unsigned lds_base) {
  asm volatile("s_mov_b32 m0, %2\n\ts_nop 0\n\tglobal_load_lds_dwordx4 %0, %1" :: "v"(voff), "s"(sbase), "s"(lds_base) : "memory"); }
DI void gldsn_dev(const void* sbase, unsigned voff, unsigned lds_base) {
  asm volatile("s_mov_b32 m0, %2\n\ts_nop 0\n\tglobal_load_lds_dwordx4 %0, %1 sc1" :: "v"(voff), "s"(sbase), "s"(lds_base) : "memory"); }
DI u32x4 ldtr(const LAS char* p, int off0, int off1) {
  typedef LAS s16x4* lp;
  s16x4 a = __builtin_amdgcn_ds_read_tr16_b64_v4i16((lp)(p + off0)), b = __builtin_amdgcn_ds_read_tr16_b64_v4i16((lp)(p + off1));
  u32x2 x = __builtin_bit_cast(u32x2, a), y = __builtin_bit_cast(u32x2, b);
  return u32x4{x[0], x[1], y[0], y[1]};
}
DI void st16_wt(char* base, unsigned off, u32x4 w, unsigned bytes) {
  const __amdgpu_buffer_rsrc_t rs = __builtin_amdgcn_make_buffer_rsrc((void*)base, 0, bytes, 0x00020000);
  __builtin_amdgcn_raw_buffer_store_b128(w, rs, off, 0, 16);
}
DI u32x4 pair8(u32x2 a, u32x2 b) {
  auto s0 = __builtin_amdgcn_permlane32_swap(a[0], b[0], false, false);
  auto s1 = __builtin_amdgcn_permlane32_swap(a[1], b[1], false, false);
  return u32x4{s0[0], s1[0], s0[1], s1[1]};
}
DI void touch_lines(const void* base, int lines, unsigned& junk, int lane) {
  for (int l0 = 0; l0 < lines; l0 += 64) { const int l = l0 + lane < lines ? l0 + lane : lines - 1;
    asm volatile("global_load_dword %0, %1, %2" : "+v"(junk) : "v"((unsigned)l * 128u), "s"(base) : "memory"); }
}
#define WAITV(n) asm volatile("s_waitcnt vmcnt(" #n ")" ::: "memory")
#define BARRIER() asm volatile("s_waitcnt lgkmcnt(0)\n\ts_barrier" ::: "memory")

constexpr int SEQ = 2048, DM = 640, NFH = 864;
constexpr size_t WS_FLAG = 0;
constexpr size_t WS_QI  = 8192;
constexpr size_t WS_KV  = WS_QI + 2621440;
constexpr size_t WS_Z1  = WS_KV + 196608;
constexpr size_t WS_Z2R = WS_Z1 + 3538944;
constexpr size_t WS_Z2N = WS_Z2R + 3538944;
constexpr size_t WS_AO  = WS_Z2N + 4194304;
constexpr size_t WS_W3  = WS_AO + 6160384;
constexpr size_t WS_END = WS_W3 + 8 * 94 * 2560;
constexpr int KV_CHUNK = 12288, KV_VOFF = 6144;
struct Params {
  const char *x, *wq, *wk, *wv, *wo, *win, *wsp, *bsp, *wout;
  char* ws; char* out;
};

constexpr int K1_STAGE = 36864, K1_NS = 4, K1_XT = 16384;
constexpr int K1_CSP = K1_NS * K1_STAGE;
constexpr int K1_CSF = K1_CSP + 2560;
constexpr int K1_STP = K1_CSF + 640;
constexpr int K1_LDS = K1_STP + 1024;
constexpr float QSCALE = 0.22360679774997896f * 1.4426950408889634f;
enum { T_Q = 0, T_K = 1, T_V = 2, T_Z1 = 3, T_Z2 = 4, T_E = 5 };
DI void k1_slot(int cg, int j, int& type, int& idx) {
  if (j < 3 || (j == 3 && cg < 6)) { const int z = cg < 6 ? cg * 4 + j : 24 + (cg - 6) * 3 + j; type = z < 27 ? T_Z1 : T_Z2; idx = z < 27 ? z : z - 27; }
  else if (cg == 15) { type = j == 3 ? T_K : T_V; idx = 0; }
  else if (cg < 4) { type = T_E; idx = 0; }
  else if (cg < 6) { type = T_Q; idx = cg - 4; }
  else { type = T_Q; idx = 2 + (cg - 6) * 2 + (j - 3); }
}
DI f32x2 gelu2(f32x2 x) {
  const f32x2 c1 = {-0.10294324f, -0.10294324f}, c0 = {-2.3022082f, -2.3022082f}, one = {1.f, 1.f};
  const f32x2 a = x * __builtin_elementwise_fma(x * x, c1, c0);
  const f32x2 d = f32x2{__builtin_amdgcn_exp2f(a[0]), __builtin_amdgcn_exp2f(a[1])} + one;
  return x * f32x2{__builtin_amdgcn_rcpf(d[0]), __builtin_amdgcn_rcpf(d[1])};
}
DI void k1_loader(const Params& p, char* lds_, int rb, int cg, int lw) {
  const int lane = threadIdx.x & 63;
  __builtin_amdgcn_s_setprio(3);
  const unsigned l0 = (unsigned)(uintptr_t)lds_;
  const char* base[5]; unsigned stride[5], voff[5];
  const uintptr_t pwq = (uintptr_t)p.wq, pwin = (uintptr_t)p.win, pwk = (uintptr_t)p.wk, pwv = (uintptr_t)p.wv;
#pragma unroll
  for (int j = 0; j < 5; j++) {
    int type, idx; k1_slot(cg, j, type, idx);
    const unsigned rowp = 16 * lw + (lane >> 2), ch = (lane & 3) * 16;
    const bool kvs = j >= 3 && cg == 15, isq = type == T_Q || type == T_E;
    const uintptr_t a = kvs ? (j == 3 ? pwk : pwv) : isq ? pwq + idx * 64 : pwin + (type == T_Z2 ? 1728 : 0) + idx * 64;
    base[j] = (const char*)a;
    stride[j] = kvs ? 2560u : isq ? 64u * 1280u : 64u * 3456u;
    const unsigned o = (lw < 2 ? lw : 2) * 1024 + lane * 16;
    voff[j] = kvs ? (o < 2544u ? o : 2544u) : rowp * (isq ? 1280u : 3456u) + ch;
  }
  const char* xb = p.x + (size_t)rb * 128 * 1280;
  unsigned xv[4];
#pragma unroll
  for (int i = 0; i < 4; i++) { const unsigned row = 8 * (4 * lw + i) + (lane >> 3); xv[i] = row * 1280 + (((lane & 7) ^ ((row >> 1) & 7)) << 4); }
  auto issue = [&](int st) {
    const unsigned slot = l0 + (st & 3) * K1_STAGE;
#pragma unroll
    for (int i = 0; i < 4; i++) gldsn(xb + st * 128, xv[i], slot + (4 * lw + i) * 1024);
#pragma unroll
    for (int j = 0; j < 5; j++) gldsn(base[j] + (size_t)st * stride[j], voff[j], slot + K1_XT + j * 4096 + lw * 1024);
  };
  issue(0); issue(1); issue(2);
  WAITV(18);
  BARRIER();
#pragma unroll 1
  for (int st = 0; st < 10; st++) {
    if (st < 8) WAITV(9); else WAITV(0);
    BARRIER();
    if (st + 3 < 10) issue(st + 3);
  }
}
template <int ROLE>
DI void k1_body(const Params& p, char* lds_, int rb, int cg, int tt, const char* codep, int codelines) {
  constexpr int NJ = ROLE == 0 ? 3 : 2, J0 = ROLE == 0 ? 0 : 3;
  const int lane = threadIdx.x & 63, r = lane & 31, h = lane >> 5;
  LAS char* lds = (LAS char*)lds_;
  const bool kv = ROLE == 1 && cg == 15;
  int stype[NJ], sidx[NJ];
#pragma unroll
  for (int j = 0; j < NJ; j++) k1_slot(cg, J0 + j, stype[j], sidx[j]);
#define K1_BPRIO 1
  if (ROLE == 1) __builtin_amdgcn_s_setprio(K1_BPRIO);
  unsigned junk = 0;
  if (ROLE == 0 && tt == 0) touch_lines(codep, codelines, junk, lane);
  const int row = 32 * tt + r;
  unsigned xa[4], wa[4];
  const unsigned pitch = kv ? 40 : 64;
  const int blk = (lane >> 4) & 1, q = (lane & 15) >> 2, pp = lane & 3;
#pragma unroll
  for (int i = 0; i < 4; i++) {
    const unsigned ks = (i + tt) & 3;
    xa[i] = row * 128 + ((32 * ks + 16 * h) ^ (16 * ((row >> 1) & 7)));
    wa[i] = K1_XT + J0 * 4096 + (16 * ks + 8 * h + q) * pitch + 32 * blk + 8 * pp;
  }
  f32x16 acc[NJ];
#pragma unroll
  for (int j = 0; j < NJ; j++) acc[j] = f32x16{};
  f32x4 cs[NJ];
  float sq[4] = {0.f, 0.f, 0.f, 0.f}, sm[4] = {0.f, 0.f, 0.f, 0.f};
#pragma unroll
  for (int j = 0; j < NJ; j++) cs[j] = f32x4{};
  const unsigned ONES = 0x3F803F80u;
  const unsigned selw = ((lane & 15) == ((lane >> 4) & 1)) ? ONES : 0u;
  const u32x4 SEL = {selw, selw, selw, selw};
  auto mainloop = [&](auto KVC) {
    constexpr bool KV = decltype(KVC)::value;
    u32x4 xf[4] = {}, wf[4][NJ] = {};
    auto rd = [&](LAS char* sl, int i) {
      xf[i] = LDS128(sl + xa[i]);
#pragma unroll
      for (int j = 0; j < NJ; j++) wf[i][j] = ldtr(sl + wa[i], j * 4096, j * 4096 + (KV ? 160 : 256));
    };
    auto mm = [&](int i, LAS char* nsl, int ni) {
#pragma unroll
      for (int j = 0; j < NJ; j++) {
        if (KV && j == 1) acc[j] = MFMA32(xf[i], wf[i][j], acc[j]);
        else acc[j] = MFMA32(wf[i][j], xf[i], acc[j]);
        SB();
        if (j == 0) { xf[ni] = LDS128(nsl + xa[ni]); wf[ni][0] = ldtr(nsl + wa[ni], 0, KV ? 160 : 256); }
        else { wf[ni][j] = ldtr(nsl + wa[ni], j * 4096, j * 4096 + (KV ? 160 : 256)); }
        SB();
      }
      if (ROLE == 1) {
#pragma unroll
        for (int e = 0; e < 4; e++) { const float lo = bflo(xf[i][e]), hi = bfhi(xf[i][e]); sqacc(sq[(2 * e) & 3], lo); sqacc(sq[(2 * e + 1) & 3], hi); addacc(sm[e & 1], lo); addacc(sm[2 + (e & 1)], hi); }
        SB();
      }
    };
    BARRIER();
    rd(lds, 0); rd(lds, 1);
#pragma unroll 1
    for (int st = 0; st < 10; st++) {
      LAS char* sl = lds + (st & 3) * K1_STAGE;
      LAS char* sn = lds + ((st + 1) & 3) * K1_STAGE;
      mm(0, sl, 2);
      mm(1, sl, 3);
#pragma unroll
      for (int j = 0; j < NJ; j++) cs[j] = MFMA16(SEL, wf[0][j], cs[j]);
      SB();
      BARRIER();
      mm(2, sn, 0);
      mm(3, sn, 1);
      SB();
    }
  };
  if (kv) mainloop(std::true_type{}); else mainloop(std::false_type{});
  LAS float* csp = (LAS float*)(lds + K1_CSP);
  LAS float* csf = (LAS float*)(lds + K1_CSF);
  LAS f32x2* stt = (LAS f32x2*)(lds + K1_STP);
  if (lane < 16) {
#pragma unroll
    for (int j = 0; j < NJ; j++) { csp[tt * 160 + (J0 + j) * 32 + lane] = cs[j][0]; csp[tt * 160 + (J0 + j) * 32 + 16 + lane] = cs[j][1]; }
  }
  if (ROLE == 1) {
    const float sqs = half_sum((sq[0] + sq[1]) + (sq[2] + sq[3]));
    const float su = half_sum((sm[0] + sm[1]) + (sm[2] + sm[3]));
    const float mu = su * (1.f / 640.f), var = fmaxf(sqs * (1.f / 640.f) - mu * mu, 0.f), rs = __builtin_amdgcn_rsqf(var + 1e-5f);
    if (h == 0) stt[row] = f32x2{mu * rs, rs};
  }
  BARRIER();
  { const int c = threadIdx.x; if (c < 160) csf[c] = (csp[c] + csp[160 + c]) + (csp[320 + c] + csp[480 + c]); }
  const f32x2 st_ = stt[row];
  BARRIER();
  const int tb = rb * 4 + tt;
#pragma unroll
  for (int j = 0; j < NJ; j++) {
    const int type = stype[j];
    if (type == T_E) continue;
    if (ROLE == 1 && type == T_V) {
      const float csum = csf[(J0 + j) * 32 + r];
      char* vb = p.ws + WS_KV + (size_t)rb * KV_CHUNK + KV_VOFF + tt * 1536 + h * 384 + r * 16;
#pragma unroll
      for (int s = 0; s < 2; s++) {
        float v[8];
#pragma unroll
        for (int i = 0; i < 8; i++) { const f32x2 t = stt[32 * tt + 16 * s + 8 * (i >> 2) + 4 * h + (i & 3)]; v[i] = t[1] * acc[j][8 * s + i] - t[0] * csum; }
        u32x4 w = {cvtpk(v[0], v[1]), cvtpk(v[2], v[3]), cvtpk(v[4], v[5]), cvtpk(v[6], v[7])};
        if (r == 20) w = u32x4{ONES, ONES, ONES, ONES};
        if (r > 20) w = u32x4{0u, 0u, 0u, 0u};
        if (r < 24) *(u32x4*)(vb + s * 768) = w;
      }
      continue;
    }
    f32x2 v[8];
    const f32x2 rs2 = {st_[1], st_[1]}, nm2 = {-st_[0], -st_[0]};
#pragma unroll
    for (int g = 0; g < 4; g++) {
      const f32x4 c4 = *(LAS f32x4*)(csf + (J0 + j) * 32 + 8 * g + 4 * h);
      v[2 * g] = __builtin_elementwise_fma(f32x2{c4[0], c4[1]}, nm2, f32x2{acc[j][4 * g], acc[j][4 * g + 1]} * rs2);
      v[2 * g + 1] = __builtin_elementwise_fma(f32x2{c4[2], c4[3]}, nm2, f32x2{acc[j][4 * g + 2], acc[j][4 * g + 3]} * rs2);
    }
    if (ROLE == 1 && type == T_K) {
      char* kb = p.ws + WS_KV + (size_t)rb * KV_CHUNK + tt * 1536 + r * 16 + h * 8;
      *(u32x2*)(kb) = u32x2{cvtpk(v[0][0], v[0][1]), cvtpk(v[1][0], v[1][1])};
      *(u32x2*)(kb + 512) = u32x2{cvtpk(v[2][0], v[2][1]), cvtpk(v[3][0], v[3][1])};
      *(u32x2*)(kb + 1024) = h ? u32x2{0x00003F80u, 0u} : u32x2{cvtpk(v[4][0], v[4][1]), cvtpk(v[5][0], v[5][1])};
      continue;
    }
    unsigned off; char* base;
    if (type == T_Q) {
      base = p.ws + WS_QI; off = ((tb * 80 + sidx[j] * 4 + h) * 32 + r) * 16;
#pragma unroll
      for (int i = 0; i < 8; i++) v[i] *= f32x2{QSCALE, QSCALE};
    } else {
      base = p.ws + (type == T_Z1 ? WS_Z1 : WS_Z2R); off = ((tb * 108 + sidx[j] * 4 + h) * 32 + r) * 16;
#pragma unroll
      for (int i = 0; i < 8; i++) v[i] = gelu2(v[i]);
    }
#pragma unroll
    for (int g = 0; g < 4; g += 2)
      st16_wt(base, off + g * 512, pair8(u32x2{cvtpk(v[2 * g][0], v[2 * g][1]), cvtpk(v[2 * g + 1][0], v[2 * g + 1][1])}, u32x2{cvtpk(v[2 * g + 2][0], v[2 * g + 2][1]), cvtpk(v[2 * g + 3][0], v[2 * g + 3][1])}), 3538944);
  }
  asm volatile("" :: "v"(junk));
}
__global__ void __launch_bounds__(768, 3) k1_proj(Params p) {
  extern __shared__ __attribute__((aligned(16))) char lds[];
  unsigned long long pc; int codelen;
  asm volatile("s_getpc_b64 %0\nhipk_pc_mark:\n\ts_mov_b32 %1, hipk_text_mark-hipk_pc_mark" : "=s"(pc), "=s"(codelen));
  const char* codep = (const char*)(pc & ~127ull);
  const int codelines = codelen > 0 ? codelen >> 7 : 1;
  const int b = blockIdx.x, x = b & 7, j = b >> 3;
  const int rb = (x & 1) * 8 + (j & 7), cg = (x >> 1) * 4 + (j >> 3);
  const int wid = RFL(threadIdx.x >> 6);
  if (b == 0 && threadIdx.x < 512) ((u32x4*)(p.ws + WS_FLAG))[threadIdx.x] = u32x4{0u, 0u, 0u, 0u};
  if (wid < 4) k1_body<0>(p, lds, rb, cg, wid, codep, codelines); else if (wid < 8) k1_body<1>(p, lds, rb, cg, wid - 4, codep, codelines); else k1_loader(p, lds, rb, cg, wid - 8);
}

typedef __attribute__((address_space(1))) unsigned gu32;
constexpr int K2_LDS = 81920;
constexpr float SM_OFF = 100.f;
#define EX(x) __builtin_amdgcn_exp2f(x)
#define AUNIT(Sc, Sn, qn0, qn1, Oc, Op, LD0, LD1, LD2, LD3) do { \
    Sc[0] = EX(Sc[0]); Sc[1] = EX(Sc[1]); SB(); \
    Sn = MFMA32(k0, qn0, zero16); SB(); LD0; SB(); \
    Sc[2] = EX(Sc[2]); Sc[3] = EX(Sc[3]); SB(); p1[2] = cvtpk(e12, e13); p1[3] = cvtpk(e14, e15); SB(); \
    Sc[4] = EX(Sc[4]); Sc[5] = EX(Sc[5]); SB(); \
    Op = MFMA32(v1, p1, Op); SB(); LD1; SB(); \
    Sc[6] = EX(Sc[6]); Sc[7] = EX(Sc[7]); SB(); p0[0] = cvtpk(Sc[0], Sc[1]); p0[1] = cvtpk(Sc[2], Sc[3]); SB(); \
    Sc[8] = EX(Sc[8]); Sc[9] = EX(Sc[9]); SB(); \
    Sn = MFMA32(k1, qn1, Sn); SB(); LD2; SB(); \
    Sc[10] = EX(Sc[10]); Sc[11] = EX(Sc[11]); SB(); p0[2] = cvtpk(Sc[4], Sc[5]); p0[3] = cvtpk(Sc[6], Sc[7]); SB(); \
    e12 = EX(Sc[12]); e13 = EX(Sc[13]); SB(); \
    Oc = MFMA32(v0, p0, Oc); SB(); LD3; SB(); \
    e14 = EX(Sc[14]); e15 = EX(Sc[15]); SB(); p1[0] = cvtpk(Sc[8], Sc[9]); p1[1] = cvtpk(Sc[10], Sc[11]); SB(); \
  } while (0)
#define ATILE(kn, v1c, v0n) do { \
    AUNIT(sA, sB, qB0, qB1, oA, oB, k0 = LDS128(kn), v1 = LDS128(v1c), k1 = LDS128((kn) + k1d), (void)0); \
    AUNIT(sB, sA, qA0, qA1, oB, oA, (void)0, (void)0, (void)0, v0 = LDS128(v0n)); \
  } while (0)
DI float max3f(float a, float b, float c) { return __builtin_fmaxf(__builtin_fmaxf(a, b), c); }
DI float max16(const f32x16& s) {
  const float a = max3f(max3f(s[0], s[1], s[2]), s[3], s[4]), b = max3f(max3f(s[5], s[6], s[7]), s[8], s[9]), c = max3f(max3f(s[10], s[11], s[12]), s[13], s[14]);
  return max3f(max3f(a, b, c), s[15], s[15]);
}
DI f32x16 attn_exact(const char* kv, u32x4 q0, u32x4 q1, int r, int h) {
  const f32x16 zero16 = {};
  q1[2] = 0u;
  f32x16 o = {}; float m = -3.0e38f;
  const int rr = r < 21 ? r : 21;
#pragma unroll 1
  for (int t = 0; t < 64; t++) {
    const char* cb = kv + (size_t)(t >> 2) * KV_CHUNK;
    const char* kb = cb + (t & 3) * 1536 + r * 16;
    const u32x4 k0 = *(const u32x4*)(kb + h * 512), k1 = *(const u32x4*)(kb + 1024);
    const char* vb = cb + KV_VOFF + (t & 3) * 1536 + h * 384 + rr * 16;
    const u32x4 v0 = *(const u32x4*)(vb), v1 = *(const u32x4*)(vb + 768);
    f32x16 s = MFMA32(k0, q0, zero16); s = MFMA32(k1, q1, s);
    float mx = s[0];
#pragma unroll
    for (int i = 1; i < 16; i++) mx = fmaxf(mx, s[i]);
    const float mn = fmaxf(m, half_max(mx)), a = EX(m - mn);
    m = mn;
#pragma unroll
    for (int i = 0; i < 16; i++) { o[i] *= a; s[i] = EX(s[i] - mn); }
    const u32x4 p0 = {cvtpk(s[0], s[1]), cvtpk(s[2], s[3]), cvtpk(s[4], s[5]), cvtpk(s[6], s[7])};
    const u32x4 p1 = {cvtpk(s[8], s[9]), cvtpk(s[10], s[11]), cvtpk(s[12], s[13]), cvtpk(s[14], s[15])};
    o = MFMA32(v0, p0, o); o = MFMA32(v1, p1, o);
  }
  return o;
}
DI void attn_role(const Params& p, char* lds_, int wg) {
  const int tid = threadIdx.x, lane = tid & 63, r = lane & 31, h = lane >> 5;
  const int wid = RFL(tid >> 6);
  const int pi = wid & 3, kh = wid >> 2;
  const int tb = wg >> 2, headA = (wg & 3) * 8 + 2 * pi;
  const unsigned l0 = (unsigned)(uintptr_t)lds_;
  LAS char* lds = (LAS char*)lds_;
  const char* kvg = p.ws + WS_KV;
  const char* kvsrc = kvg + (size_t)kh * 8 * KV_CHUNK + pi * 3072;
  const unsigned voff = lane * 16;
  const unsigned ring = l0 + kh * 3 * KV_CHUNK + pi * 3072;
#define APRIO 2
  __builtin_amdgcn_s_setprio(APRIO);
  glds3(kvsrc, voff, ring);
  u32x4 qA0, qA1, qB0, qB1;
  {
    const char* qrow = p.ws + WS_QI + (size_t)tb * 80 * 32 * 16;
    auto poff = [&](int c4) { return (unsigned)(r * 16 + (c4 >> 1) * 512 + (c4 & 1) * 8); };
    u32x2 a0, a1, a2, b0, b1, b2;
    const int cA = headA * 5, cB = cA + 5;
    asm volatile("global_load_dwordx2 %0, %6, %12\n\tglobal_load_dwordx2 %1, %7, %12\n\tglobal_load_dwordx2 %2, %8, %12\n\t"
                 "global_load_dwordx2 %3, %9, %12\n\tglobal_load_dwordx2 %4, %10, %12\n\tglobal_load_dwordx2 %5, %11, %12"
                 : "=&v"(a0), "=&v"(a1), "=&v"(a2), "=&v"(b0), "=&v"(b1), "=&v"(b2)
                 : "v"(poff(cA + 2 * h)), "v"(poff(cA + 2 * h + 1)), "v"(poff(cA + 4)), "v"(poff(cB + 2 * h)), "v"(poff(cB + 2 * h + 1)), "v"(poff(cB + 4)), "s"(qrow) : "memory");
    glds3(kvsrc + KV_CHUNK, voff, ring + KV_CHUNK);
    asm volatile("s_waitcnt vmcnt(3)\n\ts_barrier" : "+v"(a0), "+v"(a1), "+v"(a2), "+v"(b0), "+v"(b1), "+v"(b2) :: "memory");
    qA0 = u32x4{a0[0], a0[1], a1[0], a1[1]}; qB0 = u32x4{b0[0], b0[1], b1[0], b1[1]};
    qA1 = h ? u32x4{0u, 0u, 0u, 0u} : u32x4{a2[0], a2[1], 0u, 0u};
    qB1 = h ? u32x4{0u, 0u, 0u, 0u} : u32x4{b2[0], b2[1], 0u, 0u};
  }
  const f32x16 zero16 = {};
  const int k1d = 1024 - h * 512;
  const int rr = r < 21 ? r : 21;
  u32x4 k0, k1; f32x16 sA, sB;
  {
    LAS char* kb0 = lds + h * 512 + r * 16;
    k0 = LDS128(kb0); k1 = LDS128(kb0 + k1d);
    const u32x4 k2 = LDS128(kb0 + 1536), k3 = LDS128(kb0 + 1536 + k1d);
    sA = MFMA32(k0, qA0, zero16); sB = MFMA32(k0, qB0, zero16); sA = MFMA32(k1, qA1, sA); sB = MFMA32(k1, qB1, sB);
    f32x16 tA = MFMA32(k2, qA0, zero16), tB = MFMA32(k2, qB0, zero16); tA = MFMA32(k3, qA1, tA); tB = MFMA32(k3, qB1, tB);
    const float mA = half_max(fmaxf(max16(sA), max16(tA))), mB = half_max(fmaxf(max16(sB), max16(tB)));
    if (h == 0) { qA1[2] = cvtpk(-(mA + SM_OFF), 0.f); qB1[2] = cvtpk(-(mB + SM_OFF), 0.f); }
  }
  LAS char* kbase = lds + kh * 3 * KV_CHUNK + h * 512 + r * 16;
  LAS char* vbase = lds + kh * 3 * KV_CHUNK + KV_VOFF + h * 384 + rr * 16;
  k0 = LDS128(kbase); k1 = LDS128(kbase + k1d);
  sA = MFMA32(k0, qA0, zero16); sA = MFMA32(k1, qA1, sA);
  u32x4 v0 = LDS128(vbase), v1 = v0, p0 = {0u, 0u, 0u, 0u}, p1 = {0u, 0u, 0u, 0u};
  f32x16 oA = {}, oB = {};
  float e12 = 0.f, e13 = 0.f, e14 = 0.f, e15 = 0.f;
  int sc = 0;
#pragma unroll 1
  for (int c = 0; c < 8; c++) {
    const int sn = sc == 2 * KV_CHUNK ? 0 : sc + KV_CHUNK, sp = sn == 2 * KV_CHUNK ? 0 : sn + KV_CHUNK;
    if (c + 2 < 8) glds3(kvsrc + (size_t)(c + 2) * KV_CHUNK, voff, ring + sp);
    LAS char* kc = kbase + sc; LAS char* kn = kbase + sn; LAS char* vc = vbase + sc; LAS char* vn = vbase + sn;
    ATILE(kc + 1 * 1536, vc + 1 * 768, vc + 2 * 768);
    ATILE(kc + 2 * 1536, vc + 3 * 768, vc + 4 * 768);
    if (c == 0) asm volatile("s_waitcnt vmcnt(3)\n\ts_barrier" ::: "memory");
    ATILE(kc + 3 * 1536, vc + 5 * 768, vc + 6 * 768);
    ATILE(kn, vc + 7 * 768, vn);
    asm volatile("s_waitcnt vmcnt(0) lgkmcnt(0)\n\ts_barrier" ::: "memory");
    sc = sn;
  }
  p1[2] = cvtpk(e12, e13); p1[3] = cvtpk(e14, e15);
  oB = MFMA32(v1, p1, oB);
  LAS f32x4* ex = (LAS f32x4*)(lds + (pi * 2 + kh) * 3072 + lane * 16);
  LAS const f32x4* exr = (LAS const f32x4*)(lds + (pi * 2 + (kh ^ 1)) * 3072 + lane * 16);
  f32x16 o;
  if (kh == 0) { o = oA;
#pragma unroll
    for (int g = 0; g < 3; g++) ex[g * 64] = f32x4{oB[4 * g], oB[4 * g + 1], oB[4 * g + 2], oB[4 * g + 3]};
  } else { o = oB;
#pragma unroll
    for (int g = 0; g < 3; g++) ex[g * 64] = f32x4{oA[4 * g], oA[4 * g + 1], oA[4 * g + 2], oA[4 * g + 3]};
  }
  BARRIER();
#pragma unroll
  for (int g = 0; g < 3; g++) { f32x4 t = exr[g * 64]; o[4 * g] += t[0]; o[4 * g + 1] += t[1]; o[4 * g + 2] += t[2]; o[4 * g + 3] += t[3]; }
  {
    float z[4];
#pragma unroll
    for (int i = 0; i < 4; i++) z[i] = __builtin_fmaf(o[i], 0.f, __builtin_fmaf(o[4 + i], 0.f, o[8 + i] * 0.f));
    const float chk = (z[0] + z[1]) + (z[2] + z[3]);
    const bool bad = !(chk == 0.f) || (h == 1 && !(o[8] < 1.0e37f));
    if (__builtin_expect(__any(bad), 0)) o = attn_exact(kvg, kh ? qB0 : qA0, kh ? qB1 : qA1, r, h);
  }
  auto lr = __builtin_amdgcn_permlane32_swap(__float_as_uint(o[8]), __float_as_uint(o[8]), false, false);
  const float il = __builtin_amdgcn_rcpf(__uint_as_float(lr[1]));
  LAS char* ost = lds + 32768;
  const int hl = 2 * pi + kh;
#pragma unroll
  for (int g = 0; g < 3; g++) {
    if (g < 2 || h == 0) {
      const int kk = hl * 20 + (2 * g + h) * 4;
      *(LAS u32x2*)(ost + ((kk >> 3) * 32 + r) * 16 + ((kk >> 2) & 1) * 8) = u32x2{cvtpk(o[4 * g] * il, o[4 * g + 1] * il), cvtpk(o[4 * g + 2] * il, o[4 * g + 3] * il)};
    }
  }
  BARRIER();
#pragma unroll
  for (int k = 0; k < 2; k++) {
    const int i = tid + 512 * k;
    if (i < 640) st16_wt(p.ws + WS_AO, (((wg & 3) * 20 + (i >> 5)) * 2048 + tb * 32 + (i & 31)) * 16, *(LAS u32x4*)(ost + i * 16), 6160384);
  }
  asm volatile("s_waitcnt vmcnt(0)" ::: "memory");
  BARRIER();
  if (tid == 0) __hip_atomic_fetch_add((gu32*)(p.ws + WS_FLAG) + 1024 + (tb >> 1) * 16, 1u, __ATOMIC_RELAXED, __HIP_MEMORY_SCOPE_AGENT);
}

DI int fb_start(int fb) { return (fb >> 2) * 216 + (fb & 3) * 56; }
constexpr int SP_STAGE = 24576, SP_ZOFF = 16384;

constexpr int F_ACT = 8192, F_STAGE = F_ACT + 10240;
DI void fin_role(const Params& p, char* lds_, int b, int wid) {
  const int tid = threadIdx.x, lane = tid & 63, r = lane & 31, h = lane >> 5;
  const unsigned l0 = (unsigned)(uintptr_t)lds_;
  LAS char* lds = (LAS char*)lds_;
  const int x = b & 7, jj = b >> 3;
  const int tbk = (x >> 1) * 4 + (jj & 3), fb = (x & 1) * 8 + (jj >> 2);
  const int tb = 2 * tbk + (fb >> 3), nb = fb & 7;
  gu32* cnt = (gu32*)(p.ws + WS_FLAG);
  BARRIER();
  if (tid == 0) __hip_atomic_fetch_add(cnt + 512 + tbk * 16, 1u, __ATOMIC_RELAXED, __HIP_MEMORY_SCOPE_AGENT);
  if (wid >= 4) {
    const int lw = wid - 4;
    const char* ab = p.ws + WS_AO + (size_t)tb * 64 * 16;
    const char* wb = p.ws + WS_W3 + (size_t)nb * 94 * 2560;
    const unsigned lv = lane * 16;
    auto ks0 = [&](int s) { return s < 14 ? 40 + 4 * s : 4 * (s - 14); };
    auto issue_w = [&](int s) {
      const int p0 = lw < 2 ? 3 * lw : 2 + 2 * lw;
      const char* src = wb + (size_t)ks0(s) * 2560;
      const unsigned dst = l0 + (s & 3) * F_STAGE + F_ACT;
#pragma unroll
      for (int i = 0; i < 3; i++) { const int pc = (lw >= 2 && i == 2) ? p0 : p0 + i; gldsn_dev(src + (s == 13 && pc >= 5 ? 0 : pc * 1024), lv, dst + pc * 1024); }
    };
    auto issue_a = [&](int s) {
#pragma unroll
      for (int i = 0; i < 2; i++) gldsn_dev(ab + (size_t)(2 * ks0(s) + (s == 13 && lw >= 2 ? 0 : 2 * lw) + i) * 32768, lv, l0 + (s & 3) * F_STAGE + (2 * lw + i) * 1024);
    };
    auto await = [&](gu32* word, unsigned want) {
      unsigned spins = 0;
      while ((unsigned)RFL(__hip_atomic_load(word, __ATOMIC_RELAXED, __HIP_MEMORY_SCOPE_AGENT)) < want) { if (++spins > (1u << 22)) break; __builtin_amdgcn_s_sleep(1); }
      asm volatile("" ::: "memory");
    };
    await(cnt + 1536 + nb * 16, 94u);
    issue_w(0); issue_w(1); issue_w(2);
    await(cnt + 512 + tbk * 16, 16u);
    issue_a(0); issue_a(1); issue_a(2);
#pragma unroll 1
    for (int s = 0; s < 24; s++) {
      if (s == 0 || s == 14) WAITV(4); else if (s == 1 || s == 15) WAITV(7); else if (s == 12) WAITV(8); else if (s == 13) WAITV(6);
      else if (s == 22) WAITV(5); else if (s == 23) WAITV(0); else WAITV(10);
      BARRIER();
      if (s + 3 < 24) {
        if (s < 11 || s > 13) issue_a(s + 3);
        issue_w(s + 3);
      }
      if (s == 13) { await(cnt + 1024 + tb * 16, 8u); issue_a(14); issue_a(15); issue_a(16); }
    }
    return;
  }
#define FPRIO 1
  __builtin_amdgcn_s_setprio(FPRIO);
  const int tt = wid & 1, two = wid < 2;
  u32x4 xr[3];
#pragma unroll
  for (int k = 0; k < 3; k++) { const int i = tid + 256 * k < 640 ? tid + 256 * k : 0, row = i / 10, c = i - row * 10;
    xr[k] = *(const u32x4*)(p.x + ((size_t)(tb * 64 + row) * 640 + nb * 80 + c * 8) * 2); }
  const unsigned wa = F_ACT + (two ? lane : 128 + h * 16 + (r & 15)) * 16;
  const unsigned aa = h * 1024 + (32 * tt + r) * 16;
  f32x16 acc = {}, acc2 = {};
#pragma unroll 1
  for (int s = 0; s < 24; s++) {
    BARRIER();
    LAS char* sl = lds + (s & 3) * F_STAGE;
    u32x4 af[4], wf[4], wg[4];
#pragma unroll
    for (int ks = 0; ks < 4; ks++) { af[ks] = LDS128(sl + aa + ks * 2048); wf[ks] = LDS128(sl + wa + ks * 2560); }
    if (two) {
#pragma unroll
      for (int ks = 0; ks < 4; ks++) wg[ks] = LDS128(sl + wa + 1024 + ks * 2560);
      acc = MFMA32(wf[0], af[0], acc); acc2 = MFMA32(wg[0], af[0], acc2); acc = MFMA32(wf[1], af[1], acc); acc2 = MFMA32(wg[1], af[1], acc2);
      if (s != 13) { acc = MFMA32(wf[2], af[2], acc); acc2 = MFMA32(wg[2], af[2], acc2); acc = MFMA32(wf[3], af[3], acc); acc2 = MFMA32(wg[3], af[3], acc2); }
    } else {
      acc = MFMA32(wf[0], af[0], acc); acc = MFMA32(wf[1], af[1], acc);
      if (s != 13) { acc = MFMA32(wf[2], af[2], acc); acc = MFMA32(wf[3], af[3], acc); }
    }
  }
  BARRIER();
  LAS float* ot = (LAS float*)lds;
  constexpr int OP = 84;
  const int n0 = two ? 0 : 64;
#pragma unroll
  for (int g = 0; g < 4; g++) {
    const int n = n0 + 8 * g + 4 * h;
    if (n < 80) *(LAS f32x4*)(ot + (32 * tt + r) * OP + n) = f32x4{acc[4 * g], acc[4 * g + 1], acc[4 * g + 2], acc[4 * g + 3]};
    if (two) *(LAS f32x4*)(ot + (32 * tt + r) * OP + 32 + n) = f32x4{acc2[4 * g], acc2[4 * g + 1], acc2[4 * g + 2], acc2[4 * g + 3]};
  }
  BARRIER();
#pragma unroll
  for (int k = 0; k < 3; k++) {
    const int i = tid + 256 * k;
    if (i < 640) {
      const int row = i / 10, c = i - row * 10;
      const size_t go = ((size_t)(tb * 64 + row) * 640 + nb * 80 + c * 8) * 2;
      const f32x4 a = *(LAS f32x4*)(ot + row * OP + c * 8), a2 = *(LAS f32x4*)(ot + row * OP + c * 8 + 4);
      const u32x4 w = {cvtpk(a[0] + bflo(xr[k][0]), a[1] + bfhi(xr[k][0])), cvtpk(a[2] + bflo(xr[k][1]), a[3] + bfhi(xr[k][1])),
                       cvtpk(a2[0] + bflo(xr[k][2]), a2[1] + bfhi(xr[k][2])), cvtpk(a2[2] + bflo(xr[k][3]), a2[3] + bfhi(xr[k][3]))};
      st16_wt(p.out, (unsigned)go, w, 2621440);
    }
  }
}
DI void sp_role(const Params& p, char* lds_, int b) {
  const int wid = RFL(threadIdx.x >> 6);
  __builtin_amdgcn_s_setprio(3);
  const int tid = threadIdx.x, lane = tid & 63, r = lane & 31, h = lane >> 5;
  const unsigned l0 = (unsigned)(uintptr_t)lds_;
  LAS char* lds = (LAS char*)lds_;
  const int x = b & 7, jj = b >> 3;
  const int tbk = (x >> 1) * 4 + (jj & 3), fb = (x & 1) * 8 + (jj >> 2);
  gu32* cnt = (gu32*)(p.ws + WS_FLAG);
  if (wid >= 4) {
    const int lw = wid - 4;
    const char* wb = p.wsp + (size_t)tbk * 128 * 4096;
    unsigned wv4[4];
#pragma unroll
    for (int i = 0; i < 4; i++) { const unsigned row = 8 * (4 * lw + i) + (lane >> 3); wv4[i] = row * 4096 + (((lane & 7) ^ ((row >> 1) & 7)) << 4); }
    const char* zb = p.ws + WS_Z2N + (size_t)fb * 32 * 8192 + lw * 2048;
    const unsigned zv = lane * 16;
    auto issue_w = [&](int st) {
#pragma unroll
      for (int i = 0; i < 4; i++) gldsn(wb + st * 128, wv4[i], l0 + (st % 3) * SP_STAGE + (4 * lw + i) * 1024);
    };
    auto issue_z = [&](int st) {
      asm volatile("s_mov_b32 m0, %2\n\ts_nop 0\n\tglobal_load_lds_dwordx4 %0, %1 sc1\n\tglobal_load_lds_dwordx4 %0, %1 offset:1024 sc1"
                   :: "v"(zv), "s"(zb + (size_t)st * 8192), "s"(l0 + (st % 3) * SP_STAGE + SP_ZOFF + lw * 2048) : "memory");
    };
    BARRIER(); BARRIER();
    issue_w(0); issue_w(1);
    BARRIER();
    {
      const int u = b * 3 + lw - 1;
      if (lw > 0 && u < 752) {
        const int ks = u >> 3, nb = u & 7;
        const uintptr_t pwo = (uintptr_t)p.wo, pwout = (uintptr_t)p.wout;
        const GAS char* src = (const GAS char*)(ks < 40 ? pwo + (size_t)ks * 16 * 1280 : pwout + (size_t)(ks - 40) * 16 * 1280) + nb * 160;
        LAS char* sc = lds + 3 * SP_STAGE + (lw - 1) * 2560;
#pragma unroll
        for (int i = 0; i < 3; i++) {
          const int c = lane + 64 * i, rw = c / 10, cc = c - rw * 10;
          if (c < 160) *(LAS u32x4*)(sc + c * 16) = *(const GAS u32x4*)(src + rw * 1280 + cc * 16);
        }
        asm volatile("s_waitcnt vmcnt(0) lgkmcnt(0)" ::: "memory");
        LAS char* ta = sc + (8 * h + ((lane & 15) >> 2)) * 160 + 32 * ((lane >> 4) & 1) + 8 * (lane & 3);
        const unsigned dst = (nb * 94 + ks) * 2560;
        const u32x4 f0 = ldtr(ta, 0, 640), f1 = ldtr(ta, 64, 64 + 640), f2 = ldtr(ta, 128, 128 + 640);
        st16_wt(p.ws + WS_W3, dst + lane * 16, f0, 8 * 94 * 2560);
        st16_wt(p.ws + WS_W3, dst + 1024 + lane * 16, f1, 8 * 94 * 2560);
        if (r < 16) st16_wt(p.ws + WS_W3, dst + 2048 + (h * 16 + r) * 16, f2, 8 * 94 * 2560);
        asm volatile("s_waitcnt vmcnt(0) lgkmcnt(0)" ::: "memory");
        if (lane == 0) __hip_atomic_fetch_add(cnt + 1536 + nb * 16, 1u, __ATOMIC_RELAXED, __HIP_MEMORY_SCOPE_AGENT);
      }
    }
    if (lw > 0) {
      const char* src = p.wsp + (unsigned)RFL(b * 32768 + (lw - 1) * 12288);
      const int n2 = lw == 3 ? 4 : 6;
      for (int i = 0; i < n2; i++)
        asm volatile("s_mov_b32 m0, %2\n\ts_nop 0\n\tglobal_load_lds_dwordx4 %0, %1\n\tglobal_load_lds_dwordx4 %0, %1 offset:1024"
                     :: "v"(zv), "s"(src + i * 2048), "s"(l0 + 3 * SP_STAGE - 2048) : "memory");
    }
    if (lw == 0) {
      {
      __builtin_amdgcn_s_sleep(24);
      unsigned spins = 0;
      for (;;) {
        const unsigned c = __hip_atomic_load(cnt + (lane & 31) * 16, __ATOMIC_RELAXED, __HIP_MEMORY_SCOPE_AGENT);
        if (__all(c >= 8u)) break;
        if (++spins > (1u << 21)) break;
        __builtin_amdgcn_s_sleep(6);
      }
      }
      const char* z00 = p.ws + WS_Z2N + (size_t)fb * 32 * 8192;
#pragma unroll
      for (int i = 0; i < 4; i++)
        asm volatile("s_mov_b32 m0, %2\n\ts_nop 0\n\tglobal_load_lds_dwordx4 %0, %1 sc1\n\tglobal_load_lds_dwordx4 %0, %1 offset:1024 sc1\n\tglobal_load_lds_dwordx4 %0, %1 offset:2048 sc1\n\tglobal_load_lds_dwordx4 %0, %1 offset:3072 sc1"
                     :: "v"(zv), "s"(z00 + i * 4096), "s"(l0 + (i >> 1) * SP_STAGE + SP_ZOFF + (i & 1) * 4096) : "memory");
      WAITV(8);
    }
#pragma unroll 1
    for (int st = 0; st < 32; st++) {
      if (st == 0) { if (lw) WAITV(0); } else if (st < 31) WAITV(6); else WAITV(0);
      BARRIER();
      if (st + 2 < 32) { issue_w(st + 2); issue_z(st + 2); }
    }
    fin_role(p, lds_, b, wid);
    return;
  }
#define LNPRIO 3
  __builtin_amdgcn_s_setprio(LNPRIO);
  {
    LAS float* red = (LAS float*)(lds + K2_LDS - 256);
    const bool act = tid < 216;
    const int g8 = act ? tid >> 1 : 107, half = tid & 1;
    const char* src = p.ws + WS_Z2R + ((size_t)((b >> 2) * 108 + g8) * 32 + (b & 3) * 8 + 4 * half) * 16;
    u32x4 raw[4];
#pragma unroll
    for (int i = 0; i < 4; i++) raw[i] = *(const u32x4*)(src + i * 16);
    const unsigned ONES = act ? 0x3F803F80u : 0u;
    LAS float* part = (LAS float*)(lds + 2 * SP_STAGE);
#pragma unroll
    for (int t = 0; t < 4; t++) {
      float su = 0.f, sq = 0.f;
#pragma unroll
      for (int e = 0; e < 4; e++) { const unsigned a = act ? raw[t][e] : 0u; su = dot2(a, ONES, su); sq = dot2(a, a, sq); }
      part[(2 * (4 * half + t)) * 128 + (tid >> 1)] = su;
      part[(2 * (4 * half + t) + 1) * 128 + (tid >> 1)] = sq;
    }
    BARRIER();
    {
      float s4[4];
#pragma unroll
      for (int k = 0; k < 4; k++) { const f32x2 a = *(LAS f32x2*)(part + (4 * wid + k) * 128 + lane * 2); s4[k] = a[0] + a[1]; }
#pragma unroll
      for (int k = 0; k < 4; k++) s4[k] = wave_sum(s4[k]);
      if (lane < 2) {
        const float su = lane ? s4[2] : s4[0], sq = lane ? s4[3] : s4[1];
        const float mu = su * (1.f / 864.f), var = fmaxf(sq * (1.f / 864.f) - mu * mu, 0.f), rs = __builtin_amdgcn_rsqf(var + 1e-5f);
        ((LAS f32x2*)red)[2 * wid + lane] = f32x2{rs, -mu * rs};
      }
    }
    BARRIER();
    LAS char* tr = lds + 2 * SP_STAGE + 8192;
    {
      float v[4][8];
#pragma unroll
      for (int t = 0; t < 4; t++) {
        const f32x2 c = ((LAS f32x2*)red)[4 * half + t];
#pragma unroll
        for (int e = 0; e < 4; e++) { v[t][2 * e] = __builtin_fmaf(bflo(raw[t][e]), c[0], c[1]); v[t][2 * e + 1] = __builtin_fmaf(bfhi(raw[t][e]), c[0], c[1]); }
      }
      if (act) {
#pragma unroll
        for (int i = 0; i < 8; i++) *(LAS u32x2*)(tr + (8 * g8 + i) * 16 + half * 8) = u32x2{cvtpk(v[0][i], v[1][i]), cvtpk(v[2][i], v[3][i])};
      }
    }
    asm volatile("s_waitcnt lgkmcnt(0)" ::: "memory");
    {
      const __amdgpu_buffer_rsrc_t rsrc = __builtin_amdgcn_make_buffer_rsrc((void*)(p.ws + WS_Z2N), 0, 4194304, 0x00020000);
      const unsigned sbase = (((b >> 3) * 4 + ((b >> 1) & 3)) * 2 + (b & 1)) * 1024;
#pragma unroll
      for (int i = 0; i < 4; i++) {
        const int f = 256 * wid + 64 * i + lane;
        if (f < 864) {
          const u32x4 w = *(LAS u32x4*)(tr + f * 16);
          const int q4 = (f >= 216) + (f >= 432) + (f >= 648), rem = f - q4 * 216, sub = (rem >= 56) + (rem >= 112) + (rem >= 168);
          const unsigned off = (unsigned)(q4 * 4 + sub) * (32u * 8192u) + sbase + (rem - sub * 56) * 16;
          __builtin_amdgcn_raw_buffer_store_b128(w, rsrc, off, 0, 16);
        }
      }
    }
    if (tid < 160) {
      const int fbw = tid < 96 ? (tid >> 3) / 3 * 4 + (tid >> 3) % 3 : ((tid - 96) >> 4) * 4 + 3, row = tid < 96 ? 56 + (tid & 7) : 48 + (tid & 15);
      st16_wt(p.ws + WS_Z2N, (unsigned)fbw * (32u * 8192u) + (((b >> 3) * 4 + ((b >> 1) & 3)) * 2 + (b & 1)) * 1024 + row * 16, u32x4{0u, 0u, 0u, 0u}, 4194304);
    }
    asm volatile("s_waitcnt vmcnt(0)" ::: "memory");
    BARRIER();
    if (tid == 0) __hip_atomic_fetch_add(cnt + (b >> 3) * 16, 1u, __ATOMIC_RELAXED, __HIP_MEMORY_SCOPE_AGENT);
  }
  const unsigned row = 32 * wid + r;
  unsigned wa[4];
#pragma unroll
  for (int ks = 0; ks < 4; ks++) wa[ks] = row * 128 + ((32 * ks + 16 * h) ^ (16 * ((row >> 1) & 7)));
  const unsigned za = SP_ZOFF + h * 1024 + r * 16;
  f32x16 acc0 = {}, acc1 = {};
#pragma unroll 1
  for (int st = 0; st < 32; st++) {
    BARRIER();
    LAS char* sl = lds + (st % 3) * SP_STAGE;
    u32x4 wf[4], z0[4], z1[4];
#pragma unroll
    for (int ks = 0; ks < 4; ks++) { wf[ks] = LDS128(sl + wa[ks]); z0[ks] = LDS128(sl + za + ks * 2048); z1[ks] = LDS128(sl + za + ks * 2048 + 512); }
#pragma unroll
    for (int ks = 0; ks < 4; ks++) { acc0 = MFMA32(z0[ks], wf[ks], acc0); acc1 = MFMA32(z1[ks], wf[ks], acc1); }
  }
  const int tok = tbk * 128 + row, f0 = fb_start(fb), width = (fb & 3) == 3 ? 48 : 56;
  const float bias = bflo(*(const unsigned short*)(p.bsp + tok * 2));
  const char* z1b = p.ws + WS_Z1 + ((size_t)((tok >> 5) * 108 + (f0 >> 3)) * 32 + r) * 16 + h * 8;
#pragma unroll
  for (int j = 0; j < 2; j++) {
    const f32x16& a = j ? acc1 : acc0;
    u32x2 w[4];
#pragma unroll
    for (int g = 0; g < 4; g++) {
      const int fl = 32 * j + 8 * g;
      u32x2 z = {0u, 0u};
      if (fl < width) z = *(const u32x2*)(z1b + (4 * j + g) * 512);
      w[g] = u32x2{cvtpk(bflo(z[0]) * (a[4 * g] + bias), bfhi(z[0]) * (a[4 * g + 1] + bias)), cvtpk(bflo(z[1]) * (a[4 * g + 2] + bias), bfhi(z[1]) * (a[4 * g + 3] + bias))};
    }
#pragma unroll
    for (int g = 0; g < 4; g += 2) {
      const u32x4 w8 = pair8(w[g], w[g + 1]);
      const int fl = 32 * j + 8 * (g + h);
      if (fl < width) st16_wt(p.ws + WS_AO, ((80 + ((f0 + fl) >> 3)) * 2048 + tok) * 16, w8, 6160384);
    }
  }
  asm volatile("s_waitcnt vmcnt(0)" ::: "memory");
  fin_role(p, lds_, b, wid);
}
__global__ void __launch_bounds__(512, 4) k2_mix(Params p) {
  extern __shared__ __attribute__((aligned(16))) char lds[];
  if (blockIdx.x >= 256) sp_role(p, lds, blockIdx.x - 256); else attn_role(p, lds, blockIdx.x);
  asm volatile("hipk_text_mark:");
}

extern "C" void kernel_launch(void* const* d_in, const int* in_sizes, int n_in, void* d_out, int out_size, void* d_ws, size_t ws_size, hipStream_t stream) {
  Params p;
  p.x = (const char*)d_in[0]; p.wq = (const char*)d_in[1]; p.wk = (const char*)d_in[2]; p.wv = (const char*)d_in[3]; p.wo = (const char*)d_in[4];
  p.win = (const char*)d_in[6]; p.wsp = (const char*)d_in[7]; p.bsp = (const char*)d_in[8]; p.wout = (const char*)d_in[9];
  p.ws = (char*)d_ws; p.out = (char*)d_out;
  (void)hipFuncSetAttribute((const void*)k1_proj, hipFuncAttributeMaxDynamicSharedMemorySize, K1_LDS);
  (void)hipFuncSetAttribute((const void*)k2_mix, hipFuncAttributeMaxDynamicSharedMemorySize, K2_LDS);
  k1_proj<<<256, 768, K1_LDS, stream>>>(p);
  k2_mix<<<512, 512, K2_LDS, stream>>>(p);
}
```

```cpp
#include <hip/hip_runtime.h>
#include <stdint.h>
#include <type_traits>
typedef __bf16 bf16x8 __attribute__((ext_vector_type(8)));
typedef __bf16 bf16x2 __attribute__((ext_vector_type(2)));
typedef short s16x4 __attribute__((ext_vector_type(4)));
typedef float f32x16 __attribute__((ext_vector_type(16)));
typedef float f32x4 __attribute__((ext_vector_type(4)));
typedef float f32x2 __attribute__((ext_vector_type(2)));
typedef unsigned u32x4 __attribute__((ext_vector_type(4)));
typedef unsigned u32x2 __attribute__((ext_vector_type(2)));
#define DI __device__ __forceinline__
#define LAS __attribute__((address_space(3)))
#define GAS __attribute__((address_space(1)))
#define SB() __builtin_amdgcn_sched_barrier(0)
#define RFL(x) __builtin_amdgcn_readfirstlane(x)
#define MFMA32(a, b, c) __builtin_amdgcn_mfma_f32_32x32x16_bf16(__builtin_bit_cast(bf16x8, a), __builtin_bit_cast(bf16x8, b), c, 0, 0, 0)
#define MFMA16(a, b, c) __builtin_amdgcn_mfma_f32_16x16x32_bf16(__builtin_bit_cast(bf16x8, a), __builtin_bit_cast(bf16x8, b), c, 0, 0, 0)
DI f32x4 mfma4sq(unsigned x0, unsigned x1, f32x4 c) { u32x2 v = {x0, x1}; s16x4 a = __builtin_bit_cast(s16x4, v); return __builtin_amdgcn_mfma_f32_4x4x4bf16_1k(a, a, c, 0, 0, 0); }
#define MFMA4(x0, x1, c) mfma4sq(x0, x1, c)
#define LDS128(p) (*(const LAS u32x4*)(p))
DI unsigned cvtpk(float lo, float hi) { f32x2 v = {lo, hi}; bf16x2 b = __builtin_convertvector(v, bf16x2); return __builtin_bit_cast(unsigned, b); }
DI float bflo(unsigned u) { return __uint_as_float(u << 16); }
DI float bfhi(unsigned u) { return __uint_as_float(u & 0xffff0000u); }
DI void sqacc(float& a, float x) { asm("v_fmac_f32_e32 %0, %1, %1" : "+v"(a) : "v"(x)); }
DI void addacc(float& a, float x) { asm("v_add_f32_e32 %0, %0, %1" : "+v"(a) : "v"(x)); }
DI float dot2(unsigned a, unsigned b, float c) { return __builtin_amdgcn_fdot2_f32_bf16(__builtin_bit_cast(bf16x2, a), __builtin_bit_cast(bf16x2, b), c, false); }
DI float half_sum(float m) { auto r = __builtin_amdgcn_permlane32_swap(__float_as_uint(m), __float_as_uint(m), false, false); return __uint_as_float(r[0]) + __uint_as_float(r[1]); }
DI float half_max(float m) { auto r = __builtin_amdgcn_permlane32_swap(__float_as_uint(m), __float_as_uint(m), false, false); return fmaxf(__uint_as_float(r[0]), __uint_as_float(r[1])); }
template <int CTRL, int RMASK> DI float dpp_add(float v) { return v + __uint_as_float(__builtin_amdgcn_update_dpp(0u, __float_as_uint(v), CTRL, RMASK, 0xf, true)); }
DI float wave_sum(float v) {
  v = dpp_add<0x111, 0xf>(v); v = dpp_add<0x112, 0xf>(v); v = dpp_add<0x114, 0xf>(v); v = dpp_add<0x118, 0xf>(v);
  v = dpp_add<0x142, 0xa>(v); v = dpp_add<0x143, 0xc>(v);
  return __uint_as_float(__builtin_amdgcn_readlane(__float_as_uint(v), 63));
}
DI void glds1(const void* sbase, unsigned voff, unsigned lds_base) {
  unsigned sv;
  asm volatile("s_mov_b32 %0, m0\n\ts_mov_b32 m0, %3\n\ts_nop 0\n\tglobal_load_lds_dwordx4 %1, %2\n\ts_mov_b32 m0, %0"
               : "=&s"(sv) : "v"(voff), "s"(sbase), "s"(lds_base) : "memory"); }
DI void glds3(const void* sbase, unsigned voff, unsigned lds_base) {
  unsigned sv;
  asm volatile("s_mov_b32 %0, m0\n\ts_mov_b32 m0, %3\n\ts_nop 0\n\tglobal_load_lds_dwordx4 %1, %2\n\tglobal_load_lds_dwordx4 %1, %2 offset:1024\n\tglobal_load_lds_dwordx4 %1, %2 offset:2048\n\ts_mov_b32 m0, %0"
               : "=&s"(sv) : "v"(voff), "s"(sbase), "s"(lds_base) : "memory"); }
DI void gldsn(const void* sbase, unsigned voff, unsigned lds_base) {
  asm volatile("s_mov_b32 m0, %2\n\ts_nop 0\n\tglobal_load_lds_dwordx4 %0, %1" :: "v"(voff), "s"(sbase), "s"(lds_base) : "memory"); }
DI void gldsn_dev(const void* sbase, unsigned voff, unsigned lds_base) {
  asm volatile("s_mov_b32 m0, %2\n\ts_nop 0\n\tglobal_load_lds_dwordx4 %0, %1 sc1" :: "v"(voff), "s"(sbase), "s"(lds_base) : "memory"); }
DI u32x4 ldtr(const LAS char* p, int off0, int off1) {
  typedef LAS s16x4* lp;
  s16x4 a = __builtin_amdgcn_ds_read_tr16_b64_v4i16((lp)(p + off0)), b = __builtin_amdgcn_ds_read_tr16_b64_v4i16((lp)(p + off1));
  u32x2 x = __builtin_bit_cast(u32x2, a), y = __builtin_bit_cast(u32x2, b);
  return u32x4{x[0], x[1], y[0], y[1]};
}
DI void st16_wt(char* base, unsigned off, u32x4 w, unsigned bytes) {
  const __amdgpu_buffer_rsrc_t rs = __builtin_amdgcn_make_buffer_rsrc((void*)base, 0, bytes, 0x00020000);
  __builtin_amdgcn_raw_buffer_store_b128(w, rs, off, 0, 16);
}
DI u32x4 pair8(u32x2 a, u32x2 b) {
  auto s0 = __builtin_amdgcn_permlane32_swap(a[0], b[0], false, false);
  auto s1 = __builtin_amdgcn_permlane32_swap(a[1], b[1], false, false);
  return u32x4{s0[0], s1[0], s0[1], s1[1]};
}
DI void touch_lines(const void* base, int lines, unsigned& junk, int lane) {
  for (int l0 = 0; l0 < lines; l0 += 64) { const int l = l0 + lane < lines ? l0 + lane : lines - 1;
    asm volatile("global_load_dword %0, %1, %2" : "+v"(junk) : "v"((unsigned)l * 128u), "s"(base) : "memory"); }
}
#define WAITV(n) asm volatile("s_waitcnt vmcnt(" #n ")" ::: "memory")
#define BARRIER() asm volatile("s_waitcnt lgkmcnt(0)\n\ts_barrier" ::: "memory")

constexpr int SEQ = 2048, DM = 640, NFH = 864;
constexpr size_t WS_FLAG = 0;
constexpr size_t WS_QI  = 8192;
constexpr size_t WS_KV  = WS_QI + 2621440;
constexpr size_t WS_Z1  = WS_KV + 196608;
constexpr size_t WS_Z2R = WS_Z1 + 3538944;
constexpr size_t WS_Z2N = WS_Z2R + 3538944;
constexpr size_t WS_AO  = WS_Z2N + 4194304;
constexpr size_t WS_W3  = WS_AO + 6160384;
constexpr size_t WS_END = WS_W3 + 8 * 94 * 2560;
constexpr int KV_CHUNK = 12288, KV_VOFF = 6144;
struct Params {
  const char *x, *wq, *wk, *wv, *wo, *win, *wsp, *bsp, *wout;
  char* ws; char* out;
};

constexpr int K1_STAGE = 36864, K1_NS = 4, K1_XT = 16384;
constexpr int K1_CSP = K1_NS * K1_STAGE;
constexpr int K1_CSF = K1_CSP + 2560;
constexpr int K1_STP = K1_CSF + 640;
constexpr int K1_LDS = K1_STP + 1024;
constexpr float QSCALE = 0.22360679774997896f * 1.4426950408889634f;
enum { T_Q = 0, T_K = 1, T_V = 2, T_Z1 = 3, T_Z2 = 4, T_E = 5 };
DI void k1_slot(int cg, int j, int& type, int& idx) {
  if (j < 3 || (j == 3 && cg < 6)) { const int z = cg < 6 ? cg * 4 + j : 24 + (cg - 6) * 3 + j; type = z < 27 ? T_Z1 : T_Z2; idx = z < 27 ? z : z - 27; }
  else if (cg == 15) { type = j == 3 ? T_K : T_V; idx = 0; }
  else if (cg < 4) { type = T_E; idx = 0; }
  else if (cg < 6) { type = T_Q; idx = cg - 4; }
  else { type = T_Q; idx = 2 + (cg - 6) * 2 + (j - 3); }
}
DI f32x2 gelu2(f32x2 x) {
  const f32x2 c1 = {-0.10294324f, -0.10294324f}, c0 = {-2.3022082f, -2.3022082f}, one = {1.f, 1.f};
  const f32x2 a = x * __builtin_elementwise_fma(x * x, c1, c0);
  const f32x2 d = f32x2{__builtin_amdgcn_exp2f(a[0]), __builtin_amdgcn_exp2f(a[1])} + one;
  return x * f32x2{__builtin_amdgcn_rcpf(d[0]), __builtin_amdgcn_rcpf(d[1])};
}
DI void k1_loader(const Params& p, char* lds_, int rb, int cg, int lw) {
  const int lane = threadIdx.x & 63;
  __builtin_amdgcn_s_setprio(3);
  const unsigned l0 = (unsigned)(uintptr_t)lds_;
  const char* xb = p.x + (size_t)rb * 128 * 1280;
  unsigned xv[4];
#pragma unroll
  for (int i = 0; i < 4; i++) { const unsigned row = 8 * (4 * lw + i) + (lane >> 3); xv[i] = row * 1280 + (((lane & 7) ^ ((row >> 1) & 7)) << 4); }
#pragma unroll
  for (int i = 0; i < 4; i++) gldsn(xb, xv[i], l0 + (4 * lw + i) * 1024);
  const char* base[5]; unsigned stride[5], voff[5];
  const uintptr_t pwq = (uintptr_t)p.wq, pwin = (uintptr_t)p.win, pwk = (uintptr_t)p.wk, pwv = (uintptr_t)p.wv;
#pragma unroll
  for (int j = 0; j < 5; j++) {
    int type, idx; k1_slot(cg, j, type, idx);
    const unsigned rowp = 16 * lw + (lane >> 2), ch = (lane & 3) * 16;
    const bool kvs = j >= 3 && cg == 15, isq = type == T_Q || type == T_E;
    const uintptr_t a = kvs ? (j == 3 ? pwk : pwv) : isq ? pwq + idx * 64 : pwin + (type == T_Z2 ? 1728 : 0) + idx * 64;
    base[j] = (const char*)a;
    stride[j] = kvs ? 2560u : isq ? 64u * 1280u : 64u * 3456u;
    const unsigned o = (lw < 2 ? lw : 2) * 1024 + lane * 16;
    voff[j] = kvs ? (o < 2544u ? o : 2544u) : rowp * (isq ? 1280u : 3456u) + ch;
  }
  auto issue = [&](int st) {
    const unsigned slot = l0 + (st & 3) * K1_STAGE;
    if (st > 0) {
#pragma unroll
      for (int i = 0; i < 4; i++) gldsn(xb + st * 128, xv[i], slot + (4 * lw + i) * 1024);
    }
#pragma unroll
    for (int j = 0; j < 5; j++) gldsn(base[j] + (size_t)st * stride[j], voff[j], slot + K1_XT + j * 4096 + lw * 1024);
  };
  issue(0); issue(1); issue(2);
  WAITV(18);
  BARRIER();
#pragma unroll 1
  for (int st = 0; st < 10; st++) {
    if (st < 8) WAITV(9); else WAITV(0);
    BARRIER();
    if (st + 3 < 10) issue(st + 3);
  }
}
template <int ROLE>
DI void k1_body(const Params& p, char* lds_, int rb, int cg, int tt) {
  constexpr int NJ = ROLE == 0 ? 3 : 2, J0 = ROLE == 0 ? 0 : 3;
  const int lane = threadIdx.x & 63, r = lane & 31, h = lane >> 5;
  LAS char* lds = (LAS char*)lds_;
  const bool kv = ROLE == 1 && cg == 15;
  int stype[NJ], sidx[NJ];
#pragma unroll
  for (int j = 0; j < NJ; j++) k1_slot(cg, J0 + j, stype[j], sidx[j]);
#define K1_BPRIO 1
  if (ROLE == 1) __builtin_amdgcn_s_setprio(K1_BPRIO);
  const int row = 32 * tt + r;
  unsigned xa[4], wa[4];
  const unsigned pitch = kv ? 40 : 64;
  const int blk = (lane >> 4) & 1, q = (lane & 15) >> 2, pp = lane & 3;
#pragma unroll
  for (int i = 0; i < 4; i++) {
    const unsigned ks = (i + tt) & 3;
    xa[i] = row * 128 + ((32 * ks + 16 * h) ^ (16 * ((row >> 1) & 7)));
    wa[i] = K1_XT + J0 * 4096 + (16 * ks + 8 * h + q) * pitch + 32 * blk + 8 * pp;
  }
  f32x16 acc[NJ];
#pragma unroll
  for (int j = 0; j < NJ; j++) acc[j] = f32x16{};
  f32x4 cs[NJ];
  float sq[4] = {0.f, 0.f, 0.f, 0.f}, sm[4] = {0.f, 0.f, 0.f, 0.f};
#pragma unroll
  for (int j = 0; j < NJ; j++) cs[j] = f32x4{};
  const unsigned ONES = 0x3F803F80u;
  const unsigned selw = ((lane & 15) == ((lane >> 4) & 1)) ? ONES : 0u;
  const u32x4 SEL = {selw, selw, selw, selw};
  auto mainloop = [&](auto KVC) {
    constexpr bool KV = decltype(KVC)::value;
    u32x4 xf[4] = {}, wf[4][NJ] = {};
    auto rd = [&](LAS char* sl, int i) {
      xf[i] = LDS128(sl + xa[i]);
#pragma unroll
      for (int j = 0; j < NJ; j++) wf[i][j] = ldtr(sl + wa[i], j * 4096, j * 4096 + (KV ? 160 : 256));
    };
    auto mm = [&](int i, LAS char* nsl, int ni) {
#pragma unroll
      for (int j = 0; j < NJ; j++) {
        if (KV && j == 1) acc[j] = MFMA32(xf[i], wf[i][j], acc[j]);
        else acc[j] = MFMA32(wf[i][j], xf[i], acc[j]);
        SB();
        if (j == 0) { xf[ni] = LDS128(nsl + xa[ni]); wf[ni][0] = ldtr(nsl + wa[ni], 0, KV ? 160 : 256); }
        else { wf[ni][j] = ldtr(nsl + wa[ni], j * 4096, j * 4096 + (KV ? 160 : 256)); }
        SB();
      }
      if (ROLE == 1) {
#pragma unroll
        for (int e = 0; e < 4; e++) { const float lo = bflo(xf[i][e]), hi = bfhi(xf[i][e]); sqacc(sq[(2 * e) & 3], lo); sqacc(sq[(2 * e + 1) & 3], hi); addacc(sm[e & 1], lo); addacc(sm[2 + (e & 1)], hi); }
        SB();
      }
    };
    BARRIER();
    rd(lds, 0); rd(lds, 1);
#pragma unroll 1
    for (int st = 0; st < 10; st++) {
      LAS char* sl = lds + (st & 3) * K1_STAGE;
      LAS char* sn = lds + ((st + 1) & 3) * K1_STAGE;
      mm(0, sl, 2);
      mm(1, sl, 3);
#pragma unroll
      for (int j = 0; j < NJ; j++) cs[j] = MFMA16(SEL, wf[0][j], cs[j]);
      SB();
      BARRIER();
      mm(2, sn, 0);
      mm(3, sn, 1);
      SB();
    }
  };
  if (kv) mainloop(std::true_type{}); else mainloop(std::false_type{});
  LAS float* csp = (LAS float*)(lds + K1_CSP);
  LAS float* csf = (LAS float*)(lds + K1_CSF);
  LAS f32x2* stt = (LAS f32x2*)(lds + K1_STP);
  if (lane < 16) {
#pragma unroll
    for (int j = 0; j < NJ; j++) { csp[tt * 160 + (J0 + j) * 32 + lane] = cs[j][0]; csp[tt * 160 + (J0 + j) * 32 + 16 + lane] = cs[j][1]; }
  }
  if (ROLE == 1) {
    const float sqs = half_sum((sq[0] + sq[1]) + (sq[2] + sq[3]));
    const float su = half_sum((sm[0] + sm[1]) + (sm[2] + sm[3]));
    const float mu = su * (1.f / 640.f), var = fmaxf(sqs * (1.f / 640.f) - mu * mu, 0.f), rs = __builtin_amdgcn_rsqf(var + 1e-5f);
    if (h == 0) stt[row] = f32x2{mu * rs, rs};
  }
  BARRIER();
  { const int c = threadIdx.x; if (c < 160) csf[c] = (csp[c] + csp[160 + c]) + (csp[320 + c] + csp[480 + c]); }
  const f32x2 st_ = stt[row];
  BARRIER();
  const int tb = rb * 4 + tt;
#pragma unroll
  for (int j = 0; j < NJ; j++) {
    const int type = stype[j];
    if (type == T_E) continue;
    if (ROLE == 1 && type == T_V) {
      const float csum = csf[(J0 + j) * 32 + r];
      char* vb = p.ws + WS_KV + (size_t)rb * KV_CHUNK + KV_VOFF + tt * 1536 + h * 384 + r * 16;
#pragma unroll
      for (int s = 0; s < 2; s++) {
        float v[8];
#pragma unroll
        for (int i = 0; i < 8; i++) { const f32x2 t = stt[32 * tt + 16 * s + 8 * (i >> 2) + 4 * h + (i & 3)]; v[i] = t[1] * acc[j][8 * s + i] - t[0] * csum; }
        u32x4 w = {cvtpk(v[0], v[1]), cvtpk(v[2], v[3]), cvtpk(v[4], v[5]), cvtpk(v[6], v[7])};
        if (r == 20) w = u32x4{ONES, ONES, ONES, ONES};
        if (r > 20) w = u32x4{0u, 0u, 0u, 0u};
        if (r < 24) *(u32x4*)(vb + s * 768) = w;
      }
      continue;
    }
    f32x2 v[8];
    const f32x2 rs2 = {st_[1], st_[1]}, nm2 = {-st_[0], -st_[0]};
#pragma unroll
    for (int g = 0; g < 4; g++) {
      const f32x4 c4 = *(LAS f32x4*)(csf + (J0 + j) * 32 + 8 * g + 4 * h);
      v[2 * g] = __builtin_elementwise_fma(f32x2{c4[0], c4[1]}, nm2, f32x2{acc[j][4 * g], acc[j][4 * g + 1]} * rs2);
      v[2 * g + 1] = __builtin_elementwise_fma(f32x2{c4[2], c4[3]}, nm2, f32x2{acc[j][4 * g + 2], acc[j][4 * g + 3]} * rs2);
    }
    if (ROLE == 1 && type == T_K) {
      char* kb = p.ws + WS_KV + (size_t)rb * KV_CHUNK + tt * 1536 + r * 16 + h * 8;
      *(u32x2*)(kb) = u32x2{cvtpk(v[0][0], v[0][1]), cvtpk(v[1][0], v[1][1])};
      *(u32x2*)(kb + 512) = u32x2{cvtpk(v[2][0], v[2][1]), cvtpk(v[3][0], v[3][1])};
      *(u32x2*)(kb + 1024) = h ? u32x2{0x00003F80u, 0u} : u32x2{cvtpk(v[4][0], v[4][1]), cvtpk(v[5][0], v[5][1])};
      continue;
    }
    unsigned off; char* base;
    if (type == T_Q) {
      base = p.ws + WS_QI; off = ((tb * 80 + sidx[j] * 4 + h) * 32 + r) * 16;
#pragma unroll
      for (int i = 0; i < 8; i++) v[i] *= f32x2{QSCALE, QSCALE};
    } else {
      base = p.ws + (type == T_Z1 ? WS_Z1 : WS_Z2R); off = ((tb * 108 + sidx[j] * 4 + h) * 32 + r) * 16;
#pragma unroll
      for (int i = 0; i < 8; i++) v[i] = gelu2(v[i]);
    }
#pragma unroll
    for (int g = 0; g < 4; g += 2)
      st16_wt(base, off + g * 512, pair8(u32x2{cvtpk(v[2 * g][0], v[2 * g][1]), cvtpk(v[2 * g + 1][0], v[2 * g + 1][1])}, u32x2{cvtpk(v[2 * g + 2][0], v[2 * g + 2][1]), cvtpk(v[2 * g + 3][0], v[2 * g + 3][1])}), 3538944);
  }
}
__global__ void __launch_bounds__(768, 3) k1_proj(Params p) {
  extern __shared__ __attribute__((aligned(16))) char lds[];
  unsigned junk = 0;
  if (threadIdx.x < 64) {
    unsigned long long pc; int codelen;
    asm volatile("s_getpc_b64 %0\nhipk_pc_mark:\n\ts_mov_b32 %1, hipk_text_mark-hipk_pc_mark" : "=s"(pc), "=s"(codelen));
    touch_lines((const char*)(pc & ~127ull), codelen > 0 ? codelen >> 7 : 1, junk, threadIdx.x);
  }
  const int b = blockIdx.x, x = b & 7, j = b >> 3;
  const int rb = (x & 1) * 8 + (j & 7), cg = (x >> 1) * 4 + (j >> 3);
  const int wid = RFL(threadIdx.x >> 6);
  if (b == 0 && threadIdx.x < 512) ((u32x4*)(p.ws + WS_FLAG))[threadIdx.x] = u32x4{0u, 0u, 0u, 0u};
  if (wid < 4) k1_body<0>(p, lds, rb, cg, wid); else if (wid < 8) k1_body<1>(p, lds, rb, cg, wid - 4); else k1_loader(p, lds, rb, cg, wid - 8);
  asm volatile("" :: "v"(junk));
}

typedef __attribute__((address_space(1))) unsigned gu32;
constexpr int K2_LDS = 81920;
constexpr float SM_OFF = 100.f;
#define EX(x) __builtin_amdgcn_exp2f(x)
#define AUNIT(Sc, Sn, qn0, qn1, Oc, Op, LD0, LD1, LD2, LD3) do { \
    Sc[0] = EX(Sc[0]); Sc[1] = EX(Sc[1]); SB(); \
    Sn = MFMA32(k0, qn0, zero16); SB(); LD0; SB(); \
    Sc[2] = EX(Sc[2]); Sc[3] = EX(Sc[3]); SB(); p1[2] = cvtpk(e12, e13); p1[3] = cvtpk(e14, e15); SB(); \
    Sc[4] = EX(Sc[4]); Sc[5] = EX(Sc[5]); SB(); \
    Op = MFMA32(v1, p1, Op); SB(); LD1; SB(); \
    Sc[6] = EX(Sc[6]); Sc[7] = EX(Sc[7]); SB(); p0[0] = cvtpk(Sc[0], Sc[1]); p0[1] = cvtpk(Sc[2], Sc[3]); SB(); \
    Sc[8] = EX(Sc[8]); Sc[9] = EX(Sc[9]); SB(); \
    Sn = MFMA32(k1, qn1, Sn); SB(); LD2; SB(); \
    Sc[10] = EX(Sc[10]); Sc[11] = EX(Sc[11]); SB(); p0[2] = cvtpk(Sc[4], Sc[5]); p0[3] = cvtpk(Sc[6], Sc[7]); SB(); \
    e12 = EX(Sc[12]); e13 = EX(Sc[13]); SB(); \
    Oc = MFMA32(v0, p0, Oc); SB(); LD3; SB(); \
    e14 = EX(Sc[14]); e15 = EX(Sc[15]); SB(); p1[0] = cvtpk(Sc[8], Sc[9]); p1[1] = cvtpk(Sc[10], Sc[11]); SB(); \
  } while (0)
#define ATILE(kn, v1c, v0n) do { \
    AUNIT(sA, sB, qB0, qB1, oA, oB, k0 = LDS128(kn), v1 = LDS128(v1c), k1 = LDS128((kn) + k1d), (void)0); \
    AUNIT(sB, sA, qA0, qA1, oB, oA, (void)0, (void)0, (void)0, v0 = LDS128(v0n)); \
  } while (0)
DI float max3f(float a, float b, float c) { return __builtin_fmaxf(__builtin_fmaxf(a, b), c); }
DI float max16(const f32x16& s) {
  const float a = max3f(max3f(s[0], s[1], s[2]), s[3], s[4]), b = max3f(max3f(s[5], s[6], s[7]), s[8], s[9]), c = max3f(max3f(s[10], s[11], s[12]), s[13], s[14]);
  return max3f(max3f(a, b, c), s[15], s[15]);
}
DI f32x16 attn_exact(const char* kv, u32x4 q0, u32x4 q1, int r, int h) {
  const f32x16 zero16 = {};
  q1[2] = 0u;
  f32x16 o = {}; float m = -3.0e38f;
  const int rr = r < 21 ? r : 21;
#pragma unroll 1
  for (int t = 0; t < 64; t++) {
    const char* cb = kv + (size_t)(t >> 2) * KV_CHUNK;
    const char* kb = cb + (t & 3) * 1536 + r * 16;
    const u32x4 k0 = *(const u32x4*)(kb + h * 512), k1 = *(const u32x4*)(kb + 1024);
    const char* vb = cb + KV_VOFF + (t & 3) * 1536 + h * 384 + rr * 16;
    const u32x4 v0 = *(const u32x4*)(vb), v1 = *(const u32x4*)(vb + 768);
    f32x16 s = MFMA32(k0, q0, zero16); s = MFMA32(k1, q1, s);
    float mx = s[0];
#pragma unroll
    for (int i = 1; i < 16; i++) mx = fmaxf(mx, s[i]);
    const float mn = fmaxf(m, half_max(mx)), a = EX(m - mn);
    m = mn;
#pragma unroll
    for (int i = 0; i < 16; i++) { o[i] *= a; s[i] = EX(s[i] - mn); }
    const u32x4 p0 = {cvtpk(s[0], s[1]), cvtpk(s[2], s[3]), cvtpk(s[4], s[5]), cvtpk(s[6], s[7])};
    const u32x4 p1 = {cvtpk(s[8], s[9]), cvtpk(s[10], s[11]), cvtpk(s[12], s[13]), cvtpk(s[14], s[15])};
    o = MFMA32(v0, p0, o); o = MFMA32(v1, p1, o);
  }
  return o;
}
DI void attn_role(const Params& p, char* lds_, int wg) {
  const int tid = threadIdx.x, lane = tid & 63, r = lane & 31, h = lane >> 5;
  const int wid = RFL(tid >> 6);
  const int pi = wid & 3, kh = wid >> 2;
  const int tb = wg >> 2, headA = (wg & 3) * 8 + 2 * pi;
  const unsigned l0 = (unsigned)(uintptr_t)lds_;
  LAS char* lds = (LAS char*)lds_;
  const char* kvg = p.ws + WS_KV;
  const char* kvsrc = kvg + (size_t)kh * 8 * KV_CHUNK + pi * 3072;
  const unsigned voff = lane * 16;
  const unsigned ring = l0 + kh * 3 * KV_CHUNK + pi * 3072;
#define APRIO 2
  __builtin_amdgcn_s_setprio(APRIO);
  glds3(kvsrc, voff, ring);
  u32x4 qA0, qA1, qB0, qB1;
  {
    const char* qrow = p.ws + WS_QI + (size_t)tb * 80 * 32 * 16;
    auto poff = [&](int c4) { return (unsigned)(r * 16 + (c4 >> 1) * 512 + (c4 & 1) * 8); };
    u32x2 a0, a1, a2, b0, b1, b2;
    const int cA = headA * 5, cB = cA + 5;
    asm volatile("global_load_dwordx2 %0, %6, %12\n\tglobal_load_dwordx2 %1, %7, %12\n\tglobal_load_dwordx2 %2, %8, %12\n\t"
                 "global_load_dwordx2 %3, %9, %12\n\tglobal_load_dwordx2 %4, %10, %12\n\tglobal_load_dwordx2 %5, %11, %12"
                 : "=&v"(a0), "=&v"(a1), "=&v"(a2), "=&v"(b0), "=&v"(b1), "=&v"(b2)
                 : "v"(poff(cA + 2 * h)), "v"(poff(cA + 2 * h + 1)), "v"(poff(cA + 4)), "v"(poff(cB + 2 * h)), "v"(poff(cB + 2 * h + 1)), "v"(poff(cB + 4)), "s"(qrow) : "memory");
    glds3(kvsrc + KV_CHUNK, voff, ring + KV_CHUNK);
    asm volatile("s_waitcnt vmcnt(3)\n\ts_barrier" : "+v"(a0), "+v"(a1), "+v"(a2), "+v"(b0), "+v"(b1), "+v"(b2) :: "memory");
    qA0 = u32x4{a0[0], a0[1], a1[0], a1[1]}; qB0 = u32x4{b0[0], b0[1], b1[0], b1[1]};
    qA1 = h ? u32x4{0u, 0u, 0u, 0u} : u32x4{a2[0], a2[1], 0u, 0u};
    qB1 = h ? u32x4{0u, 0u, 0u, 0u} : u32x4{b2[0], b2[1], 0u, 0u};
  }
  const f32x16 zero16 = {};
  const int k1d = 1024 - h * 512;
  const int rr = r < 21 ? r : 21;
  u32x4 k0, k1; f32x16 sA, sB;
  {
    LAS char* kb0 = lds + h * 512 + r * 16;
    k0 = LDS128(kb0); k1 = LDS128(kb0 + k1d);
    const u32x4 k2 = LDS128(kb0 + 1536), k3 = LDS128(kb0 + 1536 + k1d);
    sA = MFMA32(k0, qA0, zero16); sB = MFMA32(k0, qB0, zero16); sA = MFMA32(k1, qA1, sA); sB = MFMA32(k1, qB1, sB);
    f32x16 tA = MFMA32(k2, qA0, zero16), tB = MFMA32(k2, qB0, zero16); tA = MFMA32(k3, qA1, tA); tB = MFMA32(k3, qB1, tB);
    const float mA = half_max(fmaxf(max16(sA), max16(tA))), mB = half_max(fmaxf(max16(sB), max16(tB)));
    if (h == 0) { qA1[2] = cvtpk(-(mA + SM_OFF), 0.f); qB1[2] = cvtpk(-(mB + SM_OFF), 0.f); }
  }
  LAS char* kbase = lds + kh * 3 * KV_CHUNK + h * 512 + r * 16;
  LAS char* vbase = lds + kh * 3 * KV_CHUNK + KV_VOFF + h * 384 + rr * 16;
  k0 = LDS128(kbase); k1 = LDS128(kbase + k1d);
  sA = MFMA32(k0, qA0, zero16); sA = MFMA32(k1, qA1, sA);
  u32x4 v0 = LDS128(vbase), v1 = v0, p0 = {0u, 0u, 0u, 0u}, p1 = {0u, 0u, 0u, 0u};
  f32x16 oA = {}, oB = {};
  float e12 = 0.f, e13 = 0.f, e14 = 0.f, e15 = 0.f;
  int sc = 0;
#pragma unroll 1
  for (int c = 0; c < 8; c++) {
    const int sn = sc == 2 * KV_CHUNK ? 0 : sc + KV_CHUNK, sp = sn == 2 * KV_CHUNK ? 0 : sn + KV_CHUNK;
    if (c + 2 < 8) glds3(kvsrc + (size_t)(c + 2) * KV_CHUNK, voff, ring + sp);
    LAS char* kc = kbase + sc; LAS char* kn = kbase + sn; LAS char* vc = vbase + sc; LAS char* vn = vbase + sn;
    ATILE(kc + 1 * 1536, vc + 1 * 768, vc + 2 * 768);
    ATILE(kc + 2 * 1536, vc + 3 * 768, vc + 4 * 768);
    if (c == 0) asm volatile("s_waitcnt vmcnt(3)\n\ts_barrier" ::: "memory");
    ATILE(kc + 3 * 1536, vc + 5 * 768, vc + 6 * 768);
    ATILE(kn, vc + 7 * 768, vn);
    asm volatile("s_waitcnt vmcnt(0) lgkmcnt(0)\n\ts_barrier" ::: "memory");
    sc = sn;
  }
  p1[2] = cvtpk(e12, e13); p1[3] = cvtpk(e14, e15);
  oB = MFMA32(v1, p1, oB);
  LAS f32x4* ex = (LAS f32x4*)(lds + (pi * 2 + kh) * 3072 + lane * 16);
  LAS const f32x4* exr = (LAS const f32x4*)(lds + (pi * 2 + (kh ^ 1)) * 3072 + lane * 16);
  f32x16 o;
  if (kh == 0) { o = oA;
#pragma unroll
    for (int g = 0; g < 3; g++) ex[g * 64] = f32x4{oB[4 * g], oB[4 * g + 1], oB[4 * g + 2], oB[4 * g + 3]};
  } else { o = oB;
#pragma unroll
    for (int g = 0; g < 3; g++) ex[g * 64] = f32x4{oA[4 * g], oA[4 * g + 1], oA[4 * g + 2], oA[4 * g + 3]};
  }
  BARRIER();
#pragma unroll
  for (int g = 0; g < 3; g++) { f32x4 t = exr[g * 64]; o[4 * g] += t[0]; o[4 * g + 1] += t[1]; o[4 * g + 2] += t[2]; o[4 * g + 3] += t[3]; }
  {
    float z[4];
#pragma unroll
    for (int i = 0; i < 4; i++) z[i] = __builtin_fmaf(o[i], 0.f, __builtin_fmaf(o[4 + i], 0.f, o[8 + i] * 0.f));
    const float chk = (z[0] + z[1]) + (z[2] + z[3]);
    const bool bad = !(chk == 0.f) || (h == 1 && !(o[8] < 1.0e37f));
    if (__builtin_expect(__any(bad), 0)) o = attn_exact(kvg, kh ? qB0 : qA0, kh ? qB1 : qA1, r, h);
  }
  auto lr = __builtin_amdgcn_permlane32_swap(__float_as_uint(o[8]), __float_as_uint(o[8]), false, false);
  const float il = __builtin_amdgcn_rcpf(__uint_as_float(lr[1]));
  LAS char* ost = lds + 32768;
  const int hl = 2 * pi + kh;
#pragma unroll
  for (int g = 0; g < 3; g++) {
    if (g < 2 || h == 0) {
      const int kk = hl * 20 + (2 * g + h) * 4;
      *(LAS u32x2*)(ost + ((kk >> 3) * 32 + r) * 16 + ((kk >> 2) & 1) * 8) = u32x2{cvtpk(o[4 * g] * il, o[4 * g + 1] * il), cvtpk(o[4 * g + 2] * il, o[4 * g + 3] * il)};
    }
  }
  BARRIER();
#pragma unroll
  for (int k = 0; k < 2; k++) {
    const int i = tid + 512 * k;
    if (i < 640) st16_wt(p.ws + WS_AO, (((wg & 3) * 20 + (i >> 5)) * 2048 + tb * 32 + (i & 31)) * 16, *(LAS u32x4*)(ost + i * 16), 6160384);
  }
  asm volatile("s_waitcnt vmcnt(0)" ::: "memory");
  BARRIER();
  if (tid == 0) __hip_atomic_fetch_add((gu32*)(p.ws + WS_FLAG) + 1024 + (tb >> 1) * 16, 1u, __ATOMIC_RELAXED, __HIP_MEMORY_SCOPE_AGENT);
}

DI int fb_start(int fb) { return (fb >> 2) * 216 + (fb & 3) * 56; }
constexpr int SP_STAGE = 24576, SP_ZOFF = 16384;

constexpr int F_ACT = 8192, F_STAGE = F_ACT + 10240;
DI void fin_role(const Params& p, char* lds_, int b, int wid) {
  const int tid = threadIdx.x, lane = tid & 63, r = lane & 31, h = lane >> 5;
  const unsigned l0 = (unsigned)(uintptr_t)lds_;
  LAS char* lds = (LAS char*)lds_;
  const int x = b & 7, jj = b >> 3;
  const int tbk = (x >> 1) * 4 + (jj & 3), fb = (x & 1) * 8 + (jj >> 2);
  const int tb = 2 * tbk + (fb >> 3), nb = fb & 7;
  gu32* cnt = (gu32*)(p.ws + WS_FLAG);
  BARRIER();
  if (tid == 0) __hip_atomic_fetch_add(cnt + 512 + tbk * 16, 1u, __ATOMIC_RELAXED, __HIP_MEMORY_SCOPE_AGENT);
  if (wid >= 4) {
    const int lw = wid - 4;
    const char* ab = p.ws + WS_AO + (size_t)tb * 64 * 16;
    const char* wb = p.ws + WS_W3 + (size_t)nb * 94 * 2560;
    const unsigned lv = lane * 16;
    auto ks0 = [&](int s) { return s < 14 ? 40 + 4 * s : 4 * (s - 14); };
    auto issue_w = [&](int s) {
      const int p0 = lw < 2 ? 3 * lw : 2 + 2 * lw;
      const char* src = wb + (size_t)ks0(s) * 2560;
      const unsigned dst = l0 + (s & 3) * F_STAGE + F_ACT;
#pragma unroll
      for (int i = 0; i < 3; i++) { const int pc = (lw >= 2 && i == 2) ? p0 : p0 + i; gldsn_dev(src + (s == 13 && pc >= 5 ? 0 : pc * 1024), lv, dst + pc * 1024); }
    };
    auto issue_a = [&](int s) {
#pragma unroll
      for (int i = 0; i < 2; i++) gldsn_dev(ab + (size_t)(2 * ks0(s) + (s == 13 && lw >= 2 ? 0 : 2 * lw) + i) * 32768, lv, l0 + (s & 3) * F_STAGE + (2 * lw + i) * 1024);
    };
    auto await = [&](gu32* word, unsigned want) {
      unsigned spins = 0;
      while ((unsigned)RFL(__hip_atomic_load(word, __ATOMIC_RELAXED, __HIP_MEMORY_SCOPE_AGENT)) < want) { if (++spins > (1u << 22)) break; __builtin_amdgcn_s_sleep(1); }
      asm volatile("" ::: "memory");
    };
    await(cnt + 1536 + nb * 16, 94u);
    issue_w(0); issue_w(1); issue_w(2);
    await(cnt + 512 + tbk * 16, 16u);
    issue_a(0); issue_a(1); issue_a(2);
#pragma unroll 1
    for (int s = 0; s < 24; s++) {
      if (s == 0 || s == 14) WAITV(4); else if (s == 1 || s == 15) WAITV(7); else if (s == 12) WAITV(8); else if (s == 13) WAITV(6);
      else if (s == 22) WAITV(5); else if (s == 23) WAITV(0); else WAITV(10);
      BARRIER();
      if (s + 3 < 24) {
        if (s < 11 || s > 13) issue_a(s + 3);
        issue_w(s + 3);
      }
      if (s == 13) { await(cnt + 1024 + tb * 16, 8u); issue_a(14); issue_a(15); issue_a(16); }
    }
    return;
  }
#define FPRIO 1
  __builtin_amdgcn_s_setprio(FPRIO);
  const int tt = wid & 1, two = wid < 2;
  u32x4 xr[3];
#pragma unroll
  for (int k = 0; k < 3; k++) { const int i = tid + 256 * k < 640 ? tid + 256 * k : 0, row = i / 10, c = i - row * 10;
    xr[k] = *(const u32x4*)(p.x + ((size_t)(tb * 64 + row) * 640 + nb * 80 + c * 8) * 2); }
  const unsigned wa = F_ACT + (two ? lane : 128 + h * 16 + (r & 15)) * 16;
  const unsigned aa = h * 1024 + (32 * tt + r) * 16;
  f32x16 acc = {}, acc2 = {};
#pragma unroll 1
  for (int s = 0; s < 24; s++) {
    BARRIER();
    LAS char* sl = lds + (s & 3) * F_STAGE;
    u32x4 af[4], wf[4], wg[4];
#pragma unroll
    for (int ks = 0; ks < 4; ks++) { af[ks] = LDS128(sl + aa + ks * 2048); wf[ks] = LDS128(sl + wa + ks * 2560); }
    if (two) {
#pragma unroll
      for (int ks = 0; ks < 4; ks++) wg[ks] = LDS128(sl + wa + 1024 + ks * 2560);
      acc = MFMA32(wf[0], af[0], acc); acc2 = MFMA32(wg[0], af[0], acc2); acc = MFMA32(wf[1], af[1], acc); acc2 = MFMA32(wg[1], af[1], acc2);
      if (s != 13) { acc = MFMA32(wf[2], af[2], acc); acc2 = MFMA32(wg[2], af[2], acc2); acc = MFMA32(wf[3], af[3], acc); acc2 = MFMA32(wg[3], af[3], acc2); }
    } else {
      acc = MFMA32(wf[0], af[0], acc); acc = MFMA32(wf[1], af[1], acc);
      if (s != 13) { acc = MFMA32(wf[2], af[2], acc); acc = MFMA32(wf[3], af[3], acc); }
    }
  }
  BARRIER();
  LAS float* ot = (LAS float*)lds;
  constexpr int OP = 84;
  const int n0 = two ? 0 : 64;
#pragma unroll
  for (int g = 0; g < 4; g++) {
    const int n = n0 + 8 * g + 4 * h;
    if (n < 80) *(LAS f32x4*)(ot + (32 * tt + r) * OP + n) = f32x4{acc[4 * g], acc[4 * g + 1], acc[4 * g + 2], acc[4 * g + 3]};
    if (two) *(LAS f32x4*)(ot + (32 * tt + r) * OP + 32 + n) = f32x4{acc2[4 * g], acc2[4 * g + 1], acc2[4 * g + 2], acc2[4 * g + 3]};
  }
  BARRIER();
#pragma unroll
  for (int k = 0; k < 3; k++) {
    const int i = tid + 256 * k;
    if (i < 640) {
      const int row = i / 10, c = i - row * 10;
      const size_t go = ((size_t)(tb * 64 + row) * 640 + nb * 80 + c * 8) * 2;
      const f32x4 a = *(LAS f32x4*)(ot + row * OP + c * 8), a2 = *(LAS f32x4*)(ot + row * OP + c * 8 + 4);
      const u32x4 w = {cvtpk(a[0] + bflo(xr[k][0]), a[1] + bfhi(xr[k][0])), cvtpk(a[2] + bflo(xr[k][1]), a[3] + bfhi(xr[k][1])),
                       cvtpk(a2[0] + bflo(xr[k][2]), a2[1] + bfhi(xr[k][2])), cvtpk(a2[2] + bflo(xr[k][3]), a2[3] + bfhi(xr[k][3]))};
      st16_wt(p.out, (unsigned)go, w, 2621440);
    }
  }
}
DI void sp_role(const Params& p, char* lds_, int b) {
  const int wid = RFL(threadIdx.x >> 6);
  __builtin_amdgcn_s_setprio(3);
  const int tid = threadIdx.x, lane = tid & 63, r = lane & 31, h = lane >> 5;
  const unsigned l0 = (unsigned)(uintptr_t)lds_;
  LAS char* lds = (LAS char*)lds_;
  const int x = b & 7, jj = b >> 3;
  const int tbk = (x >> 1) * 4 + (jj & 3), fb = (x & 1) * 8 + (jj >> 2);
  gu32* cnt = (gu32*)(p.ws + WS_FLAG);
  if (wid >= 4) {
    const int lw = wid - 4;
    const char* wb = p.wsp + (size_t)tbk * 128 * 4096;
    unsigned wv4[4];
#pragma unroll
    for (int i = 0; i < 4; i++) { const unsigned row = 8 * (4 * lw + i) + (lane >> 3); wv4[i] = row * 4096 + (((lane & 7) ^ ((row >> 1) & 7)) << 4); }
    const char* zb = p.ws + WS_Z2N + (size_t)fb * 32 * 8192 + lw * 2048;
    const unsigned zv = lane * 16;
    auto issue_w = [&](int st) {
#pragma unroll
      for (int i = 0; i < 4; i++) gldsn(wb + st * 128, wv4[i], l0 + (st % 3) * SP_STAGE + (4 * lw + i) * 1024);
    };
    auto issue_z = [&](int st) {
      asm volatile("s_mov_b32 m0, %2\n\ts_nop 0\n\tglobal_load_lds_dwordx4 %0, %1 sc1\n\tglobal_load_lds_dwordx4 %0, %1 offset:1024 sc1"
                   :: "v"(zv), "s"(zb + (size_t)st * 8192), "s"(l0 + (st % 3) * SP_STAGE + SP_ZOFF + lw * 2048) : "memory");
    };
    BARRIER(); BARRIER();
    issue_w(0); issue_w(1);
    BARRIER();
    {
      const int u = b * 3 + lw - 1;
      if (lw > 0 && u < 752) {
        const int ks = u >> 3, nb = u & 7;
        const uintptr_t pwo = (uintptr_t)p.wo, pwout = (uintptr_t)p.wout;
        const GAS char* src = (const GAS char*)(ks < 40 ? pwo + (size_t)ks * 16 * 1280 : pwout + (size_t)(ks - 40) * 16 * 1280) + nb * 160;
        LAS char* sc = lds + 3 * SP_STAGE + (lw - 1) * 2560;
#pragma unroll
        for (int i = 0; i < 3; i++) {
          const int c = lane + 64 * i, rw = c / 10, cc = c - rw * 10;
          if (c < 160) *(LAS u32x4*)(sc + c * 16) = *(const GAS u32x4*)(src + rw * 1280 + cc * 16);
        }
        asm volatile("s_waitcnt vmcnt(0) lgkmcnt(0)" ::: "memory");
        LAS char* ta = sc + (8 * h + ((lane & 15) >> 2)) * 160 + 32 * ((lane >> 4) & 1) + 8 * (lane & 3);
        const unsigned dst = (nb * 94 + ks) * 2560;
        const u32x4 f0 = ldtr(ta, 0, 640), f1 = ldtr(ta, 64, 64 + 640), f2 = ldtr(ta, 128, 128 + 640);
        st16_wt(p.ws + WS_W3, dst + lane * 16, f0, 8 * 94 * 2560);
        st16_wt(p.ws + WS_W3, dst + 1024 + lane * 16, f1, 8 * 94 * 2560);
        if (r < 16) st16_wt(p.ws + WS_W3, dst + 2048 + (h * 16 + r) * 16, f2, 8 * 94 * 2560);
        asm volatile("s_waitcnt vmcnt(0) lgkmcnt(0)" ::: "memory");
        if (lane == 0) __hip_atomic_fetch_add(cnt + 1536 + nb * 16, 1u, __ATOMIC_RELAXED, __HIP_MEMORY_SCOPE_AGENT);
      }
    }
    if (lw > 0) {
      const char* src = p.wsp + (unsigned)RFL(b * 32768 + (lw - 1) * 12288);
      const int n2 = lw == 3 ? 4 : 6;
      for (int i = 0; i < n2; i++)
        asm volatile("s_mov_b32 m0, %2\n\ts_nop 0\n\tglobal_load_lds_dwordx4 %0, %1\n\tglobal_load_lds_dwordx4 %0, %1 offset:1024"
                     :: "v"(zv), "s"(src + i * 2048), "s"(l0 + 3 * SP_STAGE - 2048) : "memory");
    }
    if (lw == 0) {
      {
      __builtin_amdgcn_s_sleep(24);
      unsigned spins = 0;
      for (;;) {
        const unsigned c = __hip_atomic_load(cnt + (lane & 31) * 16, __ATOMIC_RELAXED, __HIP_MEMORY_SCOPE_AGENT);
        if (__all(c >= 8u)) break;
        if (++spins > (1u << 21)) break;
        __builtin_amdgcn_s_sleep(6);
      }
      }
      const char* z00 = p.ws + WS_Z2N + (size_t)fb * 32 * 8192;
#pragma unroll
      for (int i = 0; i < 4; i++)
        asm volatile("s_mov_b32 m0, %2\n\ts_nop 0\n\tglobal_load_lds_dwordx4 %0, %1 sc1\n\tglobal_load_lds_dwordx4 %0, %1 offset:1024 sc1\n\tglobal_load_lds_dwordx4 %0, %1 offset:2048 sc1\n\tglobal_load_lds_dwordx4 %0, %1 offset:3072 sc1"
                     :: "v"(zv), "s"(z00 + i * 4096), "s"(l0 + (i >> 1) * SP_STAGE + SP_ZOFF + (i & 1) * 4096) : "memory");
      WAITV(8);
    }
#pragma unroll 1
    for (int st = 0; st < 32; st++) {
      if (st == 0) { if (lw) WAITV(0); } else if (st < 31) WAITV(6); else WAITV(0);
      BARRIER();
      if (st + 2 < 32) { issue_w(st + 2); issue_z(st + 2); }
    }
    fin_role(p, lds_, b, wid);
    return;
  }
#define LNPRIO 3
  __builtin_amdgcn_s_setprio(LNPRIO);
  {
    LAS float* red = (LAS float*)(lds + K2_LDS - 256);
    const bool act = tid < 216;
    const int g8 = act ? tid >> 1 : 107, half = tid & 1;
    const char* src = p.ws + WS_Z2R + ((size_t)((b >> 2) * 108 + g8) * 32 + (b & 3) * 8 + 4 * half) * 16;
    u32x4 raw[4];
#pragma unroll
    for (int i = 0; i < 4; i++) raw[i] = *(const u32x4*)(src + i * 16);
    const unsigned ONES = act ? 0x3F803F80u : 0u;
    LAS float* part = (LAS float*)(lds + 2 * SP_STAGE);
#pragma unroll
    for (int t = 0; t < 4; t++) {
      float su = 0.f, sq = 0.f;
#pragma unroll
      for (int e = 0; e < 4; e++) { const unsigned a = act ? raw[t][e] : 0u; su = dot2(a, ONES, su); sq = dot2(a, a, sq); }
      part[(2 * (4 * half + t)) * 128 + (tid >> 1)] = su;
      part[(2 * (4 * half + t) + 1) * 128 + (tid >> 1)] = sq;
    }
    BARRIER();
    {
      float s4[4];
#pragma unroll
      for (int k = 0; k < 4; k++) { const f32x2 a = *(LAS f32x2*)(part + (4 * wid + k) * 128 + lane * 2); s4[k] = a[0] + a[1]; }
#pragma unroll
      for (int k = 0; k < 4; k++) s4[k] = wave_sum(s4[k]);
      if (lane < 2) {
        const float su = lane ? s4[2] : s4[0], sq = lane ? s4[3] : s4[1];
        const float mu = su * (1.f / 864.f), var = fmaxf(sq * (1.f / 864.f) - mu * mu, 0.f), rs = __builtin_amdgcn_rsqf(var + 1e-5f);
        ((LAS f32x2*)red)[2 * wid + lane] = f32x2{rs, -mu * rs};
      }
    }
    BARRIER();
    LAS char* tr = lds + 2 * SP_STAGE + 8192;
    {
      float v[4][8];
#pragma unroll
      for (int t = 0; t < 4; t++) {
        const f32x2 c = ((LAS f32x2*)red)[4 * half + t];
#pragma unroll
        for (int e = 0; e < 4; e++) { v[t][2 * e] = __builtin_fmaf(bflo(raw[t][e]), c[0], c[1]); v[t][2 * e + 1] = __builtin_fmaf(bfhi(raw[t][e]), c[0], c[1]); }
      }
      if (act) {
#pragma unroll
        for (int i = 0; i < 8; i++) *(LAS u32x2*)(tr + (8 * g8 + i) * 16 + half * 8) = u32x2{cvtpk(v[0][i], v[1][i]), cvtpk(v[2][i], v[3][i])};
      }
    }
    asm volatile("s_waitcnt lgkmcnt(0)" ::: "memory");
    {
      const __amdgpu_buffer_rsrc_t rsrc = __builtin_amdgcn_make_buffer_rsrc((void*)(p.ws + WS_Z2N), 0, 4194304, 0x00020000);
      const unsigned sbase = (((b >> 3) * 4 + ((b >> 1) & 3)) * 2 + (b & 1)) * 1024;
#pragma unroll
      for (int i = 0; i < 4; i++) {
        const int f = 256 * wid + 64 * i + lane;
        if (f < 864) {
          const u32x4 w = *(LAS u32x4*)(tr + f * 16);
          const int q4 = (f >= 216) + (f >= 432) + (f >= 648), rem = f - q4 * 216, sub = (rem >= 56) + (rem >= 112) + (rem >= 168);
          const unsigned off = (unsigned)(q4 * 4 + sub) * (32u * 8192u) + sbase + (rem - sub * 56) * 16;
          __builtin_amdgcn_raw_buffer_store_b128(w, rsrc, off, 0, 16);
        }
      }
    }
    if (tid < 160) {
      const int fbw = tid < 96 ? (tid >> 3) / 3 * 4 + (tid >> 3) % 3 : ((tid - 96) >> 4) * 4 + 3, row = tid < 96 ? 56 + (tid & 7) : 48 + (tid & 15);
      st16_wt(p.ws + WS_Z2N, (unsigned)fbw * (32u * 8192u) + (((b >> 3) * 4 + ((b >> 1) & 3)) * 2 + (b & 1)) * 1024 + row * 16, u32x4{0u, 0u, 0u, 0u}, 4194304);
    }
    asm volatile("s_waitcnt vmcnt(0)" ::: "memory");
    BARRIER();
    if (tid == 0) __hip_atomic_fetch_add(cnt + (b >> 3) * 16, 1u, __ATOMIC_RELAXED, __HIP_MEMORY_SCOPE_AGENT);
  }
  const unsigned row = 32 * wid + r;
  unsigned wa[4];
#pragma unroll
  for (int ks = 0; ks < 4; ks++) wa[ks] = row * 128 + ((32 * ks + 16 * h) ^ (16 * ((row >> 1) & 7)));
  const unsigned za = SP_ZOFF + h * 1024 + r * 16;
  f32x16 acc0 = {}, acc1 = {};
#pragma unroll 1
  for (int st = 0; st < 32; st++) {
    BARRIER();
    LAS char* sl = lds + (st % 3) * SP_STAGE;
    u32x4 wf[4], z0[4], z1[4];
#pragma unroll
    for (int ks = 0; ks < 4; ks++) { wf[ks] = LDS128(sl + wa[ks]); z0[ks] = LDS128(sl + za + ks * 2048); z1[ks] = LDS128(sl + za + ks * 2048 + 512); }
#pragma unroll
    for (int ks = 0; ks < 4; ks++) { acc0 = MFMA32(z0[ks], wf[ks], acc0); acc1 = MFMA32(z1[ks], wf[ks], acc1); }
  }
  const int tok = tbk * 128 + row, f0 = fb_start(fb), width = (fb & 3) == 3 ? 48 : 56;
  const float bias = bflo(*(const unsigned short*)(p.bsp + tok * 2));
  const char* z1b = p.ws + WS_Z1 + ((size_t)((tok >> 5) * 108 + (f0 >> 3)) * 32 + r) * 16 + h * 8;
#pragma unroll
  for (int j = 0; j < 2; j++) {
    const f32x16& a = j ? acc1 : acc0;
    u32x2 w[4];
#pragma unroll
    for (int g = 0; g < 4; g++) {
      const int fl = 32 * j + 8 * g;
      u32x2 z = {0u, 0u};
      if (fl < width) z = *(const u32x2*)(z1b + (4 * j + g) * 512);
      w[g] = u32x2{cvtpk(bflo(z[0]) * (a[4 * g] + bias), bfhi(z[0]) * (a[4 * g + 1] + bias)), cvtpk(bflo(z[1]) * (a[4 * g + 2] + bias), bfhi(z[1]) * (a[4 * g + 3] + bias))};
    }
#pragma unroll
    for (int g = 0; g < 4; g += 2) {
      const u32x4 w8 = pair8(w[g], w[g + 1]);
      const int fl = 32 * j + 8 * (g + h);
      if (fl < width) st16_wt(p.ws + WS_AO, ((80 + ((f0 + fl) >> 3)) * 2048 + tok) * 16, w8, 6160384);
    }
  }
  asm volatile("s_waitcnt vmcnt(0)" ::: "memory");
  fin_role(p, lds_, b, wid);
}
__global__ void __launch_bounds__(512, 4) k2_mix(Params p) {
  extern __shared__ __attribute__((aligned(16))) char lds[];
  unsigned junk = 0;
  if (blockIdx.x >= 256) sp_role(p, lds, blockIdx.x - 256); else attn_role(p, lds, blockIdx.x);
  asm volatile("hipk_text_mark:" :: "v"(junk));
}

extern "C" void kernel_launch(void* const* d_in, const int* in_sizes, int n_in, void* d_out, int out_size, void* d_ws, size_t ws_size, hipStream_t stream) {
  Params p;
  p.x = (const char*)d_in[0]; p.wq = (const char*)d_in[1]; p.wk = (const char*)d_in[2]; p.wv = (const char*)d_in[3]; p.wo = (const char*)d_in[4];
  p.win = (const char*)d_in[6]; p.wsp = (const char*)d_in[7]; p.bsp = (const char*)d_in[8]; p.wout = (const char*)d_in[9];
  p.ws = (char*)d_ws; p.out = (char*)d_out;
  (void)hipFuncSetAttribute((const void*)k1_proj, hipFuncAttributeMaxDynamicSharedMemorySize, K1_LDS);
  (void)hipFuncSetAttribute((const void*)k2_mix, hipFuncAttributeMaxDynamicSharedMemorySize, K2_LDS);
  k1_proj<<<256, 768, K1_LDS, stream>>>(p);
  k2_mix<<<512, 512, K2_LDS, stream>>>(p);
}
```

```cpp
#include <hip/hip_runtime.h>
#include <stdint.h>
#include <type_traits>
typedef __bf16 bf16x8 __attribute__((ext_vector_type(8)));
typedef __bf16 bf16x2 __attribute__((ext_vector_type(2)));
typedef short s16x4 __attribute__((ext_vector_type(4)));
typedef float f32x16 __attribute__((ext_vector_type(16)));
typedef float f32x4 __attribute__((ext_vector_type(4)));
typedef float f32x2 __attribute__((ext_vector_type(2)));
typedef unsigned u32x4 __attribute__((ext_vector_type(4)));
typedef unsigned u32x2 __attribute__((ext_vector_type(2)));
#define DI __device__ __forceinline__
#define LAS __attribute__((address_space(3)))
#define GAS __attribute__((address_space(1)))
#define SB() __builtin_amdgcn_sched_barrier(0)
#define RFL(x) __builtin_amdgcn_readfirstlane(x)
#define MFMA32(a, b, c) __builtin_amdgcn_mfma_f32_32x32x16_bf16(__builtin_bit_cast(bf16x8, a), __builtin_bit_cast(bf16x8, b), c, 0, 0, 0)
#define MFMA16(a, b, c) __builtin_amdgcn_mfma_f32_16x16x32_bf16(__builtin_bit_cast(bf16x8, a), __builtin_bit_cast(bf16x8, b), c, 0, 0, 0)
DI f32x4 mfma4sq(unsigned x0, unsigned x1, f32x4 c) { u32x2 v = {x0, x1}; s16x4 a = __builtin_bit_cast(s16x4, v); return __builtin_amdgcn_mfma_f32_4x4x4bf16_1k(a, a, c, 0, 0, 0); }
#define MFMA4(x0, x1, c) mfma4sq(x0, x1, c)
DI f32x16 mfma8(u32x2 a, u32x2 b, f32x16 c) { return __builtin_amdgcn_mfma_f32_32x32x8bf16_1k(__builtin_bit_cast(s16x4, a), __builtin_bit_cast(s16x4, b), c, 0, 0, 0); }
#define MFMA8(a, b, c) mfma8(a, b, c)
#define LDS64(p) (*(const LAS u32x2*)(p))
#define LDS128(p) (*(const LAS u32x4*)(p))
DI unsigned cvtpk(float lo, float hi) { f32x2 v = {lo, hi}; bf16x2 b = __builtin_convertvector(v, bf16x2); return __builtin_bit_cast(unsigned, b); }
DI float bflo(unsigned u) { return __uint_as_float(u << 16); }
DI float bfhi(unsigned u) { return __uint_as_float(u & 0xffff0000u); }
DI void sqacc(float& a, float x) { asm("v_fmac_f32_e32 %0, %1, %1" : "+v"(a) : "v"(x)); }
DI void addacc(float& a, float x) { asm("v_add_f32_e32 %0, %0, %1" : "+v"(a) : "v"(x)); }
DI float dot2(unsigned a, unsigned b, float c) { return __builtin_amdgcn_fdot2_f32_bf16(__builtin_bit_cast(bf16x2, a), __builtin_bit_cast(bf16x2, b), c, false); }
DI float half_sum(float m) { auto r = __builtin_amdgcn_permlane32_swap(__float_as_uint(m), __float_as_uint(m), false, false); return __uint_as_float(r[0]) + __uint_as_float(r[1]); }
DI float half_max(float m) { auto r = __builtin_amdgcn_permlane32_swap(__float_as_uint(m), __float_as_uint(m), false, false); return fmaxf(__uint_as_float(r[0]), __uint_as_float(r[1])); }
template <int CTRL, int RMASK> DI float dpp_add(float v) { return v + __uint_as_float(__builtin_amdgcn_update_dpp(0u, __float_as_uint(v), CTRL, RMASK, 0xf, true)); }
DI float wave_sum(float v) {
  v = dpp_add<0x111, 0xf>(v); v = dpp_add<0x112, 0xf>(v); v = dpp_add<0x114, 0xf>(v); v = dpp_add<0x118, 0xf>(v);
  v = dpp_add<0x142, 0xa>(v); v = dpp_add<0x143, 0xc>(v);
  return __uint_as_float(__builtin_amdgcn_readlane(__float_as_uint(v), 63));
}
DI void glds1(const void* sbase, unsigned voff, unsigned lds_base) {
  unsigned sv;
  asm volatile("s_mov_b32 %0, m0\n\ts_mov_b32 m0, %3\n\ts_nop 0\n\tglobal_load_lds_dwordx4 %1, %2\n\ts_mov_b32 m0, %0"
               : "=&s"(sv) : "v"(voff), "s"(sbase), "s"(lds_base) : "memory"); }
DI void glds3(const void* sbase, unsigned voff, unsigned lds_base) {
  unsigned sv;
  asm volatile("s_mov_b32 %0, m0\n\ts_mov_b32 m0, %3\n\ts_nop 0\n\tglobal_load_lds_dwordx4 %1, %2\n\tglobal_load_lds_dwordx4 %1, %2 offset:1024\n\tglobal_load_lds_dwordx4 %1, %2 offset:2048\n\ts_mov_b32 m0, %0"
               : "=&s"(sv) : "v"(voff), "s"(sbase), "s"(lds_base) : "memory"); }
DI void gldsn(const void* sbase, unsigned voff, unsigned lds_base) {
  asm volatile("s_mov_b32 m0, %2\n\ts_nop 0\n\tglobal_load_lds_dwordx4 %0, %1" :: "v"(voff), "s"(sbase), "s"(lds_base) : "memory"); }
DI void gldsn_dev(const void* sbase, unsigned voff, unsigned lds_base) {
  asm volatile("s_mov_b32 m0, %2\n\ts_nop 0\n\tglobal_load_lds_dwordx4 %0, %1 sc1" :: "v"(voff), "s"(sbase), "s"(lds_base) : "memory"); }
DI u32x4 ldtr(const LAS char* p, int off0, int off1) {
  typedef LAS s16x4* lp;
  s16x4 a = __builtin_amdgcn_ds_read_tr16_b64_v4i16((lp)(p + off0)), b = __builtin_amdgcn_ds_read_tr16_b64_v4i16((lp)(p + off1));
  u32x2 x = __builtin_bit_cast(u32x2, a), y = __builtin_bit_cast(u32x2, b);
  return u32x4{x[0], x[1], y[0], y[1]};
}
DI void st16_wt(char* base, unsigned off, u32x4 w, unsigned bytes) {
  const __amdgpu_buffer_rsrc_t rs = __builtin_amdgcn_make_buffer_rsrc((void*)base, 0, bytes, 0x00020000);
  __builtin_amdgcn_raw_buffer_store_b128(w, rs, off, 0, 16);
}
DI u32x4 pair8(u32x2 a, u32x2 b) {
  auto s0 = __builtin_amdgcn_permlane32_swap(a[0], b[0], false, false);
  auto s1 = __builtin_amdgcn_permlane32_swap(a[1], b[1], false, false);
  return u32x4{s0[0], s1[0], s0[1], s1[1]};
}
DI void touch_lines(const void* base, int lines, unsigned& junk, int lane) {
  for (int l0 = 0; l0 < lines; l0 += 64) { const int l = l0 + lane < lines ? l0 + lane : lines - 1;
    asm volatile("global_load_dword %0, %1, %2" : "+v"(junk) : "v"((unsigned)l * 128u), "s"(base) : "memory"); }
}
#define WAITV(n) asm volatile("s_waitcnt vmcnt(" #n ")" ::: "memory")
#define BARRIER() asm volatile("s_waitcnt lgkmcnt(0)\n\ts_barrier" ::: "memory")

constexpr int SEQ = 2048, DM = 640, NFH = 864;
constexpr size_t WS_FLAG = 0;
constexpr size_t WS_QI  = 8192;
constexpr size_t WS_KV  = WS_QI + 2621440;
constexpr size_t WS_Z1  = WS_KV + 196608;
constexpr size_t WS_Z2R = WS_Z1 + 3538944;
constexpr size_t WS_Z2N = WS_Z2R + 3538944;
constexpr size_t WS_AO  = WS_Z2N + 4194304;
constexpr size_t WS_W3  = WS_AO + 6160384;
constexpr size_t WS_END = WS_W3 + 8 * 94 * 2560;
constexpr int KV_CHUNK = 12288, KV_VOFF = 6144;
struct Params {
  const char *x, *wq, *wk, *wv, *wo, *win, *wsp, *bsp, *wout;
  char* ws; char* out;
};

constexpr int K1_STAGE = 36864, K1_NS = 4, K1_XT = 16384;
constexpr int K1_CSP = K1_NS * K1_STAGE;
constexpr int K1_CSF = K1_CSP + 2560;
constexpr int K1_STP = K1_CSF + 640;
constexpr int K1_LDS = K1_STP + 1024;
constexpr float QSCALE = 0.22360679774997896f * 1.4426950408889634f;
enum { T_Q = 0, T_K = 1, T_V = 2, T_Z1 = 3, T_Z2 = 4, T_E = 5 };
DI void k1_slot(int cg, int j, int& type, int& idx) {
  if (j < 3 || (j == 3 && cg < 6)) { const int z = cg < 6 ? cg * 4 + j : 24 + (cg - 6) * 3 + j; type = z < 27 ? T_Z1 : T_Z2; idx = z < 27 ? z : z - 27; }
  else if (cg == 15) { type = j == 3 ? T_K : T_V; idx = 0; }
  else if (cg < 4) { type = T_E; idx = 0; }
  else if (cg < 6) { type = T_Q; idx = cg - 4; }
  else { type = T_Q; idx = 2 + (cg - 6) * 2 + (j - 3); }
}
DI f32x2 gelu2(f32x2 x) {
  const f32x2 c1 = {-0.10294324f, -0.10294324f}, c0 = {-2.3022082f, -2.3022082f}, one = {1.f, 1.f};
  const f32x2 a = x * __builtin_elementwise_fma(x * x, c1, c0);
  const f32x2 d = f32x2{__builtin_amdgcn_exp2f(a[0]), __builtin_amdgcn_exp2f(a[1])} + one;
  return x * f32x2{__builtin_amdgcn_rcpf(d[0]), __builtin_amdgcn_rcpf(d[1])};
}
DI void k1_loader(const Params& p, char* lds_, int rb, int cg, int lw) {
  const int lane = threadIdx.x & 63;
  __builtin_amdgcn_s_setprio(3);
  const unsigned l0 = (unsigned)(uintptr_t)lds_;
  const char* xb = p.x + (size_t)rb * 128 * 1280;
  unsigned xv[4];
#pragma unroll
  for (int i = 0; i < 4; i++) { const unsigned row = 8 * (4 * lw + i) + (lane >> 3); xv[i] = row * 1280 + (((lane & 7) ^ ((row >> 1) & 7)) << 4); }
#pragma unroll
  for (int i = 0; i < 4; i++) gldsn(xb, xv[i], l0 + (4 * lw + i) * 1024);
  const char* base[5]; unsigned stride[5], voff[5];
  const uintptr_t pwq = (uintptr_t)p.wq, pwin = (uintptr_t)p.win, pwk = (uintptr_t)p.wk, pwv = (uintptr_t)p.wv;
#pragma unroll
  for (int j = 0; j < 5; j++) {
    int type, idx; k1_slot(cg, j, type, idx);
    const unsigned rowp = 16 * lw + (lane >> 2), ch = (lane & 3) * 16;
    const bool kvs = j >= 3 && cg == 15, isq = type == T_Q || type == T_E;
    const uintptr_t a = kvs ? (j == 3 ? pwk : pwv) : isq ? pwq + idx * 64 : pwin + (type == T_Z2 ? 1728 : 0) + idx * 64;
    base[j] = (const char*)a;
    stride[j] = kvs ? 2560u : isq ? 64u * 1280u : 64u * 3456u;
    const unsigned o = (lw < 2 ? lw : 2) * 1024 + lane * 16;
    voff[j] = kvs ? (o < 2544u ? o : 2544u) : rowp * (isq ? 1280u : 3456u) + ch;
  }
  auto issue = [&](int st) {
    const unsigned slot = l0 + (st & 3) * K1_STAGE;
    if (st > 0) {
#pragma unroll
      for (int i = 0; i < 4; i++) gldsn(xb + st * 128, xv[i], slot + (4 * lw + i) * 1024);
    }
#pragma unroll
    for (int j = 0; j < 5; j++) gldsn(base[j] + (size_t)st * stride[j], voff[j], slot + K1_XT + j * 4096 + lw * 1024);
  };
  issue(0); issue(1); issue(2);
  WAITV(18);
  BARRIER();
#pragma unroll 1
  for (int st = 0; st < 10; st++) {
    if (st < 8) WAITV(9); else WAITV(0);
    BARRIER();
    if (st + 3 < 10) issue(st + 3);
  }
}
template <int ROLE>
DI void k1_body(const Params& p, char* lds_, int rb, int cg, int tt) {
  constexpr int NJ = ROLE == 0 ? 3 : 2, J0 = ROLE == 0 ? 0 : 3;
  const int lane = threadIdx.x & 63, r = lane & 31, h = lane >> 5;
  LAS char* lds = (LAS char*)lds_;
  const bool kv = ROLE == 1 && cg == 15;
  int stype[NJ], sidx[NJ];
#pragma unroll
  for (int j = 0; j < NJ; j++) k1_slot(cg, J0 + j, stype[j], sidx[j]);
#define K1_BPRIO 1
  if (ROLE == 1) __builtin_amdgcn_s_setprio(K1_BPRIO);
  const int row = 32 * tt + r;
  unsigned xa[4], wa[4];
  const unsigned pitch = kv ? 40 : 64;
  const int blk = (lane >> 4) & 1, q = (lane & 15) >> 2, pp = lane & 3;
#pragma unroll
  for (int i = 0; i < 4; i++) {
    const unsigned ks = (i + tt) & 3;
    xa[i] = row * 128 + ((32 * ks + 16 * h) ^ (16 * ((row >> 1) & 7)));
    wa[i] = K1_XT + J0 * 4096 + (16 * ks + 8 * h + q) * pitch + 32 * blk + 8 * pp;
  }
  f32x16 acc[NJ];
#pragma unroll
  for (int j = 0; j < NJ; j++) acc[j] = f32x16{};
  f32x4 cs[NJ];
  float sq[4] = {0.f, 0.f, 0.f, 0.f}, sm[4] = {0.f, 0.f, 0.f, 0.f};
#pragma unroll
  for (int j = 0; j < NJ; j++) cs[j] = f32x4{};
  const unsigned ONES = 0x3F803F80u;
  const unsigned selw = ((lane & 15) == ((lane >> 4) & 1)) ? ONES : 0u;
  const u32x4 SEL = {selw, selw, selw, selw};
  auto mainloop = [&](auto KVC) {
    constexpr bool KV = decltype(KVC)::value;
    u32x4 xf[4] = {}, wf[4][NJ] = {};
    auto rd = [&](LAS char* sl, int i) {
      xf[i] = LDS128(sl + xa[i]);
#pragma unroll
      for (int j = 0; j < NJ; j++) wf[i][j] = ldtr(sl + wa[i], j * 4096, j * 4096 + (KV ? 160 : 256));
    };
    auto mm = [&](int i, LAS char* nsl, int ni) {
#pragma unroll
      for (int j = 0; j < NJ; j++) {
        if (KV && j == 1) acc[j] = MFMA32(xf[i], wf[i][j], acc[j]);
        else acc[j] = MFMA32(wf[i][j], xf[i], acc[j]);
        SB();
        if (j == 0) { xf[ni] = LDS128(nsl + xa[ni]); wf[ni][0] = ldtr(nsl + wa[ni], 0, KV ? 160 : 256); }
        else { wf[ni][j] = ldtr(nsl + wa[ni], j * 4096, j * 4096 + (KV ? 160 : 256)); }
        SB();
      }
      if (ROLE == 1) {
#pragma unroll
        for (int e = 0; e < 4; e++) { const float lo = bflo(xf[i][e]), hi = bfhi(xf[i][e]); sqacc(sq[(2 * e) & 3], lo); sqacc(sq[(2 * e + 1) & 3], hi); addacc(sm[e & 1], lo); addacc(sm[2 + (e & 1)], hi); }
        SB();
      }
    };
    BARRIER();
    rd(lds, 0); rd(lds, 1);
#pragma unroll 1
    for (int st = 0; st < 10; st++) {
      LAS char* sl = lds + (st & 3) * K1_STAGE;
      LAS char* sn = lds + ((st + 1) & 3) * K1_STAGE;
      mm(0, sl, 2);
      mm(1, sl, 3);
#pragma unroll
      for (int j = 0; j < NJ; j++) cs[j] = MFMA16(SEL, wf[0][j], cs[j]);
      SB();
      BARRIER();
      mm(2, sn, 0);
      mm(3, sn, 1);
      SB();
    }
  };
  if (kv) mainloop(std::true_type{}); else mainloop(std::false_type{});
  LAS float* csp = (LAS float*)(lds + K1_CSP);
  LAS float* csf = (LAS float*)(lds + K1_CSF);
  LAS f32x2* stt = (LAS f32x2*)(lds + K1_STP);
  if (lane < 16) {
#pragma unroll
    for (int j = 0; j < NJ; j++) { csp[tt * 160 + (J0 + j) * 32 + lane] = cs[j][0]; csp[tt * 160 + (J0 + j) * 32 + 16 + lane] = cs[j][1]; }
  }
  if (ROLE == 1) {
    const float sqs = half_sum((sq[0] + sq[1]) + (sq[2] + sq[3]));
    const float su = half_sum((sm[0] + sm[1]) + (sm[2] + sm[3]));
    const float mu = su * (1.f / 640.f), var = fmaxf(sqs * (1.f / 640.f) - mu * mu, 0.f), rs = __builtin_amdgcn_rsqf(var + 1e-5f);
    if (h == 0) stt[row] = f32x2{mu * rs, rs};
  }
  BARRIER();
  { const int c = threadIdx.x; if (c < 160) csf[c] = (csp[c] + csp[160 + c]) + (csp[320 + c] + csp[480 + c]); }
  const f32x2 st_ = stt[row];
  BARRIER();
  const int tb = rb * 4 + tt;
#pragma unroll
  for (int j = 0; j < NJ; j++) {
    const int type = stype[j];
    if (type == T_E) continue;
    if (ROLE == 1 && type == T_V) {
      const float csum = csf[(J0 + j) * 32 + r];
      char* vb = p.ws + WS_KV + (size_t)rb * KV_CHUNK + KV_VOFF + tt * 1536 + h * 384 + r * 16;
#pragma unroll
      for (int s = 0; s < 2; s++) {
        float v[8];
#pragma unroll
        for (int i = 0; i < 8; i++) { const f32x2 t = stt[32 * tt + 16 * s + 8 * (i >> 2) + 4 * h + (i & 3)]; v[i] = t[1] * acc[j][8 * s + i] - t[0] * csum; }
        u32x4 w = {cvtpk(v[0], v[1]), cvtpk(v[2], v[3]), cvtpk(v[4], v[5]), cvtpk(v[6], v[7])};
        if (r == 20) w = u32x4{ONES, ONES, ONES, ONES};
        if (r > 20) w = u32x4{0u, 0u, 0u, 0u};
        if (r < 24) *(u32x4*)(vb + s * 768) = w;
      }
      continue;
    }
    f32x2 v[8];
    const f32x2 rs2 = {st_[1], st_[1]}, nm2 = {-st_[0], -st_[0]};
#pragma unroll
    for (int g = 0; g < 4; g++) {
      const f32x4 c4 = *(LAS f32x4*)(csf + (J0 + j) * 32 + 8 * g + 4 * h);
      v[2 * g] = __builtin_elementwise_fma(f32x2{c4[0], c4[1]}, nm2, f32x2{acc[j][4 * g], acc[j][4 * g + 1]} * rs2);
      v[2 * g + 1] = __builtin_elementwise_fma(f32x2{c4[2], c4[3]}, nm2, f32x2{acc[j][4 * g + 2], acc[j][4 * g + 3]} * rs2);
    }
    if (ROLE == 1 && type == T_K) {
      char* kb = p.ws + WS_KV + (size_t)rb * KV_CHUNK + tt * 1536;
      *(u32x2*)(kb + r * 16 + h * 8) = u32x2{cvtpk(v[0][0], v[0][1]), cvtpk(v[1][0], v[1][1])};
      *(u32x2*)(kb + 512 + r * 16 + h * 8) = u32x2{cvtpk(v[2][0], v[2][1]), cvtpk(v[3][0], v[3][1])};
      *(u32x2*)(kb + 1024 + h * 256 + r * 8) = h ? u32x2{0x00003F80u, 0u} : u32x2{cvtpk(v[4][0], v[4][1]), cvtpk(v[5][0], v[5][1])};
      continue;
    }
    unsigned off; char* base;
    if (type == T_Q) {
      base = p.ws + WS_QI; off = ((tb * 80 + sidx[j] * 4 + h) * 32 + r) * 16;
#pragma unroll
      for (int i = 0; i < 8; i++) v[i] *= f32x2{QSCALE, QSCALE};
    } else {
      base = p.ws + (type == T_Z1 ? WS_Z1 : WS_Z2R); off = ((tb * 108 + sidx[j] * 4 + h) * 32 + r) * 16;
#pragma unroll
      for (int i = 0; i < 8; i++) v[i] = gelu2(v[i]);
    }
#pragma unroll
    for (int g = 0; g < 4; g += 2)
      st16_wt(base, off + g * 512, pair8(u32x2{cvtpk(v[2 * g][0], v[2 * g][1]), cvtpk(v[2 * g + 1][0], v[2 * g + 1][1])}, u32x2{cvtpk(v[2 * g + 2][0], v[2 * g + 2][1]), cvtpk(v[2 * g + 3][0], v[2 * g + 3][1])}), 3538944);
  }
}
__global__ void __launch_bounds__(768, 3) k1_proj(Params p) {
  extern __shared__ __attribute__((aligned(16))) char lds[];
  unsigned junk = 0;
  if (threadIdx.x < 64) {
    unsigned long long pc; int codelen;
    asm volatile("s_getpc_b64 %0\nhipk_pc_mark:\n\ts_mov_b32 %1, hipk_text_mark-hipk_pc_mark" : "=s"(pc), "=s"(codelen));
    touch_lines((const char*)(pc & ~127ull), codelen > 0 ? codelen >> 7 : 1, junk, threadIdx.x);
  }
  const int b = blockIdx.x, x = b & 7, j = b >> 3;
  const int rb = (x & 1) * 8 + (j & 7), cg = (x >> 1) * 4 + (j >> 3);
  const int wid = RFL(threadIdx.x >> 6);
  if (b == 0 && threadIdx.x < 512) ((u32x4*)(p.ws + WS_FLAG))[threadIdx.x] = u32x4{0u, 0u, 0u, 0u};
  if (wid < 4) k1_body<0>(p, lds, rb, cg, wid); else if (wid < 8) k1_body<1>(p, lds, rb, cg, wid - 4); else k1_loader(p, lds, rb, cg, wid - 8);
  asm volatile("" :: "v"(junk));
}

typedef __attribute__((address_space(1))) unsigned gu32;
constexpr int K2_LDS = 81920;
constexpr float SM_OFF = 100.f;
#define EX(x) __builtin_amdgcn_exp2f(x)
#define AUNIT(Sc, Sn, qn0, qn1, Oc, Op, LD0, LD1, LD2, LD3) do { \
    Sc[0] = EX(Sc[0]); Sc[1] = EX(Sc[1]); SB(); \
    Sn = MFMA32(k0, qn0, zero16); SB(); LD0; SB(); \
    Sc[2] = EX(Sc[2]); Sc[3] = EX(Sc[3]); SB(); p1[2] = cvtpk(e12, e13); p1[3] = cvtpk(e14, e15); SB(); \
    Sc[4] = EX(Sc[4]); Sc[5] = EX(Sc[5]); SB(); \
    Op = MFMA32(v1, p1, Op); SB(); LD1; SB(); \
    Sc[6] = EX(Sc[6]); Sc[7] = EX(Sc[7]); SB(); p0[0] = cvtpk(Sc[0], Sc[1]); p0[1] = cvtpk(Sc[2], Sc[3]); SB(); \
    Sc[8] = EX(Sc[8]); Sc[9] = EX(Sc[9]); SB(); \
    Sn = MFMA8(k1, qn1, Sn); SB(); LD2; SB(); \
    Sc[10] = EX(Sc[10]); Sc[11] = EX(Sc[11]); SB(); p0[2] = cvtpk(Sc[4], Sc[5]); p0[3] = cvtpk(Sc[6], Sc[7]); SB(); \
    e12 = EX(Sc[12]); e13 = EX(Sc[13]); SB(); \
    Oc = MFMA32(v0, p0, Oc); SB(); LD3; SB(); \
    e14 = EX(Sc[14]); e15 = EX(Sc[15]); SB(); p1[0] = cvtpk(Sc[8], Sc[9]); p1[1] = cvtpk(Sc[10], Sc[11]); SB(); \
  } while (0)
#define PSW(a)
#define ATILE(kn, v1c, v0n) do { \
    PSW(0); AUNIT(sA, sB, qB0, qB1, oA, oB, k0 = LDS128(kn), v1 = LDS128(v1c), k1 = LDS64((kn) + k1d), (void)0); \
    PSW(1); AUNIT(sB, sA, qA0, qA1, oB, oA, (void)0, (void)0, (void)0, v0 = LDS128(v0n)); \
  } while (0)
DI float max3f(float a, float b, float c) { return __builtin_fmaxf(__builtin_fmaxf(a, b), c); }
DI float max16(const f32x16& s) {
  const float a = max3f(max3f(s[0], s[1], s[2]), s[3], s[4]), b = max3f(max3f(s[5], s[6], s[7]), s[8], s[9]), c = max3f(max3f(s[10], s[11], s[12]), s[13], s[14]);
  return max3f(max3f(a, b, c), s[15], s[15]);
}
DI f32x16 attn_exact(const char* kv, u32x4 q0, u32x2 q1, int r, int h) {
  const f32x16 zero16 = {};
  if (h) q1 = u32x2{0u, 0u};
  f32x16 o = {}; float m = -3.0e38f;
  const int rr = r < 21 ? r : 21;
#pragma unroll 1
  for (int t = 0; t < 64; t++) {
    const char* cb = kv + (size_t)(t >> 2) * KV_CHUNK;
    const char* kb = cb + (t & 3) * 1536 + r * 16;
    const u32x4 k0 = *(const u32x4*)(kb + h * 512); const u32x2 k1 = *(const u32x2*)(cb + (t & 3) * 1536 + 1024 + h * 256 + r * 8);
    const char* vb = cb + KV_VOFF + (t & 3) * 1536 + h * 384 + rr * 16;
    const u32x4 v0 = *(const u32x4*)(vb), v1 = *(const u32x4*)(vb + 768);
    f32x16 s = MFMA32(k0, q0, zero16); s = MFMA8(k1, q1, s);
    float mx = s[0];
#pragma unroll
    for (int i = 1; i < 16; i++) mx = fmaxf(mx, s[i]);
    const float mn = fmaxf(m, half_max(mx)), a = EX(m - mn);
    m = mn;
#pragma unroll
    for (int i = 0; i < 16; i++) { o[i] *= a; s[i] = EX(s[i] - mn); }
    const u32x4 p0 = {cvtpk(s[0], s[1]), cvtpk(s[2], s[3]), cvtpk(s[4], s[5]), cvtpk(s[6], s[7])};
    const u32x4 p1 = {cvtpk(s[8], s[9]), cvtpk(s[10], s[11]), cvtpk(s[12], s[13]), cvtpk(s[14], s[15])};
    o = MFMA32(v0, p0, o); o = MFMA32(v1, p1, o);
  }
  return o;
}
DI void attn_role(const Params& p, char* lds_, int wg) {
  const int tid = threadIdx.x, lane = tid & 63, r = lane & 31, h = lane >> 5;
  const int wid = RFL(tid >> 6);
  const int pi = wid & 3, kh = wid >> 2;
  const int tb = wg >> 2, headA = (wg & 3) * 8 + 2 * pi;
  const unsigned l0 = (unsigned)(uintptr_t)lds_;
  LAS char* lds = (LAS char*)lds_;
  const char* kvg = p.ws + WS_KV;
  const char* kvsrc = kvg + (size_t)kh * 8 * KV_CHUNK + pi * 3072;
  const unsigned voff = lane * 16;
  const unsigned ring = l0 + kh * 3 * KV_CHUNK + pi * 3072;
#define APRIO 2
  __builtin_amdgcn_s_setprio(APRIO);
  glds3(kvsrc, voff, ring);
  u32x4 qA0, qB0; u32x2 qA1, qB1;
  {
    const char* qrow = p.ws + WS_QI + (size_t)tb * 80 * 32 * 16;
    auto poff = [&](int c4) { return (unsigned)(r * 16 + (c4 >> 1) * 512 + (c4 & 1) * 8); };
    u32x2 a0, a1, a2, b0, b1, b2;
    const int cA = headA * 5, cB = cA + 5;
    asm volatile("global_load_dwordx2 %0, %6, %12\n\tglobal_load_dwordx2 %1, %7, %12\n\tglobal_load_dwordx2 %2, %8, %12\n\t"
                 "global_load_dwordx2 %3, %9, %12\n\tglobal_load_dwordx2 %4, %10, %12\n\tglobal_load_dwordx2 %5, %11, %12"
                 : "=&v"(a0), "=&v"(a1), "=&v"(a2), "=&v"(b0), "=&v"(b1), "=&v"(b2)
                 : "v"(poff(cA + 2 * h)), "v"(poff(cA + 2 * h + 1)), "v"(poff(cA + 4)), "v"(poff(cB + 2 * h)), "v"(poff(cB + 2 * h + 1)), "v"(poff(cB + 4)), "s"(qrow) : "memory");
    glds3(kvsrc + KV_CHUNK, voff, ring + KV_CHUNK);
    asm volatile("s_waitcnt vmcnt(3)\n\ts_barrier" : "+v"(a0), "+v"(a1), "+v"(a2), "+v"(b0), "+v"(b1), "+v"(b2) :: "memory");
    qA0 = u32x4{a0[0], a0[1], a1[0], a1[1]}; qB0 = u32x4{b0[0], b0[1], b1[0], b1[1]};
    qA1 = h ? u32x2{0u, 0u} : a2;
    qB1 = h ? u32x2{0u, 0u} : b2;
  }
  const f32x16 zero16 = {};
  const int k1d = 1024 - h * 256 - r * 8;
  const int rr = r < 21 ? r : 21;
  u32x4 k0; u32x2 k1; f32x16 sA, sB;
  {
    LAS char* kb0 = lds + h * 512 + r * 16;
    k0 = LDS128(kb0); k1 = LDS64(kb0 + k1d);
    const u32x4 k2 = LDS128(kb0 + 1536); const u32x2 k3 = LDS64(kb0 + 1536 + k1d);
    sA = MFMA32(k0, qA0, zero16); sB = MFMA32(k0, qB0, zero16); sA = MFMA8(k1, qA1, sA); sB = MFMA8(k1, qB1, sB);
    f32x16 tA = MFMA32(k2, qA0, zero16), tB = MFMA32(k2, qB0, zero16); tA = MFMA8(k3, qA1, tA); tB = MFMA8(k3, qB1, tB);
    const float mA = half_max(fmaxf(max16(sA), max16(tA))), mB = half_max(fmaxf(max16(sB), max16(tB)));
    if (h == 1) { qA1[0] = cvtpk(-(mA + SM_OFF), 0.f); qB1[0] = cvtpk(-(mB + SM_OFF), 0.f); }
  }
  LAS char* kbase = lds + kh * 3 * KV_CHUNK + h * 512 + r * 16;
  LAS char* vbase = lds + kh * 3 * KV_CHUNK + KV_VOFF + h * 384 + rr * 16;
  k0 = LDS128(kbase); k1 = LDS64(kbase + k1d);
  sA = MFMA32(k0, qA0, zero16); sA = MFMA8(k1, qA1, sA);
  u32x4 v0 = LDS128(vbase), v1 = v0, p0 = {0u, 0u, 0u, 0u}, p1 = {0u, 0u, 0u, 0u};
  f32x16 oA = {}, oB = {};
  float e12 = 0.f, e13 = 0.f, e14 = 0.f, e15 = 0.f;
  int sc = 0;
#pragma unroll 1
  for (int c = 0; c < 8; c++) {
    const int sn = sc == 2 * KV_CHUNK ? 0 : sc + KV_CHUNK, sp = sn == 2 * KV_CHUNK ? 0 : sn + KV_CHUNK;
    if (c + 2 < 8) glds3(kvsrc + (size_t)(c + 2) * KV_CHUNK, voff, ring + sp);
    LAS char* kc = kbase + sc; LAS char* kn = kbase + sn; LAS char* vc = vbase + sc; LAS char* vn = vbase + sn;
    ATILE(kc + 1 * 1536, vc + 1 * 768, vc + 2 * 768);
    ATILE(kc + 2 * 1536, vc + 3 * 768, vc + 4 * 768);
    if (c == 0) asm volatile("s_waitcnt vmcnt(3)\n\ts_barrier" ::: "memory");
    ATILE(kc + 3 * 1536, vc + 5 * 768, vc + 6 * 768);
    ATILE(kn, vc + 7 * 768, vn);
    asm volatile("s_waitcnt vmcnt(0) lgkmcnt(0)\n\ts_barrier" ::: "memory");
    sc = sn;
  }
  p1[2] = cvtpk(e12, e13); p1[3] = cvtpk(e14, e15);
  oB = MFMA32(v1, p1, oB);
  LAS f32x4* ex = (LAS f32x4*)(lds + (pi * 2 + kh) * 3072 + lane * 16);
  LAS const f32x4* exr = (LAS const f32x4*)(lds + (pi * 2 + (kh ^ 1)) * 3072 + lane * 16);
  f32x16 o;
  if (kh == 0) { o = oA;
#pragma unroll
    for (int g = 0; g < 3; g++) ex[g * 64] = f32x4{oB[4 * g], oB[4 * g + 1], oB[4 * g + 2], oB[4 * g + 3]};
  } else { o = oB;
#pragma unroll
    for (int g = 0; g < 3; g++) ex[g * 64] = f32x4{oA[4 * g], oA[4 * g + 1], oA[4 * g + 2], oA[4 * g + 3]};
  }
  BARRIER();
#pragma unroll
  for (int g = 0; g < 3; g++) { f32x4 t = exr[g * 64]; o[4 * g] += t[0]; o[4 * g + 1] += t[1]; o[4 * g + 2] += t[2]; o[4 * g + 3] += t[3]; }
  {
    float z[4];
#pragma unroll
    for (int i = 0; i < 4; i++) z[i] = __builtin_fmaf(o[i], 0.f, __builtin_fmaf(o[4 + i], 0.f, o[8 + i] * 0.f));
    const float chk = (z[0] + z[1]) + (z[2] + z[3]);
    const bool bad = !(chk == 0.f) || (h == 1 && !(o[8] < 1.0e37f));
    if (__builtin_expect(__any(bad), 0)) o = attn_exact(kvg, kh ? qB0 : qA0, kh ? qB1 : qA1, r, h);
  }
  auto lr = __builtin_amdgcn_permlane32_swap(__float_as_uint(o[8]), __float_as_uint(o[8]), false, false);
  const float il = __builtin_amdgcn_rcpf(__uint_as_float(lr[1]));
  LAS char* ost = lds + 32768;
  const int hl = 2 * pi + kh;
#pragma unroll
  for (int g = 0; g < 3; g++) {
    if (g < 2 || h == 0) {
      const int kk = hl * 20 + (2 * g + h) * 4;
      *(LAS u32x2*)(ost + ((kk >> 3) * 32 + r) * 16 + ((kk >> 2) & 1) * 8) = u32x2{cvtpk(o[4 * g] * il, o[4 * g + 1] * il), cvtpk(o[4 * g + 2] * il, o[4 * g + 3] * il)};
    }
  }
  BARRIER();
#pragma unroll
  for (int k = 0; k < 2; k++) {
    const int i = tid + 512 * k;
    if (i < 640) st16_wt(p.ws + WS_AO, (((wg & 3) * 20 + (i >> 5)) * 2048 + tb * 32 + (i & 31)) * 16, *(LAS u32x4*)(ost + i * 16), 6160384);
  }
  asm volatile("s_waitcnt vmcnt(0)" ::: "memory");
  BARRIER();
  if (tid == 0) __hip_atomic_fetch_add((gu32*)(p.ws + WS_FLAG) + 1024 + (tb >> 1) * 16, 1u, __ATOMIC_RELAXED, __HIP_MEMORY_SCOPE_AGENT);
}

DI int fb_start(int fb) { return (fb >> 2) * 216 + (fb & 3) * 56; }
constexpr int SP_STAGE = 24576, SP_ZOFF = 16384;

constexpr int F_ACT = 8192, F_STAGE = F_ACT + 10240;
DI void fin_role(const Params& p, char* lds_, int b, int wid) {
  const int tid = threadIdx.x, lane = tid & 63, r = lane & 31, h = lane >> 5;
  const unsigned l0 = (unsigned)(uintptr_t)lds_;
  LAS char* lds = (LAS char*)lds_;
  const int x = b & 7, jj = b >> 3;
  const int tbk = (x >> 1) * 4 + (jj & 3), fb = (x & 1) * 8 + (jj >> 2);
  const int tb = 2 * tbk + (fb >> 3), nb = fb & 7;
  gu32* cnt = (gu32*)(p.ws + WS_FLAG);
  BARRIER();
  if (tid == 0) __hip_atomic_fetch_add(cnt + 512 + tbk * 16, 1u, __ATOMIC_RELAXED, __HIP_MEMORY_SCOPE_AGENT);
  if (wid >= 4) {
    const int lw = wid - 4;
    const char* ab = p.ws + WS_AO + (size_t)tb * 64 * 16;
    const char* wb = p.ws + WS_W3 + (size_t)nb * 94 * 2560;
    const unsigned lv = lane * 16;
    auto ks0 = [&](int s) { return s < 14 ? 40 + 4 * s : 4 * (s - 14); };
    auto issue_w = [&](int s) {
      const int p0 = lw < 2 ? 3 * lw : 2 + 2 * lw;
      const char* src = wb + (size_t)ks0(s) * 2560;
      const unsigned dst = l0 + (s & 3) * F_STAGE + F_ACT;
#pragma unroll
      for (int i = 0; i < 3; i++) { const int pc = (lw >= 2 && i == 2) ? p0 : p0 + i; gldsn_dev(src + (s == 13 && pc >= 5 ? 0 : pc * 1024), lv, dst + pc * 1024); }
    };
    auto issue_a = [&](int s) {
#pragma unroll
      for (int i = 0; i < 2; i++) gldsn_dev(ab + (size_t)(2 * ks0(s) + (s == 13 && lw >= 2 ? 0 : 2 * lw) + i) * 32768, lv, l0 + (s & 3) * F_STAGE + (2 * lw + i) * 1024);
    };
    auto await = [&](gu32* word, unsigned want) {
      unsigned spins = 0;
      while ((unsigned)RFL(__hip_atomic_load(word, __ATOMIC_RELAXED, __HIP_MEMORY_SCOPE_AGENT)) < want) { if (++spins > (1u << 22)) break; __builtin_amdgcn_s_sleep(1); }
      asm volatile("" ::: "memory");
    };
    await(cnt + 1536 + nb * 16, 94u);
    issue_w(0); issue_w(1); issue_w(2);
    await(cnt + 512 + tbk * 16, 16u);
    issue_a(0); issue_a(1); issue_a(2);
#pragma unroll 1
    for (int s = 0; s < 24; s++) {
      if (s == 0 || s == 14) WAITV(4); else if (s == 1 || s == 15) WAITV(7); else if (s == 12) WAITV(8); else if (s == 13) WAITV(6);
      else if (s == 22) WAITV(5); else if (s == 23) WAITV(0); else WAITV(10);
      BARRIER();
      if (s + 3 < 24) {
        if (s < 11 || s > 13) issue_a(s + 3);
        issue_w(s + 3);
      }
      if (s == 13) { await(cnt + 1024 + tb * 16, 8u); issue_a(14); issue_a(15); issue_a(16); }
    }
    return;
  }
#define FPRIO 1
  __builtin_amdgcn_s_setprio(FPRIO);
  const int tt = wid & 1, two = wid < 2;
  u32x4 xr[3];
#pragma unroll
  for (int k = 0; k < 3; k++) { const int i = tid + 256 * k < 640 ? tid + 256 * k : 0, row = i / 10, c = i - row * 10;
    xr[k] = *(const u32x4*)(p.x + ((size_t)(tb * 64 + row) * 640 + nb * 80 + c * 8) * 2); }
  const unsigned wa = F_ACT + (two ? lane : 128 + h * 16 + (r & 15)) * 16;
  const unsigned aa = h * 1024 + (32 * tt + r) * 16;
  f32x16 acc = {}, acc2 = {};
#pragma unroll 1
  for (int s = 0; s < 24; s++) {
    BARRIER();
    LAS char* sl = lds + (s & 3) * F_STAGE;
    u32x4 af[4], wf[4], wg[4];
#pragma unroll
    for (int ks = 0; ks < 4; ks++) { af[ks] = LDS128(sl + aa + ks * 2048); wf[ks] = LDS128(sl + wa + ks * 2560); }
    if (two) {
#pragma unroll
      for (int ks = 0; ks < 4; ks++) wg[ks] = LDS128(sl + wa + 1024 + ks * 2560);
    }
    if (two) {
      acc = MFMA32(wf[0], af[0], acc); acc2 = MFMA32(wg[0], af[0], acc2); acc = MFMA32(wf[1], af[1], acc); acc2 = MFMA32(wg[1], af[1], acc2);
      if (s != 13) { acc = MFMA32(wf[2], af[2], acc); acc2 = MFMA32(wg[2], af[2], acc2); acc = MFMA32(wf[3], af[3], acc); acc2 = MFMA32(wg[3], af[3], acc2); }
    } else {
      acc = MFMA32(wf[0], af[0], acc); acc = MFMA32(wf[1], af[1], acc);
      if (s != 13) { acc = MFMA32(wf[2], af[2], acc); acc = MFMA32(wf[3], af[3], acc); }
    }
  }
  BARRIER();
  LAS float* ot = (LAS float*)lds;
  constexpr int OP = 84;
  const int n0 = two ? 0 : 64;
#pragma unroll
  for (int g = 0; g < 4; g++) {
    const int n = n0 + 8 * g + 4 * h;
    if (n < 80) *(LAS f32x4*)(ot + (32 * tt + r) * OP + n) = f32x4{acc[4 * g], acc[4 * g + 1], acc[4 * g + 2], acc[4 * g + 3]};
    if (two) *(LAS f32x4*)(ot + (32 * tt + r) * OP + 32 + n) = f32x4{acc2[4 * g], acc2[4 * g + 1], acc2[4 * g + 2], acc2[4 * g + 3]};
  }
  BARRIER();
#pragma unroll
  for (int k = 0; k < 3; k++) {
    const int i = tid + 256 * k;
    if (i < 640) {
      const int row = i / 10, c = i - row * 10;
      const size_t go = ((size_t)(tb * 64 + row) * 640 + nb * 80 + c * 8) * 2;
      const f32x4 a = *(LAS f32x4*)(ot + row * OP + c * 8), a2 = *(LAS f32x4*)(ot + row * OP + c * 8 + 4);
      const u32x4 w = {cvtpk(a[0] + bflo(xr[k][0]), a[1] + bfhi(xr[k][0])), cvtpk(a[2] + bflo(xr[k][1]), a[3] + bfhi(xr[k][1])),
                       cvtpk(a2[0] + bflo(xr[k][2]), a2[1] + bfhi(xr[k][2])), cvtpk(a2[2] + bflo(xr[k][3]), a2[3] + bfhi(xr[k][3]))};
      st16_wt(p.out, (unsigned)go, w, 2621440);
    }
  }
}
DI void sp_role(const Params& p, char* lds_, int b) {
  const int wid = RFL(threadIdx.x >> 6);
  __builtin_amdgcn_s_setprio(3);
  const int tid = threadIdx.x, lane = tid & 63, r = lane & 31, h = lane >> 5;
  const unsigned l0 = (unsigned)(uintptr_t)lds_;
  LAS char* lds = (LAS char*)lds_;
  const int x = b & 7, jj = b >> 3;
  const int tbk = (x >> 1) * 4 + (jj & 3), fb = (x & 1) * 8 + (jj >> 2);
  gu32* cnt = (gu32*)(p.ws + WS_FLAG);
  if (wid >= 4) {
    const int lw = wid - 4;
    const char* wb = p.wsp + (size_t)tbk * 128 * 4096;
    unsigned wv4[4];
#pragma unroll
    for (int i = 0; i < 4; i++) { const unsigned row = 8 * (4 * lw + i) + (lane >> 3); wv4[i] = row * 4096 + (((lane & 7) ^ ((row >> 1) & 7)) << 4); }
    const char* zb = p.ws + WS_Z2N + (size_t)fb * 32 * 8192 + lw * 2048;
    const unsigned zv = lane * 16;
    auto issue_w = [&](int st) {
#pragma unroll
      for (int i = 0; i < 4; i++) gldsn(wb + st * 128, wv4[i], l0 + (st % 3) * SP_STAGE + (4 * lw + i) * 1024);
    };
    auto issue_z = [&](int st) {
      asm volatile("s_mov_b32 m0, %2\n\ts_nop 0\n\tglobal_load_lds_dwordx4 %0, %1 sc1\n\tglobal_load_lds_dwordx4 %0, %1 offset:1024 sc1"
                   :: "v"(zv), "s"(zb + (size_t)st * 8192), "s"(l0 + (st % 3) * SP_STAGE + SP_ZOFF + lw * 2048) : "memory");
    };
    BARRIER(); BARRIER();
    issue_w(0); issue_w(1);
    BARRIER();
    {
      const int u = b * 3 + lw - 1;
      if (lw > 0 && u < 752) {
        const int ks = u >> 3, nb = u & 7;
        const uintptr_t pwo = (uintptr_t)p.wo, pwout = (uintptr_t)p.wout;
        const GAS char* src = (const GAS char*)(ks < 40 ? pwo + (size_t)ks * 16 * 1280 : pwout + (size_t)(ks - 40) * 16 * 1280) + nb * 160;
        LAS char* sc = lds + 3 * SP_STAGE + (lw - 1) * 2560;
#pragma unroll
        for (int i = 0; i < 3; i++) {
          const int c = lane + 64 * i, rw = c / 10, cc = c - rw * 10;
          if (c < 160) *(LAS u32x4*)(sc + c * 16) = *(const GAS u32x4*)(src + rw * 1280 + cc * 16);
        }
        asm volatile("s_waitcnt vmcnt(0) lgkmcnt(0)" ::: "memory");
        LAS char* ta = sc + (8 * h + ((lane & 15) >> 2)) * 160 + 32 * ((lane >> 4) & 1) + 8 * (lane & 3);
        const unsigned dst = (nb * 94 + ks) * 2560;
        const u32x4 f0 = ldtr(ta, 0, 640), f1 = ldtr(ta, 64, 64 + 640), f2 = ldtr(ta, 128, 128 + 640);
        st16_wt(p.ws + WS_W3, dst + lane * 16, f0, 8 * 94 * 2560);
        st16_wt(p.ws + WS_W3, dst + 1024 + lane * 16, f1, 8 * 94 * 2560);
        if (r < 16) st16_wt(p.ws + WS_W3, dst + 2048 + (h * 16 + r) * 16, f2, 8 * 94 * 2560);
        asm volatile("s_waitcnt vmcnt(0) lgkmcnt(0)" ::: "memory");
        if (lane == 0) __hip_atomic_fetch_add(cnt + 1536 + nb * 16, 1u, __ATOMIC_RELAXED, __HIP_MEMORY_SCOPE_AGENT);
      }
    }
    if (lw > 0) {
      const char* src = p.wsp + (unsigned)RFL(b * 32768 + (lw - 1) * 12288);
      const int n2 = lw == 3 ? 4 : 6;
      for (int i = 0; i < n2; i++)
        asm volatile("s_mov_b32 m0, %2\n\ts_nop 0\n\tglobal_load_lds_dwordx4 %0, %1\n\tglobal_load_lds_dwordx4 %0, %1 offset:1024"
                     :: "v"(zv), "s"(src + i * 2048), "s"(l0 + 3 * SP_STAGE - 2048) : "memory");
    }
    if (lw == 0) {
      {
      __builtin_amdgcn_s_sleep(24);
      unsigned spins = 0;
      for (;;) {
        const unsigned c = __hip_atomic_load(cnt + (lane & 31) * 16, __ATOMIC_RELAXED, __HIP_MEMORY_SCOPE_AGENT);
        if (__all(c >= 8u)) break;
        if (++spins > (1u << 21)) break;
        __builtin_amdgcn_s_sleep(6);
      }
      }
      const char* z00 = p.ws + WS_Z2N + (size_t)fb * 32 * 8192;
#pragma unroll
      for (int i = 0; i < 4; i++)
        asm volatile("s_mov_b32 m0, %2\n\ts_nop 0\n\tglobal_load_lds_dwordx4 %0, %1 sc1\n\tglobal_load_lds_dwordx4 %0, %1 offset:1024 sc1\n\tglobal_load_lds_dwordx4 %0, %1 offset:2048 sc1\n\tglobal_load_lds_dwordx4 %0, %1 offset:3072 sc1"
                     :: "v"(zv), "s"(z00 + i * 4096), "s"(l0 + (i >> 1) * SP_STAGE + SP_ZOFF + (i & 1) * 4096) : "memory");
      WAITV(8);
    }
#pragma unroll 1
    for (int st = 0; st < 32; st++) {
      if (st == 0) { if (lw) WAITV(0); } else if (st < 31) WAITV(6); else WAITV(0);
      BARRIER();
      if (st + 2 < 32) { issue_w(st + 2); issue_z(st + 2); }
    }
    fin_role(p, lds_, b, wid);
    return;
  }
#define LNPRIO 3
  __builtin_amdgcn_s_setprio(LNPRIO);
  {
    LAS float* red = (LAS float*)(lds + K2_LDS - 256);
    const bool act = tid < 216;
    const int g8 = act ? tid >> 1 : 107, half = tid & 1;
    const char* src = p.ws + WS_Z2R + ((size_t)((b >> 2) * 108 + g8) * 32 + (b & 3) * 8 + 4 * half) * 16;
    u32x4 raw[4];
#pragma unroll
    for (int i = 0; i < 4; i++) raw[i] = *(const u32x4*)(src + i * 16);
    const unsigned ONES = act ? 0x3F803F80u : 0u;
    LAS float* part = (LAS float*)(lds + 2 * SP_STAGE);
#pragma unroll
    for (int t = 0; t < 4; t++) {
      float su = 0.f, sq = 0.f;
#pragma unroll
      for (int e = 0; e < 4; e++) { const unsigned a = act ? raw[t][e] : 0u; su = dot2(a, ONES, su); sq = dot2(a, a, sq); }
      part[(2 * (4 * half + t)) * 128 + (tid >> 1)] = su;
      part[(2 * (4 * half + t) + 1) * 128 + (tid >> 1)] = sq;
    }
    BARRIER();
    {
      float s4[4];
#pragma unroll
      for (int k = 0; k < 4; k++) { const f32x2 a = *(LAS f32x2*)(part + (4 * wid + k) * 128 + lane * 2); s4[k] = a[0] + a[1]; }
#pragma unroll
      for (int k = 0; k < 4; k++) s4[k] = wave_sum(s4[k]);
      if (lane < 2) {
        const float su = lane ? s4[2] : s4[0], sq = lane ? s4[3] : s4[1];
        const float mu = su * (1.f / 864.f), var = fmaxf(sq * (1.f / 864.f) - mu * mu, 0.f), rs = __builtin_amdgcn_rsqf(var + 1e-5f);
        ((LAS f32x2*)red)[2 * wid + lane] = f32x2{rs, -mu * rs};
      }
    }
    BARRIER();
    LAS char* tr = lds + 2 * SP_STAGE + 8192;
    {
      float v[4][8];
#pragma unroll
      for (int t = 0; t < 4; t++) {
        const f32x2 c = ((LAS f32x2*)red)[4 * half + t];
#pragma unroll
        for (int e = 0; e < 4; e++) { v[t][2 * e] = __builtin_fmaf(bflo(raw[t][e]), c[0], c[1]); v[t][2 * e + 1] = __builtin_fmaf(bfhi(raw[t][e]), c[0], c[1]); }
      }
      if (act) {
#pragma unroll
        for (int i = 0; i < 8; i++) *(LAS u32x2*)(tr + (8 * g8 + i) * 16 + half * 8) = u32x2{cvtpk(v[0][i], v[1][i]), cvtpk(v[2][i], v[3][i])};
      }
    }
    asm volatile("s_waitcnt lgkmcnt(0)" ::: "memory");
    {
      const __amdgpu_buffer_rsrc_t rsrc = __builtin_amdgcn_make_buffer_rsrc((void*)(p.ws + WS_Z2N), 0, 4194304, 0x00020000);
      const unsigned sbase = (((b >> 3) * 4 + ((b >> 1) & 3)) * 2 + (b & 1)) * 1024;
#pragma unroll
      for (int i = 0; i < 4; i++) {
        const int f = 256 * wid + 64 * i + lane;
        if (f < 864) {
          const u32x4 w = *(LAS u32x4*)(tr + f * 16);
          const int q4 = (f >= 216) + (f >= 432) + (f >= 648), rem = f - q4 * 216, sub = (rem >= 56) + (rem >= 112) + (rem >= 168);
          const unsigned off = (unsigned)(q4 * 4 + sub) * (32u * 8192u) + sbase + (rem - sub * 56) * 16;
          __builtin_amdgcn_raw_buffer_store_b128(w, rsrc, off, 0, 16);
        }
      }
    }
    if (tid < 160) {
      const int fbw = tid < 96 ? (tid >> 3) / 3 * 4 + (tid >> 3) % 3 : ((tid - 96) >> 4) * 4 + 3, row = tid < 96 ? 56 + (tid & 7) : 48 + (tid & 15);
      st16_wt(p.ws + WS_Z2N, (unsigned)fbw * (32u * 8192u) + (((b >> 3) * 4 + ((b >> 1) & 3)) * 2 + (b & 1)) * 1024 + row * 16, u32x4{0u, 0u, 0u, 0u}, 4194304);
    }
    asm volatile("s_waitcnt vmcnt(0)" ::: "memory");
    BARRIER();
    if (tid == 0) __hip_atomic_fetch_add(cnt + (b >> 3) * 16, 1u, __ATOMIC_RELAXED, __HIP_MEMORY_SCOPE_AGENT);
  }
  const unsigned row = 32 * wid + r;
  unsigned wa[4];
#pragma unroll
  for (int ks = 0; ks < 4; ks++) wa[ks] = row * 128 + ((32 * ks + 16 * h) ^ (16 * ((row >> 1) & 7)));
  const unsigned za = SP_ZOFF + h * 1024 + r * 16;
  f32x16 acc0 = {}, acc1 = {};
#pragma unroll 1
  for (int st = 0; st < 32; st++) {
    BARRIER();
    LAS char* sl = lds + (st % 3) * SP_STAGE;
    u32x4 wf[4], z0[4], z1[4];
#pragma unroll
    for (int ks = 0; ks < 4; ks++) { wf[ks] = LDS128(sl + wa[ks]); z0[ks] = LDS128(sl + za + ks * 2048); z1[ks] = LDS128(sl + za + ks * 2048 + 512); }
#pragma unroll
    for (int ks = 0; ks < 4; ks++) { acc0 = MFMA32(z0[ks], wf[ks], acc0); acc1 = MFMA32(z1[ks], wf[ks], acc1); }
  }
  const int tok = tbk * 128 + row, f0 = fb_start(fb), width = (fb & 3) == 3 ? 48 : 56;
  const float bias = bflo(*(const unsigned short*)(p.bsp + tok * 2));
  const char* z1b = p.ws + WS_Z1 + ((size_t)((tok >> 5) * 108 + (f0 >> 3)) * 32 + r) * 16 + h * 8;
#pragma unroll
  for (int j = 0; j < 2; j++) {
    const f32x16& a = j ? acc1 : acc0;
    u32x2 w[4];
#pragma unroll
    for (int g = 0; g < 4; g++) {
      const int fl = 32 * j + 8 * g;
      u32x2 z = {0u, 0u};
      if (fl < width) z = *(const u32x2*)(z1b + (4 * j + g) * 512);
      w[g] = u32x2{cvtpk(bflo(z[0]) * (a[4 * g] + bias), bfhi(z[0]) * (a[4 * g + 1] + bias)), cvtpk(bflo(z[1]) * (a[4 * g + 2] + bias), bfhi(z[1]) * (a[4 * g + 3] + bias))};
    }
#pragma unroll
    for (int g = 0; g < 4; g += 2) {
      const u32x4 w8 = pair8(w[g], w[g + 1]);
      const int fl = 32 * j + 8 * (g + h);
      if (fl < width) st16_wt(p.ws + WS_AO, ((80 + ((f0 + fl) >> 3)) * 2048 + tok) * 16, w8, 6160384);
    }
  }
  asm volatile("s_waitcnt vmcnt(0)" ::: "memory");
  fin_role(p, lds_, b, wid);
}
__global__ void __launch_bounds__(512, 4) k2_mix(Params p) {
  extern __shared__ __attribute__((aligned(16))) char lds[];
  unsigned junk = 0;
  if (threadIdx.x >= 448) {
    unsigned long long pc; int codelen;
    asm volatile("s_getpc_b64 %0\nhipk_pc_mark2:\n\ts_mov_b32 %1, hipk_text_mark-hipk_pc_mark2" : "=s"(pc), "=s"(codelen));
    touch_lines((const char*)(pc & ~127ull), codelen > 0 ? codelen >> 7 : 1, junk, threadIdx.x & 63);
  }
  if (blockIdx.x >= 256) sp_role(p, lds, blockIdx.x - 256); else attn_role(p, lds, blockIdx.x);
  asm volatile("hipk_text_mark:" :: "v"(junk));
}

extern "C" void kernel_launch(void* const* d_in, const int* in_sizes, int n_in, void* d_out, int out_size, void* d_ws, size_t ws_size, hipStream_t stream) {
  Params p;
  p.x = (const char*)d_in[0]; p.wq = (const char*)d_in[1]; p.wk = (const char*)d_in[2]; p.wv = (const char*)d_in[3]; p.wo = (const char*)d_in[4];
  p.win = (const char*)d_in[6]; p.wsp = (const char*)d_in[7]; p.bsp = (const char*)d_in[8]; p.wout = (const char*)d_in[9];
  p.ws = (char*)d_ws; p.out = (char*)d_out;
  (void)hipFuncSetAttribute((const void*)k1_proj, hipFuncAttributeMaxDynamicSharedMemorySize, K1_LDS);
  (void)hipFuncSetAttribute((const void*)k2_mix, hipFuncAttributeMaxDynamicSharedMemorySize, K2_LDS);
  k1_proj<<<256, 768, K1_LDS, stream>>>(p);
  k2_mix<<<512, 512, K2_LDS, stream>>>(p);
}
```

```cpp
#include <hip/hip_runtime.h>
#include <stdint.h>
#include <type_traits>
typedef __bf16 bf16x8 __attribute__((ext_vector_type(8)));
typedef __bf16 bf16x2 __attribute__((ext_vector_type(2)));
typedef short s16x4 __attribute__((ext_vector_type(4)));
typedef float f32x16 __attribute__((ext_vector_type(16)));
typedef float f32x4 __attribute__((ext_vector_type(4)));
typedef float f32x2 __attribute__((ext_vector_type(2)));
typedef unsigned u32x4 __attribute__((ext_vector_type(4)));
typedef unsigned u32x2 __attribute__((ext_vector_type(2)));
#define DI __device__ __forceinline__
#define LAS __attribute__((address_space(3)))
#define GAS __attribute__((address_space(1)))
#define SB() __builtin_amdgcn_sched_barrier(0)
#define RFL(x) __builtin_amdgcn_readfirstlane(x)
#define MFMA32(a, b, c) __builtin_amdgcn_mfma_f32_32x32x16_bf16(__builtin_bit_cast(bf16x8, a), __builtin_bit_cast(bf16x8, b), c, 0, 0, 0)
#define MFMA16(a, b, c) __builtin_amdgcn_mfma_f32_16x16x32_bf16(__builtin_bit_cast(bf16x8, a), __builtin_bit_cast(bf16x8, b), c, 0, 0, 0)
DI f32x4 mfma4sq(unsigned x0, unsigned x1, f32x4 c) { u32x2 v = {x0, x1}; s16x4 a = __builtin_bit_cast(s16x4, v); return __builtin_amdgcn_mfma_f32_4x4x4bf16_1k(a, a, c, 0, 0, 0); }
#define MFMA4(x0, x1, c) mfma4sq(x0, x1, c)
DI f32x16 mfma8(u32x2 a, u32x2 b, f32x16 c) { return __builtin_amdgcn_mfma_f32_32x32x8bf16_1k(__builtin_bit_cast(s16x4, a), __builtin_bit_cast(s16x4, b), c, 0, 0, 0); }
#define MFMA8(a, b, c) mfma8(a, b, c)
#define LDS64(p) (*(const LAS u32x2*)(p))
#define LDS128(p) (*(const LAS u32x4*)(p))
DI unsigned cvtpk(float lo, float hi) { f32x2 v = {lo, hi}; bf16x2 b = __builtin_convertvector(v, bf16x2); return __builtin_bit_cast(unsigned, b); }
DI float bflo(unsigned u) { return __uint_as_float(u << 16); }
DI float bfhi(unsigned u) { return __uint_as_float(u & 0xffff0000u); }
DI void sqacc(float& a, float x) { asm("v_fmac_f32_e32 %0, %1, %1" : "+v"(a) : "v"(x)); }
DI void addacc(float& a, float x) { asm("v_add_f32_e32 %0, %0, %1" : "+v"(a) : "v"(x)); }
DI float dot2(unsigned a, unsigned b, float c) { return __builtin_amdgcn_fdot2_f32_bf16(__builtin_bit_cast(bf16x2, a), __builtin_bit_cast(bf16x2, b), c, false); }
DI float half_sum(float m) { auto r = __builtin_amdgcn_permlane32_swap(__float_as_uint(m), __float_as_uint(m), false, false); return __uint_as_float(r[0]) + __uint_as_float(r[1]); }
DI float half_max(float m) { auto r = __builtin_amdgcn_permlane32_swap(__float_as_uint(m), __float_as_uint(m), false, false); return fmaxf(__uint_as_float(r[0]), __uint_as_float(r[1])); }
template <int CTRL, int RMASK> DI float dpp_add(float v) { return v + __uint_as_float(__builtin_amdgcn_update_dpp(0u, __float_as_uint(v), CTRL, RMASK, 0xf, true)); }
DI float wave_sum(float v) {
  v = dpp_add<0x111, 0xf>(v); v = dpp_add<0x112, 0xf>(v); v = dpp_add<0x114, 0xf>(v); v = dpp_add<0x118, 0xf>(v);
  v = dpp_add<0x142, 0xa>(v); v = dpp_add<0x143, 0xc>(v);
  return __uint_as_float(__builtin_amdgcn_readlane(__float_as_uint(v), 63));
}
DI void glds3(const void* sbase, unsigned voff, unsigned lds_base) {
  asm volatile("s_mov_b32 m0, %2\n\ts_nop 0\n\tglobal_load_lds_dwordx4 %0, %1\n\tglobal_load_lds_dwordx4 %0, %1 offset:1024\n\tglobal_load_lds_dwordx4 %0, %1 offset:2048"
               :: "v"(voff), "s"(sbase), "s"(lds_base) : "memory"); }
DI void gldsn(const void* sbase, unsigned voff, unsigned lds_base) {
  asm volatile("s_mov_b32 m0, %2\n\ts_nop 0\n\tglobal_load_lds_dwordx4 %0, %1" :: "v"(voff), "s"(sbase), "s"(lds_base) : "memory"); }
DI void gldsn_dev(const void* sbase, unsigned voff, unsigned lds_base) {
  asm volatile("s_mov_b32 m0, %2\n\ts_nop 0\n\tglobal_load_lds_dwordx4 %0, %1 sc1" :: "v"(voff), "s"(sbase), "s"(lds_base) : "memory"); }
DI u32x4 ldtr(const LAS char* p, int off0, int off1) {
  typedef LAS s16x4* lp;
  s16x4 a = __builtin_amdgcn_ds_read_tr16_b64_v4i16((lp)(p + off0)), b = __builtin_amdgcn_ds_read_tr16_b64_v4i16((lp)(p + off1));
  u32x2 x = __builtin_bit_cast(u32x2, a), y = __builtin_bit_cast(u32x2, b);
  return u32x4{x[0], x[1], y[0], y[1]};
}
DI void st16_wt(char* base, unsigned off, u32x4 w, unsigned bytes) {
  const __amdgpu_buffer_rsrc_t rs = __builtin_amdgcn_make_buffer_rsrc((void*)base, 0, bytes, 0x00020000);
  __builtin_amdgcn_raw_buffer_store_b128(w, rs, off, 0, 16);
}
DI u32x4 pair8(u32x2 a, u32x2 b) {
  auto s0 = __builtin_amdgcn_permlane32_swap(a[0], b[0], false, false);
  auto s1 = __builtin_amdgcn_permlane32_swap(a[1], b[1], false, false);
  return u32x4{s0[0], s1[0], s0[1], s1[1]};
}
DI void touch_lines(const void* base, int lines, unsigned& junk, int lane) {
  for (int l0 = 0; l0 < lines; l0 += 64) { const int l = l0 + lane < lines ? l0 + lane : lines - 1;
    asm volatile("global_load_dword %0, %1, %2" : "+v"(junk) : "v"((unsigned)l * 128u), "s"(base) : "memory"); }
}
#define WAITV(n) asm volatile("s_waitcnt vmcnt(" #n ")" ::: "memory")
#define BARRIER() asm volatile("s_waitcnt lgkmcnt(0)\n\ts_barrier" ::: "memory")

constexpr int SEQ = 2048, DM = 640, NFH = 864;
constexpr size_t WS_FLAG = 0;
constexpr size_t WS_QI  = 8192;
constexpr size_t WS_KV  = WS_QI + 2621440;
constexpr size_t WS_Z1  = WS_KV + 196608;
constexpr size_t WS_Z2R = WS_Z1 + 3538944;
constexpr size_t WS_Z2N = WS_Z2R + 3538944;
constexpr size_t WS_AO  = WS_Z2N + 4194304;
constexpr size_t WS_W3  = WS_AO + 6160384;
constexpr size_t WS_END = WS_W3 + 8 * 94 * 2560;
constexpr int KV_CHUNK = 12288, KV_VOFF = 6144;
struct Params {
  const char *x, *wq, *wk, *wv, *wo, *win, *wsp, *bsp, *wout;
  char* ws; char* out;
};

constexpr int K1_STAGE = 36864, K1_NS = 4, K1_XT = 16384;
constexpr int K1_CSP = K1_NS * K1_STAGE;
constexpr int K1_CSF = K1_CSP + 2560;
constexpr int K1_STP = K1_CSF + 640;
constexpr int K1_LDS = K1_STP + 1024;
constexpr float QSCALE = 0.22360679774997896f * 1.4426950408889634f;
enum { T_Q = 0, T_K = 1, T_V = 2, T_Z1 = 3, T_Z2 = 4, T_E = 5 };
DI void k1_slot(int cg, int j, int& type, int& idx) {
  if (j < 3 || (j == 3 && cg < 6)) { const int z = cg < 6 ? cg * 4 + j : 24 + (cg - 6) * 3 + j; type = z < 27 ? T_Z1 : T_Z2; idx = z < 27 ? z : z - 27; }
  else if (cg == 15) { type = j == 3 ? T_K : T_V; idx = 0; }
  else if (cg < 4) { type = T_E; idx = 0; }
  else if (cg < 6) { type = T_Q; idx = cg - 4; }
  else { type = T_Q; idx = 2 + (cg - 6) * 2 + (j - 3); }
}
DI f32x2 gelu2(f32x2 x) {
  const f32x2 c1 = {-0.10294324f, -0.10294324f}, c0 = {-2.3022082f, -2.3022082f}, one = {1.f, 1.f};
  const f32x2 a = x * __builtin_elementwise_fma(x * x, c1, c0);
  const f32x2 d = f32x2{__builtin_amdgcn_exp2f(a[0]), __builtin_amdgcn_exp2f(a[1])} + one;
  return x * f32x2{__builtin_amdgcn_rcpf(d[0]), __builtin_amdgcn_rcpf(d[1])};
}
DI void k1_loader(const Params& p, char* lds_, int rb, int cg, int lw) {
  const int lane = threadIdx.x & 63;
  __builtin_amdgcn_s_setprio(3);
  const unsigned l0 = (unsigned)(uintptr_t)lds_;
  const char* xb = p.x + (size_t)rb * 128 * 1280;
  unsigned xv[4];
#pragma unroll
  for (int i = 0; i < 4; i++) { const unsigned row = 8 * (4 * lw + i) + (lane >> 3); xv[i] = row * 1280 + (((lane & 7) ^ ((row >> 1) & 7)) << 4); }
#pragma unroll
  for (int i = 0; i < 4; i++) gldsn(xb, xv[i], l0 + (4 * lw + i) * 1024);
  const char* base[5]; unsigned stride[5], voff[5];
  const uintptr_t pwq = (uintptr_t)p.wq, pwin = (uintptr_t)p.win, pwk = (uintptr_t)p.wk, pwv = (uintptr_t)p.wv;
#pragma unroll
  for (int j = 0; j < 5; j++) {
    int type, idx; k1_slot(cg, j, type, idx);
    const unsigned rowp = 16 * lw + (lane >> 2), ch = (lane & 3) * 16;
    const bool kvs = j >= 3 && cg == 15, isq = type == T_Q || type == T_E;
    const uintptr_t a = kvs ? (j == 3 ? pwk : pwv) : isq ? pwq + idx * 64 : pwin + (type == T_Z2 ? 1728 : 0) + idx * 64;
    base[j] = (const char*)a;
    stride[j] = kvs ? 2560u : isq ? 64u * 1280u : 64u * 3456u;
    const unsigned o = (lw < 2 ? lw : 2) * 1024 + lane * 16;
    voff[j] = kvs ? (o < 2544u ? o : 2544u) : rowp * (isq ? 1280u : 3456u) + ch;
  }
  auto issue = [&](int st) {
    const unsigned slot = l0 + (st & 3) * K1_STAGE;
    if (st > 0) {
#pragma unroll
      for (int i = 0; i < 4; i++) gldsn(xb + st * 128, xv[i], slot + (4 * lw + i) * 1024);
    }
#pragma unroll
    for (int j = 0; j < 5; j++) gldsn(base[j] + (size_t)st * stride[j], voff[j], slot + K1_XT + j * 4096 + lw * 1024);
  };
  issue(0); issue(1); issue(2);
  WAITV(18);
  BARRIER();
#pragma unroll 1
  for (int st = 0; st < 10; st++) {
    if (st < 8) WAITV(9); else WAITV(0);
    BARRIER();
    if (st + 3 < 10) issue(st + 3);
  }
}
template <int ROLE>
DI void k1_body(const Params& p, char* lds_, int rb, int cg, int tt) {
  constexpr int NJ = ROLE == 0 ? 3 : 2, J0 = ROLE == 0 ? 0 : 3;
  const int lane = threadIdx.x & 63, r = lane & 31, h = lane >> 5;
  LAS char* lds = (LAS char*)lds_;
  const bool kv = ROLE == 1 && cg == 15;
  int stype[NJ], sidx[NJ];
#pragma unroll
  for (int j = 0; j < NJ; j++) k1_slot(cg, J0 + j, stype[j], sidx[j]);
#define K1_BPRIO 1
  if (ROLE == 1) __builtin_amdgcn_s_setprio(K1_BPRIO);
  const int row = 32 * tt + r;
  unsigned xa[4], wa[4];
  const unsigned pitch = kv ? 40 : 64;
  const int blk = (lane >> 4) & 1, q = (lane & 15) >> 2, pp = lane & 3;
#pragma unroll
  for (int i = 0; i < 4; i++) {
    const unsigned ks = (i + tt) & 3;
    xa[i] = row * 128 + ((32 * ks + 16 * h) ^ (16 * ((row >> 1) & 7)));
    wa[i] = K1_XT + J0 * 4096 + (16 * ks + 8 * h + q) * pitch + 32 * blk + 8 * pp;
  }
  f32x16 acc[NJ];
#pragma unroll
  for (int j = 0; j < NJ; j++) acc[j] = f32x16{};
  f32x4 cs[NJ];
  float sq[4] = {0.f, 0.f, 0.f, 0.f}, sm[4] = {0.f, 0.f, 0.f, 0.f};
#pragma unroll
  for (int j = 0; j < NJ; j++) cs[j] = f32x4{};
  const unsigned ONES = 0x3F803F80u;
  const unsigned selw = ((lane & 15) == ((lane >> 4) & 1)) ? ONES : 0u;
  const u32x4 SEL = {selw, selw, selw, selw};
  auto mainloop = [&](auto KVC) {
    constexpr bool KV = decltype(KVC)::value;
    u32x4 xf[4] = {}, wf[4][NJ] = {};
    auto rd = [&](LAS char* sl, int i) {
      xf[i] = LDS128(sl + xa[i]);
#pragma unroll
      for (int j = 0; j < NJ; j++) wf[i][j] = ldtr(sl + wa[i], j * 4096, j * 4096 + (KV ? 160 : 256));
    };
    auto mm = [&](int i, LAS char* nsl, int ni) {
#pragma unroll
      for (int j = 0; j < NJ; j++) {
        if (KV && j == 1) acc[j] = MFMA32(xf[i], wf[i][j], acc[j]);
        else acc[j] = MFMA32(wf[i][j], xf[i], acc[j]);
        SB();
        if (j == 0) { xf[ni] = LDS128(nsl + xa[ni]); wf[ni][0] = ldtr(nsl + wa[ni], 0, KV ? 160 : 256); }
        else { wf[ni][j] = ldtr(nsl + wa[ni], j * 4096, j * 4096 + (KV ? 160 : 256)); }
        SB();
      }
      if (ROLE == 1) {
#pragma unroll
        for (int e = 0; e < 4; e++) { const float lo = bflo(xf[i][e]), hi = bfhi(xf[i][e]); sqacc(sq[(2 * e) & 3], lo); sqacc(sq[(2 * e + 1) & 3], hi); addacc(sm[e & 1], lo); addacc(sm[2 + (e & 1)], hi); }
        SB();
      }
    };
    BARRIER();
    rd(lds, 0); rd(lds, 1);
#pragma unroll 1
    for (int st = 0; st < 10; st++) {
      LAS char* sl = lds + (st & 3) * K1_STAGE;
      LAS char* sn = lds + ((st + 1) & 3) * K1_STAGE;
      mm(0, sl, 2);
      mm(1, sl, 3);
#pragma unroll
      for (int j = 0; j < NJ; j++) cs[j] = MFMA16(SEL, wf[0][j], cs[j]);
      SB();
      BARRIER();
      mm(2, sn, 0);
      mm(3, sn, 1);
      SB();
    }
  };
  if (kv) mainloop(std::true_type{}); else mainloop(std::false_type{});
  LAS float* csp = (LAS float*)(lds + K1_CSP);
  LAS float* csf = (LAS float*)(lds + K1_CSF);
  LAS f32x2* stt = (LAS f32x2*)(lds + K1_STP);
  if (lane < 16) {
#pragma unroll
    for (int j = 0; j < NJ; j++) { csp[tt * 160 + (J0 + j) * 32 + lane] = cs[j][0]; csp[tt * 160 + (J0 + j) * 32 + 16 + lane] = cs[j][1]; }
  }
  if (ROLE == 1) {
    const float sqs = half_sum((sq[0] + sq[1]) + (sq[2] + sq[3]));
    const float su = half_sum((sm[0] + sm[1]) + (sm[2] + sm[3]));
    const float mu = su * (1.f / 640.f), var = fmaxf(sqs * (1.f / 640.f) - mu * mu, 0.f), rs = __builtin_amdgcn_rsqf(var + 1e-5f);
    if (h == 0) stt[row] = f32x2{mu * rs, rs};
  }
  BARRIER();
  { const int c = threadIdx.x; if (c < 160) csf[c] = (csp[c] + csp[160 + c]) + (csp[320 + c] + csp[480 + c]); }
  const f32x2 st_ = stt[row];
  BARRIER();
  const int tb = rb * 4 + tt;
#pragma unroll
  for (int j = 0; j < NJ; j++) {
    const int type = stype[j];
    if (type == T_E) continue;
    if (ROLE == 1 && type == T_V) {
      const float csum = csf[(J0 + j) * 32 + r];
      char* vb = p.ws + WS_KV + (size_t)rb * KV_CHUNK + KV_VOFF + tt * 1536 + h * 384 + r * 16;
#pragma unroll
      for (int s = 0; s < 2; s++) {
        float v[8];
#pragma unroll
        for (int i = 0; i < 8; i++) { const f32x2 t = stt[32 * tt + 16 * s + 8 * (i >> 2) + 4 * h + (i & 3)]; v[i] = t[1] * acc[j][8 * s + i] - t[0] * csum; }
        u32x4 w = {cvtpk(v[0], v[1]), cvtpk(v[2], v[3]), cvtpk(v[4], v[5]), cvtpk(v[6], v[7])};
        if (r == 20) w = u32x4{ONES, ONES, ONES, ONES};
        if (r > 20) w = u32x4{0u, 0u, 0u, 0u};
        if (r < 24) *(u32x4*)(vb + s * 768) = w;
      }
      continue;
    }
    f32x2 v[8];
    const f32x2 rs2 = {st_[1], st_[1]}, nm2 = {-st_[0], -st_[0]};
#pragma unroll
    for (int g = 0; g < 4; g++) {
      const f32x4 c4 = *(LAS f32x4*)(csf + (J0 + j) * 32 + 8 * g + 4 * h);
      v[2 * g] = __builtin_elementwise_fma(f32x2{c4[0], c4[1]}, nm2, f32x2{acc[j][4 * g], acc[j][4 * g + 1]} * rs2);
      v[2 * g + 1] = __builtin_elementwise_fma(f32x2{c4[2], c4[3]}, nm2, f32x2{acc[j][4 * g + 2], acc[j][4 * g + 3]} * rs2);
    }
    if (ROLE == 1 && type == T_K) {
      char* kb = p.ws + WS_KV + (size_t)rb * KV_CHUNK + tt * 1536;
      *(u32x2*)(kb + r * 16 + h * 8) = u32x2{cvtpk(v[0][0], v[0][1]), cvtpk(v[1][0], v[1][1])};
      *(u32x2*)(kb + 512 + r * 16 + h * 8) = u32x2{cvtpk(v[2][0], v[2][1]), cvtpk(v[3][0], v[3][1])};
      *(u32x2*)(kb + 1024 + h * 256 + r * 8) = h ? u32x2{0x00003F80u, 0u} : u32x2{cvtpk(v[4][0], v[4][1]), cvtpk(v[5][0], v[5][1])};
      continue;
    }
    unsigned off; char* base;
    if (type == T_Q) {
      base = p.ws + WS_QI; off = ((tb * 80 + sidx[j] * 4 + h) * 32 + r) * 16;
#pragma unroll
      for (int i = 0; i < 8; i++) v[i] *= f32x2{QSCALE, QSCALE};
    } else {
      base = p.ws + (type == T_Z1 ? WS_Z1 : WS_Z2R); off = ((tb * 108 + sidx[j] * 4 + h) * 32 + r) * 16;
#pragma unroll
      for (int i = 0; i < 8; i++) v[i] = gelu2(v[i]);
    }
#pragma unroll
    for (int g = 0; g < 4; g += 2)
      st16_wt(base, off + g * 512, pair8(u32x2{cvtpk(v[2 * g][0], v[2 * g][1]), cvtpk(v[2 * g + 1][0], v[2 * g + 1][1])}, u32x2{cvtpk(v[2 * g + 2][0], v[2 * g + 2][1]), cvtpk(v[2 * g + 3][0], v[2 * g + 3][1])}), 3538944);
  }
}
__global__ void __launch_bounds__(768, 3) k1_proj(Params p) {
  extern __shared__ __attribute__((aligned(16))) char lds[];
  unsigned junk = 0;
  if (threadIdx.x < 64) {
    unsigned long long pc; int codelen;
    asm volatile("s_getpc_b64 %0\nhipk_pc_mark:\n\ts_mov_b32 %1, hipk_text_mark-hipk_pc_mark" : "=s"(pc), "=s"(codelen));
    touch_lines((const char*)(pc & ~127ull), codelen > 0 ? codelen >> 7 : 1, junk, threadIdx.x);
  }
  const int b = blockIdx.x, x = b & 7, j = b >> 3;
  const int rb = (x & 1) * 8 + (j & 7), cg = (x >> 1) * 4 + (j >> 3);
  const int wid = RFL(threadIdx.x >> 6);
  if (b == 0 && threadIdx.x < 512) ((u32x4*)(p.ws + WS_FLAG))[threadIdx.x] = u32x4{0u, 0u, 0u, 0u};
  if (wid < 4) k1_body<0>(p, lds, rb, cg, wid); else if (wid < 8) k1_body<1>(p, lds, rb, cg, wid - 4); else k1_loader(p, lds, rb, cg, wid - 8);
  asm volatile("" :: "v"(junk));
}

typedef __attribute__((address_space(1))) unsigned gu32;
constexpr int K2_LDS = 81920;
constexpr float SM_OFF = 100.f;
#define EX(x) __builtin_amdgcn_exp2f(x)
#define AUNIT(Sc, Sn, qn0, qn1, Oc, Op, LD0, LD1, LD2, LD3) do { \
    Sc[0] = EX(Sc[0]); Sc[1] = EX(Sc[1]); SB(); \
    Sn = MFMA32(k0, qn0, zero16); SB(); LD0; SB(); \
    Sc[2] = EX(Sc[2]); Sc[3] = EX(Sc[3]); SB(); p1[2] = cvtpk(e12, e13); p1[3] = cvtpk(e14, e15); SB(); \
    Sc[4] = EX(Sc[4]); Sc[5] = EX(Sc[5]); SB(); \
    Op = MFMA32(v1, p1, Op); SB(); LD1; SB(); \
    Sc[6] = EX(Sc[6]); Sc[7] = EX(Sc[7]); SB(); p0[0] = cvtpk(Sc[0], Sc[1]); p0[1] = cvtpk(Sc[2], Sc[3]); SB(); \
    Sc[8] = EX(Sc[8]); Sc[9] = EX(Sc[9]); SB(); \
    Sn = MFMA8(k1, qn1, Sn); SB(); LD2; SB(); \
    Sc[10] = EX(Sc[10]); Sc[11] = EX(Sc[11]); SB(); p0[2] = cvtpk(Sc[4], Sc[5]); p0[3] = cvtpk(Sc[6], Sc[7]); SB(); \
    e12 = EX(Sc[12]); e13 = EX(Sc[13]); SB(); \
    Oc = MFMA32(v0, p0, Oc); SB(); LD3; SB(); \
    e14 = EX(Sc[14]); e15 = EX(Sc[15]); SB(); p1[0] = cvtpk(Sc[8], Sc[9]); p1[1] = cvtpk(Sc[10], Sc[11]); SB(); \
  } while (0)
#define PSW(a)
#define ATILE(kn, v1c, v0n) do { \
    PSW(0); AUNIT(sA, sB, qB0, qB1, oA, oB, k0 = LDS128(kn), v1 = LDS128(v1c), k1 = LDS64((kn) + k1d), (void)0); \
    PSW(1); AUNIT(sB, sA, qA0, qA1, oB, oA, (void)0, (void)0, (void)0, v0 = LDS128(v0n)); \
  } while (0)
DI float max3f(float a, float b, float c) { return __builtin_fmaxf(__builtin_fmaxf(a, b), c); }
DI float max16(const f32x16& s) {
  const float a = max3f(max3f(s[0], s[1], s[2]), s[3], s[4]), b = max3f(max3f(s[5], s[6], s[7]), s[8], s[9]), c = max3f(max3f(s[10], s[11], s[12]), s[13], s[14]);
  return max3f(max3f(a, b, c), s[15], s[15]);
}
DI f32x16 attn_exact(const char* kv, u32x4 q0, u32x2 q1, int r, int h) {
  const f32x16 zero16 = {};
  if (h) q1 = u32x2{0u, 0u};
  f32x16 o = {}; float m = -3.0e38f;
  const int rr = r < 21 ? r : 21;
#pragma unroll 1
  for (int t = 0; t < 64; t++) {
    const char* cb = kv + (size_t)(t >> 2) * KV_CHUNK;
    const char* kb = cb + (t & 3) * 1536 + r * 16;
    const u32x4 k0 = *(const u32x4*)(kb + h * 512); const u32x2 k1 = *(const u32x2*)(cb + (t & 3) * 1536 + 1024 + h * 256 + r * 8);
    const char* vb = cb + KV_VOFF + (t & 3) * 1536 + h * 384 + rr * 16;
    const u32x4 v0 = *(const u32x4*)(vb), v1 = *(const u32x4*)(vb + 768);
    f32x16 s = MFMA32(k0, q0, zero16); s = MFMA8(k1, q1, s);
    float mx = s[0];
#pragma unroll
    for (int i = 1; i < 16; i++) mx = fmaxf(mx, s[i]);
    const float mn = fmaxf(m, half_max(mx)), a = EX(m - mn);
    m = mn;
#pragma unroll
    for (int i = 0; i < 16; i++) { o[i] *= a; s[i] = EX(s[i] - mn); }
    const u32x4 p0 = {cvtpk(s[0], s[1]), cvtpk(s[2], s[3]), cvtpk(s[4], s[5]), cvtpk(s[6], s[7])};
    const u32x4 p1 = {cvtpk(s[8], s[9]), cvtpk(s[10], s[11]), cvtpk(s[12], s[13]), cvtpk(s[14], s[15])};
    o = MFMA32(v0, p0, o); o = MFMA32(v1, p1, o);
  }
  return o;
}
DI void attn_role(const Params& p, char* lds_, int wg) {
  const int tid = threadIdx.x, lane = tid & 63, r = lane & 31, h = lane >> 5;
  const int wid = RFL(tid >> 6);
  const int pi = wid & 3, kh = wid >> 2;
  const int tb = wg >> 2, headA = (wg & 3) * 8 + 2 * pi;
  const unsigned l0 = (unsigned)(uintptr_t)lds_;
  LAS char* lds = (LAS char*)lds_;
  const char* kvg = p.ws + WS_KV;
  const char* kvsrc = kvg + (size_t)kh * 8 * KV_CHUNK + pi * 3072;
  const unsigned voff = lane * 16;
  const unsigned ring = l0 + kh * 3 * KV_CHUNK + pi * 3072;
#define APRIO 2
  __builtin_amdgcn_s_setprio(APRIO);
  glds3(kvsrc, voff, ring);
  u32x4 qA0, qB0; u32x2 qA1, qB1;
  {
    const char* qrow = p.ws + WS_QI + (size_t)tb * 80 * 32 * 16;
    auto poff = [&](int c4) { return (unsigned)(r * 16 + (c4 >> 1) * 512 + (c4 & 1) * 8); };
    u32x2 a0, a1, a2, b0, b1, b2;
    const int cA = headA * 5, cB = cA + 5;
    asm volatile("global_load_dwordx2 %0, %6, %12\n\tglobal_load_dwordx2 %1, %7, %12\n\tglobal_load_dwordx2 %2, %8, %12\n\t"
                 "global_load_dwordx2 %3, %9, %12\n\tglobal_load_dwordx2 %4, %10, %12\n\tglobal_load_dwordx2 %5, %11, %12"
                 : "=&v"(a0), "=&v"(a1), "=&v"(a2), "=&v"(b0), "=&v"(b1), "=&v"(b2)
                 : "v"(poff(cA + 2 * h)), "v"(poff(cA + 2 * h + 1)), "v"(poff(cA + 4)), "v"(poff(cB + 2 * h)), "v"(poff(cB + 2 * h + 1)), "v"(poff(cB + 4)), "s"(qrow) : "memory");
    glds3(kvsrc + KV_CHUNK, voff, ring + KV_CHUNK);
    asm volatile("s_waitcnt vmcnt(3)\n\ts_barrier" : "+v"(a0), "+v"(a1), "+v"(a2), "+v"(b0), "+v"(b1), "+v"(b2) :: "memory");
    qA0 = u32x4{a0[0], a0[1], a1[0], a1[1]}; qB0 = u32x4{b0[0], b0[1], b1[0], b1[1]};
    qA1 = h ? u32x2{0u, 0u} : a2;
    qB1 = h ? u32x2{0u, 0u} : b2;
  }
  const f32x16 zero16 = {};
  const int k1d = 1024 - h * 256 - r * 8;
  const int rr = r < 21 ? r : 21;
  u32x4 k0; u32x2 k1; f32x16 sA, sB;
  {
    LAS char* kb0 = lds + h * 512 + r * 16;
    k0 = LDS128(kb0); k1 = LDS64(kb0 + k1d);
    const u32x4 k2 = LDS128(kb0 + 1536); const u32x2 k3 = LDS64(kb0 + 1536 + k1d);
    sA = MFMA32(k0, qA0, zero16); sB = MFMA32(k0, qB0, zero16); sA = MFMA8(k1, qA1, sA); sB = MFMA8(k1, qB1, sB);
    f32x16 tA = MFMA32(k2, qA0, zero16), tB = MFMA32(k2, qB0, zero16); tA = MFMA8(k3, qA1, tA); tB = MFMA8(k3, qB1, tB);
    const float mA = half_max(fmaxf(max16(sA), max16(tA))), mB = half_max(fmaxf(max16(sB), max16(tB)));
    if (h == 1) { qA1[0] = cvtpk(-(mA + SM_OFF), 0.f); qB1[0] = cvtpk(-(mB + SM_OFF), 0.f); }
  }
  LAS char* kbase = lds + kh * 3 * KV_CHUNK + h * 512 + r * 16;
  LAS char* vbase = lds + kh * 3 * KV_CHUNK + KV_VOFF + h * 384 + rr * 16;
  k0 = LDS128(kbase); k1 = LDS64(kbase + k1d);
  sA = MFMA32(k0, qA0, zero16); sA = MFMA8(k1, qA1, sA);
  u32x4 v0 = LDS128(vbase), v1 = v0, p0 = {0u, 0u, 0u, 0u}, p1 = {0u, 0u, 0u, 0u};
  f32x16 oA = {}, oB = {};
  float e12 = 0.f, e13 = 0.f, e14 = 0.f, e15 = 0.f;
  int sc = 0;
#pragma unroll 1
  for (int c = 0; c < 8; c++) {
    const int sn = sc == 2 * KV_CHUNK ? 0 : sc + KV_CHUNK, sp = sn == 2 * KV_CHUNK ? 0 : sn + KV_CHUNK;
    if (c + 2 < 8) glds3(kvsrc + (size_t)(c + 2) * KV_CHUNK, voff, ring + sp);
    LAS char* kc = kbase + sc; LAS char* kn = kbase + sn; LAS char* vc = vbase + sc; LAS char* vn = vbase + sn;
    ATILE(kc + 1 * 1536, vc + 1 * 768, vc + 2 * 768);
    ATILE(kc + 2 * 1536, vc + 3 * 768, vc + 4 * 768);
    if (c == 0) asm volatile("s_waitcnt vmcnt(3)\n\ts_barrier" ::: "memory");
    ATILE(kc + 3 * 1536, vc + 5 * 768, vc + 6 * 768);
    ATILE(kn, vc + 7 * 768, vn);
    asm volatile("s_waitcnt vmcnt(0) lgkmcnt(0)\n\ts_barrier" ::: "memory");
    sc = sn;
  }
  p1[2] = cvtpk(e12, e13); p1[3] = cvtpk(e14, e15);
  oB = MFMA32(v1, p1, oB);
  LAS f32x4* ex = (LAS f32x4*)(lds + (pi * 2 + kh) * 3072 + lane * 16);
  LAS const f32x4* exr = (LAS const f32x4*)(lds + (pi * 2 + (kh ^ 1)) * 3072 + lane * 16);
  f32x16 o;
  if (kh == 0) { o = oA;
#pragma unroll
    for (int g = 0; g < 3; g++) ex[g * 64] = f32x4{oB[4 * g], oB[4 * g + 1], oB[4 * g + 2], oB[4 * g + 3]};
  } else { o = oB;
#pragma unroll
    for (int g = 0; g < 3; g++) ex[g * 64] = f32x4{oA[4 * g], oA[4 * g + 1], oA[4 * g + 2], oA[4 * g + 3]};
  }
  BARRIER();
#pragma unroll
  for (int g = 0; g < 3; g++) { f32x4 t = exr[g * 64]; o[4 * g] += t[0]; o[4 * g + 1] += t[1]; o[4 * g + 2] += t[2]; o[4 * g + 3] += t[3]; }
  {
    float z[4];
#pragma unroll
    for (int i = 0; i < 4; i++) z[i] = __builtin_fmaf(o[i], 0.f, __builtin_fmaf(o[4 + i], 0.f, o[8 + i] * 0.f));
    const float chk = (z[0] + z[1]) + (z[2] + z[3]);
    const bool bad = !(chk == 0.f) || (h == 1 && !(o[8] < 1.0e37f));
    if (__builtin_expect(__any(bad), 0)) o = attn_exact(kvg, kh ? qB0 : qA0, kh ? qB1 : qA1, r, h);
  }
  auto lr = __builtin_amdgcn_permlane32_swap(__float_as_uint(o[8]), __float_as_uint(o[8]), false, false);
  const float il = __builtin_amdgcn_rcpf(__uint_as_float(lr[1]));
  LAS char* ost = lds + 32768;
  const int hl = 2 * pi + kh;
#pragma unroll
  for (int g = 0; g < 3; g++) {
    if (g < 2 || h == 0) {
      const int kk = hl * 20 + (2 * g + h) * 4;
      *(LAS u32x2*)(ost + ((kk >> 3) * 32 + r) * 16 + ((kk >> 2) & 1) * 8) = u32x2{cvtpk(o[4 * g] * il, o[4 * g + 1] * il), cvtpk(o[4 * g + 2] * il, o[4 * g + 3] * il)};
    }
  }
  BARRIER();
#pragma unroll
  for (int k = 0; k < 2; k++) {
    const int i = tid + 512 * k;
    if (i < 640) st16_wt(p.ws + WS_AO, (((wg & 3) * 20 + (i >> 5)) * 2048 + tb * 32 + (i & 31)) * 16, *(LAS u32x4*)(ost + i * 16), 6160384);
  }
  asm volatile("s_waitcnt vmcnt(0)" ::: "memory");
  BARRIER();
  if (tid == 0) __hip_atomic_fetch_add((gu32*)(p.ws + WS_FLAG) + 1024 + (tb >> 1) * 16, 1u, __ATOMIC_RELAXED, __HIP_MEMORY_SCOPE_AGENT);
}

DI int fb_start(int fb) { return (fb >> 2) * 216 + (fb & 3) * 56; }
constexpr int SP_STAGE = 24576, SP_ZOFF = 16384;

constexpr int F_ACT = 8192, F_STAGE = F_ACT + 10240;
DI void fin_role(const Params& p, char* lds_, int b, int wid) {
  const int tid = threadIdx.x, lane = tid & 63, r = lane & 31, h = lane >> 5;
  const unsigned l0 = (unsigned)(uintptr_t)lds_;
  LAS char* lds = (LAS char*)lds_;
  const int x = b & 7, jj = b >> 3;
  const int tbk = (x >> 1) * 4 + (jj & 3), fb = (x & 1) * 8 + (jj >> 2);
  const int tb = 2 * tbk + (fb >> 3), nb = fb & 7;
  gu32* cnt = (gu32*)(p.ws + WS_FLAG);
  BARRIER();
  if (tid == 0) __hip_atomic_fetch_add(cnt + 512 + tbk * 16, 1u, __ATOMIC_RELAXED, __HIP_MEMORY_SCOPE_AGENT);
  if (wid >= 4) {
    const int lw = wid - 4;
    const char* ab = p.ws + WS_AO + (size_t)tb * 64 * 16;
    const char* wb = p.ws + WS_W3 + (size_t)nb * 94 * 2560;
    const unsigned lv = lane * 16;
    auto ks0 = [&](int s) { return s < 14 ? 40 + 4 * s : 4 * (s - 14); };
    auto issue_w = [&](int s) {
      const int p0 = lw < 2 ? 3 * lw : 2 + 2 * lw;
      const char* src = wb + (size_t)ks0(s) * 2560;
      const unsigned dst = l0 + (s & 3) * F_STAGE + F_ACT;
#pragma unroll
      for (int i = 0; i < 3; i++) { const int pc = (lw >= 2 && i == 2) ? p0 : p0 + i; gldsn_dev(src + (s == 13 && pc >= 5 ? 0 : pc * 1024), lv, dst + pc * 1024); }
    };
    auto issue_a = [&](int s) {
#pragma unroll
      for (int i = 0; i < 2; i++) gldsn_dev(ab + (size_t)(2 * ks0(s) + (s == 13 && lw >= 2 ? 0 : 2 * lw) + i) * 32768, lv, l0 + (s & 3) * F_STAGE + (2 * lw + i) * 1024);
    };
    auto await = [&](gu32* word, unsigned want) {
      unsigned spins = 0;
      while ((unsigned)RFL(__hip_atomic_load(word, __ATOMIC_RELAXED, __HIP_MEMORY_SCOPE_AGENT)) < want) { if (++spins > (1u << 22)) break; __builtin_amdgcn_s_sleep(1); }
      asm volatile("" ::: "memory");
    };
    await(cnt + 1536 + nb * 16, 94u);
    issue_w(0); issue_w(1); issue_w(2);
    await(cnt + 512 + tbk * 16, 16u);
    issue_a(0); issue_a(1); issue_a(2);
#pragma unroll 1
    for (int s = 0; s < 24; s++) {
      if (s == 0 || s == 14) WAITV(4); else if (s == 1 || s == 15) WAITV(7); else if (s == 12) WAITV(8); else if (s == 13) WAITV(6);
      else if (s == 22) WAITV(5); else if (s == 23) WAITV(0); else WAITV(10);
      BARRIER();
      if (s + 3 < 24) {
        if (s < 11 || s > 13) issue_a(s + 3);
        issue_w(s + 3);
      }
      if (s == 13) { await(cnt + 1024 + tb * 16, 8u); issue_a(14); issue_a(15); issue_a(16); }
    }
    return;
  }
#define FPRIO 1
  __builtin_amdgcn_s_setprio(FPRIO);
  const int tt = wid & 1, two = wid < 2;
  u32x4 xr[3];
#pragma unroll
  for (int k = 0; k < 3; k++) { const int i = tid + 256 * k < 640 ? tid + 256 * k : 0, row = i / 10, c = i - row * 10;
    xr[k] = *(const u32x4*)(p.x + ((size_t)(tb * 64 + row) * 640 + nb * 80 + c * 8) * 2); }
  const unsigned wa = F_ACT + (two ? lane : 128 + h * 16 + (r & 15)) * 16;
  const unsigned aa = h * 1024 + (32 * tt + r) * 16;
  f32x16 acc = {}, acc2 = {};
#pragma unroll 1
  for (int s = 0; s < 24; s++) {
    BARRIER();
    LAS char* sl = lds + (s & 3) * F_STAGE;
    u32x4 af[4], wf[4], wg[4];
#pragma unroll
    for (int ks = 0; ks < 4; ks++) { af[ks] = LDS128(sl + aa + ks * 2048); wf[ks] = LDS128(sl + wa + ks * 2560); }
    if (two) {
#pragma unroll
      for (int ks = 0; ks < 4; ks++) wg[ks] = LDS128(sl + wa + 1024 + ks * 2560);
    }
    if (two) {
      acc = MFMA32(wf[0], af[0], acc); acc2 = MFMA32(wg[0], af[0], acc2); acc = MFMA32(wf[1], af[1], acc); acc2 = MFMA32(wg[1], af[1], acc2);
      if (s != 13) { acc = MFMA32(wf[2], af[2], acc); acc2 = MFMA32(wg[2], af[2], acc2); acc = MFMA32(wf[3], af[3], acc); acc2 = MFMA32(wg[3], af[3], acc2); }
    } else {
      acc = MFMA32(wf[0], af[0], acc); acc = MFMA32(wf[1], af[1], acc);
      if (s != 13) { acc = MFMA32(wf[2], af[2], acc); acc = MFMA32(wf[3], af[3], acc); }
    }
  }
  BARRIER();
  LAS float* ot = (LAS float*)lds;
  constexpr int OP = 84;
  const int n0 = two ? 0 : 64;
#pragma unroll
  for (int g = 0; g < 4; g++) {
    const int n = n0 + 8 * g + 4 * h;
    if (n < 80) *(LAS f32x4*)(ot + (32 * tt + r) * OP + n) = f32x4{acc[4 * g], acc[4 * g + 1], acc[4 * g + 2], acc[4 * g + 3]};
    if (two) *(LAS f32x4*)(ot + (32 * tt + r) * OP + 32 + n) = f32x4{acc2[4 * g], acc2[4 * g + 1], acc2[4 * g + 2], acc2[4 * g + 3]};
  }
  BARRIER();
#pragma unroll
  for (int k = 0; k < 3; k++) {
    const int i = tid + 256 * k;
    if (i < 640) {
      const int row = i / 10, c = i - row * 10;
      const size_t go = ((size_t)(tb * 64 + row) * 640 + nb * 80 + c * 8) * 2;
      const f32x4 a = *(LAS f32x4*)(ot + row * OP + c * 8), a2 = *(LAS f32x4*)(ot + row * OP + c * 8 + 4);
      const u32x4 w = {cvtpk(a[0] + bflo(xr[k][0]), a[1] + bfhi(xr[k][0])), cvtpk(a[2] + bflo(xr[k][1]), a[3] + bfhi(xr[k][1])),
                       cvtpk(a2[0] + bflo(xr[k][2]), a2[1] + bfhi(xr[k][2])), cvtpk(a2[2] + bflo(xr[k][3]), a2[3] + bfhi(xr[k][3]))};
      st16_wt(p.out, (unsigned)go, w, 2621440);
    }
  }
}
DI void sp_role(const Params& p, char* lds_, int b) {
  const int wid = RFL(threadIdx.x >> 6);
  __builtin_amdgcn_s_setprio(3);
  const int tid = threadIdx.x, lane = tid & 63, r = lane & 31, h = lane >> 5;
  const unsigned l0 = (unsigned)(uintptr_t)lds_;
  LAS char* lds = (LAS char*)lds_;
  const int x = b & 7, jj = b >> 3;
  const int tbk = (x >> 1) * 4 + (jj & 3), fb = (x & 1) * 8 + (jj >> 2);
  gu32* cnt = (gu32*)(p.ws + WS_FLAG);
  if (wid >= 4) {
    const int lw = wid - 4;
    const char* wb = p.wsp + (size_t)tbk * 128 * 4096;
    unsigned wv4[4];
#pragma unroll
    for (int i = 0; i < 4; i++) { const unsigned row = 8 * (4 * lw + i) + (lane >> 3); wv4[i] = row * 4096 + (((lane & 7) ^ ((row >> 1) & 7)) << 4); }
    const char* zb = p.ws + WS_Z2N + (size_t)fb * 32 * 8192 + lw * 2048;
    const unsigned zv = lane * 16;
    auto issue_w = [&](int st) {
#pragma unroll
      for (int i = 0; i < 4; i++) gldsn(wb + st * 128, wv4[i], l0 + (st % 3) * SP_STAGE + (4 * lw + i) * 1024);
    };
    auto issue_z = [&](int st) {
      asm volatile("s_mov_b32 m0, %2\n\ts_nop 0\n\tglobal_load_lds_dwordx4 %0, %1 sc1\n\tglobal_load_lds_dwordx4 %0, %1 offset:1024 sc1"
                   :: "v"(zv), "s"(zb + (size_t)st * 8192), "s"(l0 + (st % 3) * SP_STAGE + SP_ZOFF + lw * 2048) : "memory");
    };
    BARRIER(); BARRIER();
    issue_w(0); issue_w(1);
    BARRIER();
    {
      const int u = b * 3 + lw - 1;
      if (lw > 0 && u < 752) {
        const int ks = u >> 3, nb = u & 7;
        const uintptr_t pwo = (uintptr_t)p.wo, pwout = (uintptr_t)p.wout;
        const GAS char* src = (const GAS char*)(ks < 40 ? pwo + (size_t)ks * 16 * 1280 : pwout + (size_t)(ks - 40) * 16 * 1280) + nb * 160;
        LAS char* sc = lds + 3 * SP_STAGE + (lw - 1) * 2560;
#pragma unroll
        for (int i = 0; i < 3; i++) {
          const int c = lane + 64 * i, rw = c / 10, cc = c - rw * 10;
          if (c < 160) *(LAS u32x4*)(sc + c * 16) = *(const GAS u32x4*)(src + rw * 1280 + cc * 16);
        }
        asm volatile("s_waitcnt vmcnt(0) lgkmcnt(0)" ::: "memory");
        LAS char* ta = sc + (8 * h + ((lane & 15) >> 2)) * 160 + 32 * ((lane >> 4) & 1) + 8 * (lane & 3);
        const unsigned dst = (nb * 94 + ks) * 2560;
        const u32x4 f0 = ldtr(ta, 0, 640), f1 = ldtr(ta, 64, 64 + 640), f2 = ldtr(ta, 128, 128 + 640);
        st16_wt(p.ws + WS_W3, dst + lane * 16, f0, 8 * 94 * 2560);
        st16_wt(p.ws + WS_W3, dst + 1024 + lane * 16, f1, 8 * 94 * 2560);
        if (r < 16) st16_wt(p.ws + WS_W3, dst + 2048 + (h * 16 + r) * 16, f2, 8 * 94 * 2560);
        asm volatile("s_waitcnt vmcnt(0) lgkmcnt(0)" ::: "memory");
        if (lane == 0) __hip_atomic_fetch_add(cnt + 1536 + nb * 16, 1u, __ATOMIC_RELAXED, __HIP_MEMORY_SCOPE_AGENT);
      }
    }
    if (lw > 0) {
      const char* src = p.wsp + (unsigned)RFL(b * 32768 + (lw - 1) * 12288);
      const int n2 = lw == 3 ? 4 : 6;
      for (int i = 0; i < n2; i++)
        asm volatile("s_mov_b32 m0, %2\n\ts_nop 0\n\tglobal_load_lds_dwordx4 %0, %1\n\tglobal_load_lds_dwordx4 %0, %1 offset:1024"
                     :: "v"(zv), "s"(src + i * 2048), "s"(l0 + 3 * SP_STAGE - 2048) : "memory");
    }
    if (lw == 0) {
      {
      __builtin_amdgcn_s_sleep(24);
      unsigned spins = 0;
      for (;;) {
        const unsigned c = __hip_atomic_load(cnt + (lane & 31) * 16, __ATOMIC_RELAXED, __HIP_MEMORY_SCOPE_AGENT);
        if (__all(c >= 8u)) break;
        if (++spins > (1u << 21)) break;
        __builtin_amdgcn_s_sleep(6);
      }
      }
      const char* z00 = p.ws + WS_Z2N + (size_t)fb * 32 * 8192;
#pragma unroll
      for (int i = 0; i < 4; i++)
        asm volatile("s_mov_b32 m0, %2\n\ts_nop 0\n\tglobal_load_lds_dwordx4 %0, %1 sc1\n\tglobal_load_lds_dwordx4 %0, %1 offset:1024 sc1\n\tglobal_load_lds_dwordx4 %0, %1 offset:2048 sc1\n\tglobal_load_lds_dwordx4 %0, %1 offset:3072 sc1"
                     :: "v"(zv), "s"(z00 + i * 4096), "s"(l0 + (i >> 1) * SP_STAGE + SP_ZOFF + (i & 1) * 4096) : "memory");
      WAITV(8);
    }
#pragma unroll 1
    for (int st = 0; st < 32; st++) {
      if (st == 0) { if (lw) WAITV(0); } else if (st < 31) WAITV(6); else WAITV(0);
      BARRIER();
      if (st + 2 < 32) { issue_w(st + 2); issue_z(st + 2); }
    }
    fin_role(p, lds_, b, wid);
    return;
  }
#define LNPRIO 3
  __builtin_amdgcn_s_setprio(LNPRIO);
  {
    LAS float* red = (LAS float*)(lds + K2_LDS - 256);
    const bool act = tid < 216;
    const int g8 = act ? tid >> 1 : 107, half = tid & 1;
    const char* src = p.ws + WS_Z2R + ((size_t)((b >> 2) * 108 + g8) * 32 + (b & 3) * 8 + 4 * half) * 16;
    u32x4 raw[4];
#pragma unroll
    for (int i = 0; i < 4; i++) raw[i] = *(const u32x4*)(src + i * 16);
    const unsigned ONES = act ? 0x3F803F80u : 0u;
    LAS float* part = (LAS float*)(lds + 2 * SP_STAGE);
#pragma unroll
    for (int t = 0; t < 4; t++) {
      float su = 0.f, sq = 0.f;
#pragma unroll
      for (int e = 0; e < 4; e++) { const unsigned a = act ? raw[t][e] : 0u; su = dot2(a, ONES, su); sq = dot2(a, a, sq); }
      part[(2 * (4 * half + t)) * 128 + (tid >> 1)] = su;
      part[(2 * (4 * half + t) + 1) * 128 + (tid >> 1)] = sq;
    }
    BARRIER();
    {
      float s4[4];
#pragma unroll
      for (int k = 0; k < 4; k++) { const f32x2 a = *(LAS f32x2*)(part + (4 * wid + k) * 128 + lane * 2); s4[k] = a[0] + a[1]; }
#pragma unroll
      for (int k = 0; k < 4; k++) s4[k] = wave_sum(s4[k]);
      if (lane < 2) {
        const float su = lane ? s4[2] : s4[0], sq = lane ? s4[3] : s4[1];
        const float mu = su * (1.f / 864.f), var = fmaxf(sq * (1.f / 864.f) - mu * mu, 0.f), rs = __builtin_amdgcn_rsqf(var + 1e-5f);
        ((LAS f32x2*)red)[2 * wid + lane] = f32x2{rs, -mu * rs};
      }
    }
    BARRIER();
    LAS char* tr = lds + 2 * SP_STAGE + 8192;
    {
      float v[4][8];
#pragma unroll
      for (int t = 0; t < 4; t++) {
        const f32x2 c = ((LAS f32x2*)red)[4 * half + t];
#pragma unroll
        for (int e = 0; e < 4; e++) { v[t][2 * e] = __builtin_fmaf(bflo(raw[t][e]), c[0], c[1]); v[t][2 * e + 1] = __builtin_fmaf(bfhi(raw[t][e]), c[0], c[1]); }
      }
      if (act) {
#pragma unroll
        for (int i = 0; i < 8; i++) *(LAS u32x2*)(tr + (8 * g8 + i) * 16 + half * 8) = u32x2{cvtpk(v[0][i], v[1][i]), cvtpk(v[2][i], v[3][i])};
      }
    }
    asm volatile("s_waitcnt lgkmcnt(0)" ::: "memory");
    {
      const __amdgpu_buffer_rsrc_t rsrc = __builtin_amdgcn_make_buffer_rsrc((void*)(p.ws + WS_Z2N), 0, 4194304, 0x00020000);
      const unsigned sbase = (((b >> 3) * 4 + ((b >> 1) & 3)) * 2 + (b & 1)) * 1024;
#pragma unroll
      for (int i = 0; i < 4; i++) {
        const int f = 256 * wid + 64 * i + lane;
        if (f < 864) {
          const u32x4 w = *(LAS u32x4*)(tr + f * 16);
          const int q4 = (f >= 216) + (f >= 432) + (f >= 648), rem = f - q4 * 216, sub = (rem >= 56) + (rem >= 112) + (rem >= 168);
          const unsigned off = (unsigned)(q4 * 4 + sub) * (32u * 8192u) + sbase + (rem - sub * 56) * 16;
          __builtin_amdgcn_raw_buffer_store_b128(w, rsrc, off, 0, 16);
        }
      }
    }
    if (tid < 160) {
      const int fbw = tid < 96 ? (tid >> 3) / 3 * 4 + (tid >> 3) % 3 : ((tid - 96) >> 4) * 4 + 3, row = tid < 96 ? 56 + (tid & 7) : 48 + (tid & 15);
      st16_wt(p.ws + WS_Z2N, (unsigned)fbw * (32u * 8192u) + (((b >> 3) * 4 + ((b >> 1) & 3)) * 2 + (b & 1)) * 1024 + row * 16, u32x4{0u, 0u, 0u, 0u}, 4194304);
    }
    asm volatile("s_waitcnt vmcnt(0)" ::: "memory");
    BARRIER();
    if (tid == 0) __hip_atomic_fetch_add(cnt + (b >> 3) * 16, 1u, __ATOMIC_RELAXED, __HIP_MEMORY_SCOPE_AGENT);
  }
  const unsigned row = 32 * wid + r;
  unsigned wa[4];
#pragma unroll
  for (int ks = 0; ks < 4; ks++) wa[ks] = row * 128 + ((32 * ks + 16 * h) ^ (16 * ((row >> 1) & 7)));
  const unsigned za = SP_ZOFF + h * 1024 + r * 16;
  const int tok = tbk * 128 + row, f0 = fb_start(fb), width = (fb & 3) == 3 ? 48 : 56;
  const float bias = bflo(*(const unsigned short*)(p.bsp + tok * 2));
  const char* z1b = p.ws + WS_Z1 + ((size_t)((tok >> 5) * 108 + (f0 >> 3)) * 32 + r) * 16 + h * 8;
  u32x2 zg[8];
#pragma unroll
  for (int g = 0; g < 8; g++) zg[g] = *(const u32x2*)(z1b + (8 * g < width ? g : 0) * 512);
  f32x16 acc0 = {}, acc1 = {};
#pragma unroll 1
  for (int st = 0; st < 32; st++) {
    BARRIER();
    LAS char* sl = lds + (st % 3) * SP_STAGE;
    u32x4 wf[4], z0[4], z1[4];
#pragma unroll
    for (int ks = 0; ks < 4; ks++) { wf[ks] = LDS128(sl + wa[ks]); z0[ks] = LDS128(sl + za + ks * 2048); z1[ks] = LDS128(sl + za + ks * 2048 + 512); }
#pragma unroll
    for (int ks = 0; ks < 4; ks++) { acc0 = MFMA32(z0[ks], wf[ks], acc0); acc1 = MFMA32(z1[ks], wf[ks], acc1); }
  }
#pragma unroll
  for (int j = 0; j < 2; j++) {
    const f32x16& a = j ? acc1 : acc0;
    u32x2 w[4];
#pragma unroll
    for (int g = 0; g < 4; g++) {
      const u32x2 z = zg[4 * j + g];
      w[g] = u32x2{cvtpk(bflo(z[0]) * (a[4 * g] + bias), bfhi(z[0]) * (a[4 * g + 1] + bias)), cvtpk(bflo(z[1]) * (a[4 * g + 2] + bias), bfhi(z[1]) * (a[4 * g + 3] + bias))};
    }
#pragma unroll
    for (int g = 0; g < 4; g += 2) {
      const u32x4 w8 = pair8(w[g], w[g + 1]);
      const int fl = 32 * j + 8 * (g + h);
      if (fl < width) st16_wt(p.ws + WS_AO, ((80 + ((f0 + fl) >> 3)) * 2048 + tok) * 16, w8, 6160384);
    }
  }
  asm volatile("s_waitcnt vmcnt(0)" ::: "memory");
  fin_role(p, lds_, b, wid);
}
__global__ void __launch_bounds__(512, 4) k2_mix(Params p) {
  extern __shared__ __attribute__((aligned(16))) char lds[];
  unsigned junk = 0;
  if (threadIdx.x >= 448) {
    unsigned long long pc; int codelen;
    asm volatile("s_getpc_b64 %0\nhipk_pc_mark2:\n\ts_mov_b32 %1, hipk_text_mark-hipk_pc_mark2" : "=s"(pc), "=s"(codelen));
    touch_lines((const char*)(pc & ~127ull), codelen > 0 ? codelen >> 7 : 1, junk, threadIdx.x & 63);
  }
  if (blockIdx.x < 256) sp_role(p, lds, blockIdx.x); else attn_role(p, lds, blockIdx.x - 256);
  asm volatile("hipk_text_mark:" :: "v"(junk));
}

extern "C" void kernel_launch(void* const* d_in, const int* in_sizes, int n_in, void* d_out, int out_size, void* d_ws, size_t ws_size, hipStream_t stream) {
  Params p;
  p.x = (const char*)d_in[0]; p.wq = (const char*)d_in[1]; p.wk = (const char*)d_in[2]; p.wv = (const char*)d_in[3]; p.wo = (const char*)d_in[4];
  p.win = (const char*)d_in[6]; p.wsp = (const char*)d_in[7]; p.bsp = (const char*)d_in[8]; p.wout = (const char*)d_in[9];
  p.ws = (char*)d_ws; p.out = (char*)d_out;
  (void)hipFuncSetAttribute((const void*)k1_proj, hipFuncAttributeMaxDynamicSharedMemorySize, K1_LDS);
  (void)hipFuncSetAttribute((const void*)k2_mix, hipFuncAttributeMaxDynamicSharedMemorySize, K2_LDS);
  k1_proj<<<256, 768, K1_LDS, stream>>>(p);
  k2_mix<<<512, 512, K2_LDS, stream>>>(p);
}
```

```cpp
#include <hip/hip_runtime.h>
#include <stdint.h>
#include <type_traits>
typedef __bf16 bf16x8 __attribute__((ext_vector_type(8)));
typedef __bf16 bf16x2 __attribute__((ext_vector_type(2)));
typedef short s16x4 __attribute__((ext_vector_type(4)));
typedef float f32x16 __attribute__((ext_vector_type(16)));
typedef float f32x4 __attribute__((ext_vector_type(4)));
typedef float f32x2 __attribute__((ext_vector_type(2)));
typedef unsigned u32x4 __attribute__((ext_vector_type(4)));
typedef unsigned u32x2 __attribute__((ext_vector_type(2)));
#define DI __device__ __forceinline__
#define LAS __attribute__((address_space(3)))
#define GAS __attribute__((address_space(1)))
#define SB() __builtin_amdgcn_sched_barrier(0)
#define RFL(x) __builtin_amdgcn_readfirstlane(x)
#define MFMA32(a, b, c) __builtin_amdgcn_mfma_f32_32x32x16_bf16(__builtin_bit_cast(bf16x8, a), __builtin_bit_cast(bf16x8, b), c, 0, 0, 0)
#define MFMA16(a, b, c) __builtin_amdgcn_mfma_f32_16x16x32_bf16(__builtin_bit_cast(bf16x8, a), __builtin_bit_cast(bf16x8, b), c, 0, 0, 0)
DI f32x16 mfma8(u32x2 a, u32x2 b, f32x16 c) { return __builtin_amdgcn_mfma_f32_32x32x8bf16_1k(__builtin_bit_cast(s16x4, a), __builtin_bit_cast(s16x4, b), c, 0, 0, 0); }
#define MFMA8(a, b, c) mfma8(a, b, c)
#define LDS64(p) (*(const LAS u32x2*)(p))
#define LDS128(p) (*(const LAS u32x4*)(p))
DI unsigned cvtpk(float lo, float hi) { f32x2 v = {lo, hi}; bf16x2 b = __builtin_convertvector(v, bf16x2); return __builtin_bit_cast(unsigned, b); }
DI float bflo(unsigned u) { return __uint_as_float(u << 16); }
DI float bfhi(unsigned u) { return __uint_as_float(u & 0xffff0000u); }
DI void sqacc(float& a, float x) { asm("v_fmac_f32_e32 %0, %1, %1" : "+v"(a) : "v"(x)); }
DI void addacc(float& a, float x) { asm("v_add_f32_e32 %0, %0, %1" : "+v"(a) : "v"(x)); }
DI float dot2(unsigned a, unsigned b, float c) { return __builtin_amdgcn_fdot2_f32_bf16(__builtin_bit_cast(bf16x2, a), __builtin_bit_cast(bf16x2, b), c, false); }
DI float half_sum(float m) { auto r = __builtin_amdgcn_permlane32_swap(__float_as_uint(m), __float_as_uint(m), false, false); return __uint_as_float(r[0]) + __uint_as_float(r[1]); }
DI float half_max(float m) { auto r = __builtin_amdgcn_permlane32_swap(__float_as_uint(m), __float_as_uint(m), false, false); return fmaxf(__uint_as_float(r[0]), __uint_as_float(r[1])); }
template <int CTRL, int RMASK> DI float dpp_add(float v) { return v + __uint_as_float(__builtin_amdgcn_update_dpp(0u, __float_as_uint(v), CTRL, RMASK, 0xf, true)); }
DI float wave_sum(float v) {
  v = dpp_add<0x111, 0xf>(v); v = dpp_add<0x112, 0xf>(v); v = dpp_add<0x114, 0xf>(v); v = dpp_add<0x118, 0xf>(v);
  v = dpp_add<0x142, 0xa>(v); v = dpp_add<0x143, 0xc>(v);
  return __uint_as_float(__builtin_amdgcn_readlane(__float_as_uint(v), 63));
}
DI void glds3(const void* sbase, unsigned voff, unsigned lds_base) {
  asm volatile("s_mov_b32 m0, %2\n\ts_nop 0\n\tglobal_load_lds_dwordx4 %0, %1\n\tglobal_load_lds_dwordx4 %0, %1 offset:1024\n\tglobal_load_lds_dwordx4 %0, %1 offset:2048"
               :: "v"(voff), "s"(sbase), "s"(lds_base) : "memory"); }
DI void gldsn(const void* sbase, unsigned voff, unsigned lds_base) {
  asm volatile("s_mov_b32 m0, %2\n\ts_nop 0\n\tglobal_load_lds_dwordx4 %0, %1" :: "v"(voff), "s"(sbase), "s"(lds_base) : "memory"); }
DI void gldsn_dev(const void* sbase, unsigned voff, unsigned lds_base) {
  asm volatile("s_mov_b32 m0, %2\n\ts_nop 0\n\tglobal_load_lds_dwordx4 %0, %1 sc1" :: "v"(voff), "s"(sbase), "s"(lds_base) : "memory"); }
DI u32x4 ldtr(const LAS char* p, int off0, int off1) {
  typedef LAS s16x4* lp;
  s16x4 a = __builtin_amdgcn_ds_read_tr16_b64_v4i16((lp)(p + off0)), b = __builtin_amdgcn_ds_read_tr16_b64_v4i16((lp)(p + off1));
  u32x2 x = __builtin_bit_cast(u32x2, a), y = __builtin_bit_cast(u32x2, b);
  return u32x4{x[0], x[1], y[0], y[1]};
}
DI void st16_wt(char* base, unsigned off, u32x4 w, unsigned bytes) {
  const __amdgpu_buffer_rsrc_t rs = __builtin_amdgcn_make_buffer_rsrc((void*)base, 0, bytes, 0x00020000);
  __builtin_amdgcn_raw_buffer_store_b128(w, rs, off, 0, 16);
}
DI u32x4 pair8(u32x2 a, u32x2 b) {
  auto s0 = __builtin_amdgcn_permlane32_swap(a[0], b[0], false, false);
  auto s1 = __builtin_amdgcn_permlane32_swap(a[1], b[1], false, false);
  return u32x4{s0[0], s1[0], s0[1], s1[1]};
}
DI void touch_lines(const void* base, int lines, unsigned& junk, int lane) {
  for (int l0 = 0; l0 < lines; l0 += 64) { const int l = l0 + lane < lines ? l0 + lane : lines - 1;
    asm volatile("global_load_dword %0, %1, %2" : "+v"(junk) : "v"((unsigned)l * 128u), "s"(base) : "memory"); }
}
#define WAITV(n) asm volatile("s_waitcnt vmcnt(" #n ")" ::: "memory")
#define BARRIER() asm volatile("s_waitcnt lgkmcnt(0)\n\ts_barrier" ::: "memory")

constexpr size_t WS_FLAG = 0;
constexpr size_t WS_QI  = 8192;
constexpr size_t WS_KV  = WS_QI + 2621440;
constexpr size_t WS_Z1  = WS_KV + 196608;
constexpr size_t WS_Z2R = WS_Z1 + 3538944;
constexpr size_t WS_Z2N = WS_Z2R + 3538944;
constexpr size_t WS_AO  = WS_Z2N + 4194304;
constexpr size_t WS_W3  = WS_AO + 6160384;
constexpr size_t WS_END = WS_W3 + 8 * 94 * 2560;
constexpr int KV_CHUNK = 12288, KV_VOFF = 6144;
struct Params {
  const char *x, *wq, *wk, *wv, *wo, *win, *wsp, *bsp, *wout;
  char* ws; char* out;
};

constexpr int K1_STAGE = 36864, K1_NS = 4, K1_XT = 16384;
constexpr int K1_CSP = K1_NS * K1_STAGE;
constexpr int K1_CSF = K1_CSP + 2560;
constexpr int K1_STP = K1_CSF + 640;
constexpr int K1_LDS = K1_STP + 1024;
constexpr float QSCALE = 0.22360679774997896f * 1.4426950408889634f;
enum { T_Q = 0, T_K = 1, T_V = 2, T_Z1 = 3, T_Z2 = 4, T_E = 5 };
DI void k1_slot(int cg, int j, int& type, int& idx) {
  if (j < 3 || (j == 3 && cg < 6)) { const int z = cg < 6 ? cg * 4 + j : 24 + (cg - 6) * 3 + j; type = z < 27 ? T_Z1 : T_Z2; idx = z < 27 ? z : z - 27; }
  else if (cg == 15) { type = j == 3 ? T_K : T_V; idx = 0; }
  else if (cg < 4) { type = T_E; idx = 0; }
  else if (cg < 6) { type = T_Q; idx = cg - 4; }
  else { type = T_Q; idx = 2 + (cg - 6) * 2 + (j - 3); }
}
DI f32x2 gelu2(f32x2 x) {
  const f32x2 c1 = {-0.10294324f, -0.10294324f}, c0 = {-2.3022082f, -2.3022082f}, one = {1.f, 1.f};
  const f32x2 a = x * __builtin_elementwise_fma(x * x, c1, c0);
  const f32x2 d = f32x2{__builtin_amdgcn_exp2f(a[0]), __builtin_amdgcn_exp2f(a[1])} + one;
  return x * f32x2{__builtin_amdgcn_rcpf(d[0]), __builtin_amdgcn_rcpf(d[1])};
}
DI void k1_loader(const Params& p, char* lds_, int rb, int cg, int lw) {
  const int lane = threadIdx.x & 63;
  __builtin_amdgcn_s_setprio(3);
  const unsigned l0 = (unsigned)(uintptr_t)lds_;
  const char* xb = p.x + (size_t)rb * 128 * 1280;
  unsigned xv[4];
#pragma unroll
  for (int i = 0; i < 4; i++) { const unsigned row = 8 * (4 * lw + i) + (lane >> 3); xv[i] = row * 1280 + (((lane & 7) ^ ((row >> 1) & 7)) << 4); }
#pragma unroll
  for (int i = 0; i < 4; i++) gldsn(xb, xv[i], l0 + (4 * lw + i) * 1024);
  const char* base[5]; unsigned stride[5], voff[5];
  const uintptr_t pwq = (uintptr_t)p.wq, pwin = (uintptr_t)p.win, pwk = (uintptr_t)p.wk, pwv = (uintptr_t)p.wv;
#pragma unroll
  for (int j = 0; j < 5; j++) {
    int type, idx; k1_slot(cg, j, type, idx);
    const unsigned rowp = 16 * lw + (lane >> 2), ch = (lane & 3) * 16;
    const bool kvs = j >= 3 && cg == 15, isq = type == T_Q || type == T_E;
    const uintptr_t a = kvs ? (j == 3 ? pwk : pwv) : isq ? pwq + idx * 64 : pwin + (type == T_Z2 ? 1728 : 0) + idx * 64;
    base[j] = (const char*)a;
    stride[j] = kvs ? 2560u : isq ? 64u * 1280u : 64u * 3456u;
    const unsigned o = (lw < 2 ? lw : 2) * 1024 + lane * 16;
    voff[j] = kvs ? (o < 2544u ? o : 2544u) : rowp * (isq ? 1280u : 3456u) + ch;
  }
  auto issue = [&](int st) {
    const unsigned slot = l0 + (st & 3) * K1_STAGE;
    if (st > 0) {
#pragma unroll
      for (int i = 0; i < 4; i++) gldsn(xb + st * 128, xv[i], slot + (4 * lw + i) * 1024);
    }
#pragma unroll
    for (int j = 0; j < 5; j++) gldsn(base[j] + (size_t)st * stride[j], voff[j], slot + K1_XT + j * 4096 + lw * 1024);
  };
  issue(0); issue(1); issue(2);
  WAITV(18);
  BARRIER();
#pragma unroll 1
  for (int st = 0; st < 10; st++) {
    if (st < 8) WAITV(9); else WAITV(0);
    BARRIER();
    if (st + 3 < 10) issue(st + 3);
  }
}
template <int ROLE>
DI void k1_body(const Params& p, char* lds_, int rb, int cg, int tt) {
  constexpr int NJ = ROLE == 0 ? 3 : 2, J0 = ROLE == 0 ? 0 : 3;
  const int lane = threadIdx.x & 63, r = lane & 31, h = lane >> 5;
  LAS char* lds = (LAS char*)lds_;
  const bool kv = ROLE == 1 && cg == 15;
  int stype[NJ], sidx[NJ];
#pragma unroll
  for (int j = 0; j < NJ; j++) k1_slot(cg, J0 + j, stype[j], sidx[j]);
#define K1_BPRIO 1
  if (ROLE == 1) __builtin_amdgcn_s_setprio(K1_BPRIO);
  const int row = 32 * tt + r;
  unsigned xa[4], wa[4];
  const unsigned pitch = kv ? 40 : 64;
  const int blk = (lane >> 4) & 1, q = (lane & 15) >> 2, pp = lane & 3;
#pragma unroll
  for (int i = 0; i < 4; i++) {
    const unsigned ks = (i + tt) & 3;
    xa[i] = row * 128 + ((32 * ks + 16 * h) ^ (16 * ((row >> 1) & 7)));
    wa[i] = K1_XT + J0 * 4096 + (16 * ks + 8 * h + q) * pitch + 32 * blk + 8 * pp;
  }
  f32x16 acc[NJ];
#pragma unroll
  for (int j = 0; j < NJ; j++) acc[j] = f32x16{};
  f32x4 cs[NJ];
  float sq[4] = {0.f, 0.f, 0.f, 0.f}, sm[4] = {0.f, 0.f, 0.f, 0.f};
#pragma unroll
  for (int j = 0; j < NJ; j++) cs[j] = f32x4{};
  const unsigned ONES = 0x3F803F80u;
  const unsigned selw = ((lane & 15) == ((lane >> 4) & 1)) ? ONES : 0u;
  const u32x4 SEL = {selw, selw, selw, selw};
  auto mainloop = [&](auto KVC) {
    constexpr bool KV = decltype(KVC)::value;
    u32x4 xf[4] = {}, wf[4][NJ] = {};
    auto rd = [&](LAS char* sl, int i) {
      xf[i] = LDS128(sl + xa[i]);
#pragma unroll
      for (int j = 0; j < NJ; j++) wf[i][j] = ldtr(sl + wa[i], j * 4096, j * 4096 + (KV ? 160 : 256));
    };
    auto mm = [&](int i, LAS char* nsl, int ni) {
#pragma unroll
      for (int j = 0; j < NJ; j++) {
        if (KV && j == 1) acc[j] = MFMA32(xf[i], wf[i][j], acc[j]);
        else acc[j] = MFMA32(wf[i][j], xf[i], acc[j]);
        SB();
        if (j == 0) { xf[ni] = LDS128(nsl + xa[ni]); wf[ni][0] = ldtr(nsl + wa[ni], 0, KV ? 160 : 256); }
        else { wf[ni][j] = ldtr(nsl + wa[ni], j * 4096, j * 4096 + (KV ? 160 : 256)); }
        SB();
      }
      if (ROLE == 1) {
#pragma unroll
        for (int e = 0; e < 4; e++) { const float lo = bflo(xf[i][e]), hi = bfhi(xf[i][e]); sqacc(sq[(2 * e) & 3], lo); sqacc(sq[(2 * e + 1) & 3], hi); addacc(sm[e & 1], lo); addacc(sm[2 + (e & 1)], hi); }
        SB();
      }
    };
    BARRIER();
    rd(lds, 0); rd(lds, 1);
#pragma unroll 1
    for (int st = 0; st < 10; st++) {
      LAS char* sl = lds + (st & 3) * K1_STAGE;
      LAS char* sn = lds + ((st + 1) & 3) * K1_STAGE;
      mm(0, sl, 2);
      mm(1, sl, 3);
#pragma unroll
      for (int j = 0; j < NJ; j++) cs[j] = MFMA16(SEL, wf[0][j], cs[j]);
      SB();
      BARRIER();
      mm(2, sn, 0);
      mm(3, sn, 1);
      SB();
    }
  };
  if (kv) mainloop(std::true_type{}); else mainloop(std::false_type{});
  LAS float* csp = (LAS float*)(lds + K1_CSP);
  LAS float* csf = (LAS float*)(lds + K1_CSF);
  LAS f32x2* stt = (LAS f32x2*)(lds + K1_STP);
  if (lane < 16) {
#pragma unroll
    for (int j = 0; j < NJ; j++) { csp[tt * 160 + (J0 + j) * 32 + lane] = cs[j][0]; csp[tt * 160 + (J0 + j) * 32 + 16 + lane] = cs[j][1]; }
  }
  if (ROLE == 1) {
    const float sqs = half_sum((sq[0] + sq[1]) + (sq[2] + sq[3]));
    const float su = half_sum((sm[0] + sm[1]) + (sm[2] + sm[3]));
    const float mu = su * (1.f / 640.f), var = fmaxf(sqs * (1.f / 640.f) - mu * mu, 0.f), rs = __builtin_amdgcn_rsqf(var + 1e-5f);
    if (h == 0) stt[row] = f32x2{mu * rs, rs};
  }
  BARRIER();
  { const int c = threadIdx.x; if (c < 160) csf[c] = (csp[c] + csp[160 + c]) + (csp[320 + c] + csp[480 + c]); }
  const f32x2 st_ = stt[row];
  BARRIER();
  const int tb = rb * 4 + tt;
#pragma unroll
  for (int j = 0; j < NJ; j++) {
    const int type = stype[j];
    if (type == T_E) continue;
    if (ROLE == 1 && type == T_V) {
      const float csum = csf[(J0 + j) * 32 + r];
      char* vb = p.ws + WS_KV + (size_t)rb * KV_CHUNK + KV_VOFF + tt * 1536 + h * 384 + r * 16;
#pragma unroll
      for (int s = 0; s < 2; s++) {
        float v[8];
#pragma unroll
        for (int i = 0; i < 8; i++) { const f32x2 t = stt[32 * tt + 16 * s + 8 * (i >> 2) + 4 * h + (i & 3)]; v[i] = t[1] * acc[j][8 * s + i] - t[0] * csum; }
        u32x4 w = {cvtpk(v[0], v[1]), cvtpk(v[2], v[3]), cvtpk(v[4], v[5]), cvtpk(v[6], v[7])};
        if (r == 20) w = u32x4{ONES, ONES, ONES, ONES};
        if (r > 20) w = u32x4{0u, 0u, 0u, 0u};
        if (r < 24) *(u32x4*)(vb + s * 768) = w;
      }
      continue;
    }
    f32x2 v[8];
    const f32x2 rs2 = {st_[1], st_[1]}, nm2 = {-st_[0], -st_[0]};
#pragma unroll
    for (int g = 0; g < 4; g++) {
      const f32x4 c4 = *(LAS f32x4*)(csf + (J0 + j) * 32 + 8 * g + 4 * h);
      v[2 * g] = __builtin_elementwise_fma(f32x2{c4[0], c4[1]}, nm2, f32x2{acc[j][4 * g], acc[j][4 * g + 1]} * rs2);
      v[2 * g + 1] = __builtin_elementwise_fma(f32x2{c4[2], c4[3]}, nm2, f32x2{acc[j][4 * g + 2], acc[j][4 * g + 3]} * rs2);
    }
    if (ROLE == 1 && type == T_K) {
      char* kb = p.ws + WS_KV + (size_t)rb * KV_CHUNK + tt * 1536;
      *(u32x2*)(kb + r * 16 + h * 8) = u32x2{cvtpk(v[0][0], v[0][1]), cvtpk(v[1][0], v[1][1])};
      *(u32x2*)(kb + 512 + r * 16 + h * 8) = u32x2{cvtpk(v[2][0], v[2][1]), cvtpk(v[3][0], v[3][1])};
      *(u32x2*)(kb + 1024 + h * 256 + r * 8) = h ? u32x2{0x00003F80u, 0u} : u32x2{cvtpk(v[4][0], v[4][1]), cvtpk(v[5][0], v[5][1])};
      continue;
    }
    unsigned off; char* base;
    if (type == T_Q) {
      base = p.ws + WS_QI; off = ((tb * 80 + sidx[j] * 4 + h) * 32 + r) * 16;
#pragma unroll
      for (int i = 0; i < 8; i++) v[i] *= f32x2{QSCALE, QSCALE};
    } else {
      base = p.ws + (type == T_Z1 ? WS_Z1 : WS_Z2R); off = ((tb * 108 + sidx[j] * 4 + h) * 32 + r) * 16;
#pragma unroll
      for (int i = 0; i < 8; i++) v[i] = gelu2(v[i]);
    }
#pragma unroll
    for (int g = 0; g < 4; g += 2)
      st16_wt(base, off + g * 512, pair8(u32x2{cvtpk(v[2 * g][0], v[2 * g][1]), cvtpk(v[2 * g + 1][0], v[2 * g + 1][1])}, u32x2{cvtpk(v[2 * g + 2][0], v[2 * g + 2][1]), cvtpk(v[2 * g + 3][0], v[2 * g + 3][1])}), 3538944);
  }
}
__global__ void __launch_bounds__(768, 3) k1_proj(Params p) {
  extern __shared__ __attribute__((aligned(16))) char lds[];
  unsigned junk = 0;
  if (threadIdx.x < 64) {
    unsigned long long pc; int codelen;
    asm volatile("s_getpc_b64 %0\nhipk_pc_mark:\n\ts_mov_b32 %1, hipk_text_mark-hipk_pc_mark" : "=s"(pc), "=s"(codelen));
    touch_lines((const char*)(pc & ~127ull), codelen > 0 ? codelen >> 7 : 1, junk, threadIdx.x);
  }
  const int b = blockIdx.x, x = b & 7, j = b >> 3;
  const int rb = (x & 1) * 8 + (j & 7), cg = (x >> 1) * 4 + (j >> 3);
  const int wid = RFL(threadIdx.x >> 6);
  if (b == 0 && threadIdx.x < 512) ((u32x4*)(p.ws + WS_FLAG))[threadIdx.x] = u32x4{0u, 0u, 0u, 0u};
  if (wid < 4) k1_body<0>(p, lds, rb, cg, wid); else if (wid < 8) k1_body<1>(p, lds, rb, cg, wid - 4); else k1_loader(p, lds, rb, cg, wid - 8);
  asm volatile("" :: "v"(junk));
}

typedef __attribute__((address_space(1))) unsigned gu32;
constexpr int K2_LDS = 81920;
constexpr float SM_OFF = 100.f;
#define EX(x) __builtin_amdgcn_exp2f(x)
#define AUNIT(Sc, Sn, qn0, qn1, Oc, Op, LD0, LD1, LD2, LD3) do { \
    Sc[0] = EX(Sc[0]); Sc[1] = EX(Sc[1]); SB(); \
    Sn = MFMA32(k0, qn0, zero16); SB(); LD0; SB(); \
    Sc[2] = EX(Sc[2]); Sc[3] = EX(Sc[3]); SB(); p1[2] = cvtpk(e12, e13); p1[3] = cvtpk(e14, e15); SB(); \
    Sc[4] = EX(Sc[4]); Sc[5] = EX(Sc[5]); SB(); \
    Op = MFMA32(v1, p1, Op); SB(); LD1; SB(); \
    Sc[6] = EX(Sc[6]); Sc[7] = EX(Sc[7]); SB(); p0[0] = cvtpk(Sc[0], Sc[1]); p0[1] = cvtpk(Sc[2], Sc[3]); SB(); \
    Sc[8] = EX(Sc[8]); Sc[9] = EX(Sc[9]); SB(); \
    Sn = MFMA8(k1, qn1, Sn); SB(); LD2; SB(); \
    Sc[10] = EX(Sc[10]); Sc[11] = EX(Sc[11]); SB(); p0[2] = cvtpk(Sc[4], Sc[5]); p0[3] = cvtpk(Sc[6], Sc[7]); SB(); \
    e12 = EX(Sc[12]); e13 = EX(Sc[13]); SB(); \
    Oc = MFMA32(v0, p0, Oc); SB(); LD3; SB(); \
    e14 = EX(Sc[14]); e15 = EX(Sc[15]); SB(); p1[0] = cvtpk(Sc[8], Sc[9]); p1[1] = cvtpk(Sc[10], Sc[11]); SB(); \
  } while (0)
#define ATILE(kn, v1c, v0n) do { \
    AUNIT(sA, sB, qB0, qB1, oA, oB, k0 = LDS128(kn), v1 = LDS128(v1c), k1 = LDS64((kn) + k1d), (void)0); \
    AUNIT(sB, sA, qA0, qA1, oB, oA, (void)0, (void)0, (void)0, v0 = LDS128(v0n)); \
  } while (0)
DI float max3f(float a, float b, float c) { return __builtin_fmaxf(__builtin_fmaxf(a, b), c); }
DI float max16(const f32x16& s) {
  const float a = max3f(max3f(s[0], s[1], s[2]), s[3], s[4]), b = max3f(max3f(s[5], s[6], s[7]), s[8], s[9]), c = max3f(max3f(s[10], s[11], s[12]), s[13], s[14]);
  return max3f(max3f(a, b, c), s[15], s[15]);
}
DI f32x16 attn_exact(const char* kv, u32x4 q0, u32x2 q1, int r, int h) {
  const f32x16 zero16 = {};
  if (h) q1 = u32x2{0u, 0u};
  f32x16 o = {}; float m = -3.0e38f;
  const int rr = r < 21 ? r : 21;
#pragma unroll 1
  for (int t = 0; t < 64; t++) {
    const char* cb = kv + (size_t)(t >> 2) * KV_CHUNK;
    const char* kb = cb + (t & 3) * 1536 + r * 16;
    const u32x4 k0 = *(const u32x4*)(kb + h * 512); const u32x2 k1 = *(const u32x2*)(cb + (t & 3) * 1536 + 1024 + h * 256 + r * 8);
    const char* vb = cb + KV_VOFF + (t & 3) * 1536 + h * 384 + rr * 16;
    const u32x4 v0 = *(const u32x4*)(vb), v1 = *(const u32x4*)(vb + 768);
    f32x16 s = MFMA32(k0, q0, zero16); s = MFMA8(k1, q1, s);
    float mx = s[0];
#pragma unroll
    for (int i = 1; i < 16; i++) mx = fmaxf(mx, s[i]);
    const float mn = fmaxf(m, half_max(mx)), a = EX(m - mn);
    m = mn;
#pragma unroll
    for (int i = 0; i < 16; i++) { o[i] *= a; s[i] = EX(s[i] - mn); }
    const u32x4 p0 = {cvtpk(s[0], s[1]), cvtpk(s[2], s[3]), cvtpk(s[4], s[5]), cvtpk(s[6], s[7])};
    const u32x4 p1 = {cvtpk(s[8], s[9]), cvtpk(s[10], s[11]), cvtpk(s[12], s[13]), cvtpk(s[14], s[15])};
    o = MFMA32(v0, p0, o); o = MFMA32(v1, p1, o);
  }
  return o;
}
DI void attn_role(const Params& p, char* lds_, int wg) {
  const int tid = threadIdx.x, lane = tid & 63, r = lane & 31, h = lane >> 5;
  const int wid = RFL(tid >> 6);
  const int pi = wid & 3, kh = wid >> 2;
  const int tb = wg >> 2, headA = (wg & 3) * 8 + 2 * pi;
  const unsigned l0 = (unsigned)(uintptr_t)lds_;
  LAS char* lds = (LAS char*)lds_;
  const char* kvg = p.ws + WS_KV;
  const char* kvsrc = kvg + (size_t)kh * 8 * KV_CHUNK + pi * 3072;
  const unsigned voff = lane * 16;
  const unsigned ring = l0 + kh * 3 * KV_CHUNK + pi * 3072;
#define APRIO 2
  __builtin_amdgcn_s_setprio(APRIO);
  glds3(kvsrc, voff, ring);
  u32x4 qA0, qB0; u32x2 qA1, qB1;
  {
    const char* qrow = p.ws + WS_QI + (size_t)tb * 80 * 32 * 16;
    auto poff = [&](int c4) { return (unsigned)(r * 16 + (c4 >> 1) * 512 + (c4 & 1) * 8); };
    u32x2 a0, a1, a2, b0, b1, b2;
    const int cA = headA * 5, cB = cA + 5;
    asm volatile("global_load_dwordx2 %0, %6, %12\n\tglobal_load_dwordx2 %1, %7, %12\n\tglobal_load_dwordx2 %2, %8, %12\n\t"
                 "global_load_dwordx2 %3, %9, %12\n\tglobal_load_dwordx2 %4, %10, %12\n\tglobal_load_dwordx2 %5, %11, %12"
                 : "=&v"(a0), "=&v"(a1), "=&v"(a2), "=&v"(b0), "=&v"(b1), "=&v"(b2)
                 : "v"(poff(cA + 2 * h)), "v"(poff(cA + 2 * h + 1)), "v"(poff(cA + 4)), "v"(poff(cB + 2 * h)), "v"(poff(cB + 2 * h + 1)), "v"(poff(cB + 4)), "s"(qrow) : "memory");
    glds3(kvsrc + KV_CHUNK, voff, ring + KV_CHUNK);
    asm volatile("s_waitcnt vmcnt(3)\n\ts_barrier" : "+v"(a0), "+v"(a1), "+v"(a2), "+v"(b0), "+v"(b1), "+v"(b2) :: "memory");
    qA0 = u32x4{a0[0], a0[1], a1[0], a1[1]}; qB0 = u32x4{b0[0], b0[1], b1[0], b1[1]};
    qA1 = h ? u32x2{0u, 0u} : a2;
    qB1 = h ? u32x2{0u, 0u} : b2;
  }
  const f32x16 zero16 = {};
  const int k1d = 1024 - h * 256 - r * 8;
  const int rr = r < 21 ? r : 21;
  u32x4 k0; u32x2 k1; f32x16 sA, sB;
  {
    LAS char* kb0 = lds + h * 512 + r * 16;
    k0 = LDS128(kb0); k1 = LDS64(kb0 + k1d);
    const u32x4 k2 = LDS128(kb0 + 1536); const u32x2 k3 = LDS64(kb0 + 1536 + k1d);
    sA = MFMA32(k0, qA0, zero16); sB = MFMA32(k0, qB0, zero16); sA = MFMA8(k1, qA1, sA); sB = MFMA8(k1, qB1, sB);
    f32x16 tA = MFMA32(k2, qA0, zero16), tB = MFMA32(k2, qB0, zero16); tA = MFMA8(k3, qA1, tA); tB = MFMA8(k3, qB1, tB);
    const float mA = half_max(fmaxf(max16(sA), max16(tA))), mB = half_max(fmaxf(max16(sB), max16(tB)));
    if (h == 1) { qA1[0] = cvtpk(-(mA + SM_OFF), 0.f); qB1[0] = cvtpk(-(mB + SM_OFF), 0.f); }
  }
  LAS char* kbase = lds + kh * 3 * KV_CHUNK + h * 512 + r * 16;
  LAS char* vbase = lds + kh * 3 * KV_CHUNK + KV_VOFF + h * 384 + rr * 16;
  k0 = LDS128(kbase); k1 = LDS64(kbase + k1d);
  sA = MFMA32(k0, qA0, zero16); sA = MFMA8(k1, qA1, sA);
  u32x4 v0 = LDS128(vbase), v1 = v0, p0 = {0u, 0u, 0u, 0u}, p1 = {0u, 0u, 0u, 0u};
  f32x16 oA = {}, oB = {};
  float e12 = 0.f, e13 = 0.f, e14 = 0.f, e15 = 0.f;
  int sc = 0;
#pragma unroll 1
  for (int c = 0; c < 8; c++) {
    const int sn = sc == 2 * KV_CHUNK ? 0 : sc + KV_CHUNK, sp = sn == 2 * KV_CHUNK ? 0 : sn + KV_CHUNK;
    if (c + 2 < 8) glds3(kvsrc + (size_t)(c + 2) * KV_CHUNK, voff, ring + sp);
    LAS char* kc = kbase + sc; LAS char* kn = kbase + sn; LAS char* vc = vbase + sc; LAS char* vn = vbase + sn;
    ATILE(kc + 1 * 1536, vc + 1 * 768, vc + 2 * 768);
    ATILE(kc + 2 * 1536, vc + 3 * 768, vc + 4 * 768);
    if (c == 0) asm volatile("s_waitcnt vmcnt(3)\n\ts_barrier" ::: "memory");
    ATILE(kc + 3 * 1536, vc + 5 * 768, vc + 6 * 768);
    ATILE(kn, vc + 7 * 768, vn);
    asm volatile("s_waitcnt vmcnt(0) lgkmcnt(0)\n\ts_barrier" ::: "memory");
    sc = sn;
  }
  p1[2] = cvtpk(e12, e13); p1[3] = cvtpk(e14, e15);
  oB = MFMA32(v1, p1, oB);
  LAS f32x4* ex = (LAS f32x4*)(lds + (pi * 2 + kh) * 3072 + lane * 16);
  LAS const f32x4* exr = (LAS const f32x4*)(lds + (pi * 2 + (kh ^ 1)) * 3072 + lane * 16);
  f32x16 o;
  if (kh == 0) { o = oA;
#pragma unroll
    for (int g = 0; g < 3; g++) ex[g * 64] = f32x4{oB[4 * g], oB[4 * g + 1], oB[4 * g + 2], oB[4 * g + 3]};
  } else { o = oB;
#pragma unroll
    for (int g = 0; g < 3; g++) ex[g * 64] = f32x4{oA[4 * g], oA[4 * g + 1], oA[4 * g + 2], oA[4 * g + 3]};
  }
  BARRIER();
#pragma unroll
  for (int g = 0; g < 3; g++) { f32x4 t = exr[g * 64]; o[4 * g] += t[0]; o[4 * g + 1] += t[1]; o[4 * g + 2] += t[2]; o[4 * g + 3] += t[3]; }
  {
    float z[4];
#pragma unroll
    for (int i = 0; i < 4; i++) z[i] = __builtin_fmaf(o[i], 0.f, __builtin_fmaf(o[4 + i], 0.f, o[8 + i] * 0.f));
    const float chk = (z[0] + z[1]) + (z[2] + z[3]);
    const bool bad = !(chk == 0.f) || (h == 1 && !(o[8] < 1.0e37f));
    if (__builtin_expect(__any(bad), 0)) o = attn_exact(kvg, kh ? qB0 : qA0, kh ? qB1 : qA1, r, h);
  }
  auto lr = __builtin_amdgcn_permlane32_swap(__float_as_uint(o[8]), __float_as_uint(o[8]), false, false);
  const float il = __builtin_amdgcn_rcpf(__uint_as_float(lr[1]));
  LAS char* ost = lds + 32768;
  const int hl = 2 * pi + kh;
#pragma unroll
  for (int g = 0; g < 3; g++) {
    if (g < 2 || h == 0) {
      const int kk = hl * 20 + (2 * g + h) * 4;
      *(LAS u32x2*)(ost + ((kk >> 3) * 32 + r) * 16 + ((kk >> 2) & 1) * 8) = u32x2{cvtpk(o[4 * g] * il, o[4 * g + 1] * il), cvtpk(o[4 * g + 2] * il, o[4 * g + 3] * il)};
    }
  }
  BARRIER();
#pragma unroll
  for (int k = 0; k < 2; k++) {
    const int i = tid + 512 * k;
    if (i < 640) st16_wt(p.ws + WS_AO, (((wg & 3) * 20 + (i >> 5)) * 2048 + tb * 32 + (i & 31)) * 16, *(LAS u32x4*)(ost + i * 16), 6160384);
  }
  asm volatile("s_waitcnt vmcnt(0)" ::: "memory");
  BARRIER();
  if (tid == 0) __hip_atomic_fetch_add((gu32*)(p.ws + WS_FLAG) + 1024 + (tb >> 1) * 16, 1u, __ATOMIC_RELAXED, __HIP_MEMORY_SCOPE_AGENT);
}

DI int fb_start(int fb) { return (fb >> 2) * 216 + (fb & 3) * 56; }
constexpr int SP_STAGE = 24576, SP_ZOFF = 16384;

constexpr int F_ACT = 8192, F_STAGE = F_ACT + 10240;
DI void fin_role(const Params& p, char* lds_, int b, int wid) {
  const int tid = threadIdx.x, lane = tid & 63, r = lane & 31, h = lane >> 5;
  const unsigned l0 = (unsigned)(uintptr_t)lds_;
  LAS char* lds = (LAS char*)lds_;
  const int x = b & 7, jj = b >> 3;
  const int tbk = (x >> 1) * 4 + (jj & 3), fb = (x & 1) * 8 + (jj >> 2);
  const int tb = 2 * tbk + (fb >> 3), nb = fb & 7;
  gu32* cnt = (gu32*)(p.ws + WS_FLAG);
  BARRIER();
  if (tid == 0) __hip_atomic_fetch_add(cnt + 512 + tbk * 16, 1u, __ATOMIC_RELAXED, __HIP_MEMORY_SCOPE_AGENT);
  if (wid >= 4) {
    const int lw = wid - 4;
    const char* ab = p.ws + WS_AO + (size_t)tb * 64 * 16;
    const char* wb = p.ws + WS_W3 + (size_t)nb * 94 * 2560;
    const unsigned lv = lane * 16;
    auto ks0 = [&](int s) { return s < 14 ? 40 + 4 * s : 4 * (s - 14); };
    auto issue_w = [&](int s) {
      const int p0 = lw < 2 ? 3 * lw : 2 + 2 * lw;
      const char* src = wb + (size_t)ks0(s) * 2560;
      const unsigned dst = l0 + (s & 3) * F_STAGE + F_ACT;
#pragma unroll
      for (int i = 0; i < 3; i++) { const int pc = (lw >= 2 && i == 2) ? p0 : p0 + i; gldsn_dev(src + (s == 13 && pc >= 5 ? 0 : pc * 1024), lv, dst + pc * 1024); }
    };
    auto issue_a = [&](int s) {
#pragma unroll
      for (int i = 0; i < 2; i++) gldsn_dev(ab + (size_t)(2 * ks0(s) + (s == 13 && lw >= 2 ? 0 : 2 * lw) + i) * 32768, lv, l0 + (s & 3) * F_STAGE + (2 * lw + i) * 1024);
    };
    auto await = [&](gu32* word, unsigned want) {
      unsigned spins = 0;
      while ((unsigned)RFL(__hip_atomic_load(word, __ATOMIC_RELAXED, __HIP_MEMORY_SCOPE_AGENT)) < want) { if (++spins > (1u << 22)) break; __builtin_amdgcn_s_sleep(1); }
      asm volatile("" ::: "memory");
    };
    await(cnt + 1536 + nb * 16, 94u);
    issue_w(0); issue_w(1); issue_w(2);
    await(cnt + 512 + tbk * 16, 16u);
    issue_a(0); issue_a(1); issue_a(2);
#pragma unroll 1
    for (int s = 0; s < 24; s++) {
      if (s == 0 || s == 14) WAITV(4); else if (s == 1 || s == 15) WAITV(7); else if (s == 12) WAITV(8); else if (s == 13) WAITV(6);
      else if (s == 22) WAITV(5); else if (s == 23) WAITV(0); else WAITV(10);
      BARRIER();
      if (s + 3 < 24) {
        if (s < 11 || s > 13) issue_a(s + 3);
        issue_w(s + 3);
      }
      if (s == 13) { await(cnt + 1024 + tb * 16, 8u); issue_a(14); issue_a(15); issue_a(16); }
    }
    return;
  }
#define FPRIO 1
  __builtin_amdgcn_s_setprio(FPRIO);
  const int tt = wid & 1, two = wid < 2;
  u32x4 xr[3];
#pragma unroll
  for (int k = 0; k < 3; k++) { const int i = tid + 256 * k < 640 ? tid + 256 * k : 0, row = i / 10, c = i - row * 10;
    xr[k] = *(const u32x4*)(p.x + ((size_t)(tb * 64 + row) * 640 + nb * 80 + c * 8) * 2); }
  const unsigned wa = F_ACT + (two ? lane : 128 + h * 16 + (r & 15)) * 16;
  const unsigned aa = h * 1024 + (32 * tt + r) * 16;
  f32x16 acc = {}, acc2 = {};
#pragma unroll 1
  for (int s = 0; s < 24; s++) {
    BARRIER();
    LAS char* sl = lds + (s & 3) * F_STAGE;
    u32x4 af[4], wf[4], wg[4];
#pragma unroll
    for (int ks = 0; ks < 4; ks++) { af[ks] = LDS128(sl + aa + ks * 2048); wf[ks] = LDS128(sl + wa + ks * 2560); }
    if (two) {
#pragma unroll
      for (int ks = 0; ks < 4; ks++) wg[ks] = LDS128(sl + wa + 1024 + ks * 2560);
      acc = MFMA32(wf[0], af[0], acc); acc2 = MFMA32(wg[0], af[0], acc2); acc = MFMA32(wf[1], af[1], acc); acc2 = MFMA32(wg[1], af[1], acc2);
      if (s != 13) { acc = MFMA32(wf[2], af[2], acc); acc2 = MFMA32(wg[2], af[2], acc2); acc = MFMA32(wf[3], af[3], acc); acc2 = MFMA32(wg[3], af[3], acc2); }
    } else {
      acc = MFMA32(wf[0], af[0], acc); acc = MFMA32(wf[1], af[1], acc);
      if (s != 13) { acc = MFMA32(wf[2], af[2], acc); acc = MFMA32(wf[3], af[3], acc); }
    }
  }
  BARRIER();
  LAS float* ot = (LAS float*)lds;
  constexpr int OP = 84;
  const int n0 = two ? 0 : 64;
#pragma unroll
  for (int g = 0; g < 4; g++) {
    const int n = n0 + 8 * g + 4 * h;
    if (n < 80) *(LAS f32x4*)(ot + (32 * tt + r) * OP + n) = f32x4{acc[4 * g], acc[4 * g + 1], acc[4 * g + 2], acc[4 * g + 3]};
    if (two) *(LAS f32x4*)(ot + (32 * tt + r) * OP + 32 + n) = f32x4{acc2[4 * g], acc2[4 * g + 1], acc2[4 * g + 2], acc2[4 * g + 3]};
  }
  BARRIER();
#pragma unroll
  for (int k = 0; k < 3; k++) {
    const int i = tid + 256 * k;
    if (i < 640) {
      const int row = i / 10, c = i - row * 10;
      const size_t go = ((size_t)(tb * 64 + row) * 640 + nb * 80 + c * 8) * 2;
      const f32x4 a = *(LAS f32x4*)(ot + row * OP + c * 8), a2 = *(LAS f32x4*)(ot + row * OP + c * 8 + 4);
      const u32x4 w = {cvtpk(a[0] + bflo(xr[k][0]), a[1] + bfhi(xr[k][0])), cvtpk(a[2] + bflo(xr[k][1]), a[3] + bfhi(xr[k][1])),
                       cvtpk(a2[0] + bflo(xr[k][2]), a2[1] + bfhi(xr[k][2])), cvtpk(a2[2] + bflo(xr[k][3]), a2[3] + bfhi(xr[k][3]))};
      st16_wt(p.out, (unsigned)go, w, 2621440);
    }
  }
}
DI void sp_role(const Params& p, char* lds_, int b) {
  const int wid = RFL(threadIdx.x >> 6);
  __builtin_amdgcn_s_setprio(3);
  const int tid = threadIdx.x, lane = tid & 63, r = lane & 31, h = lane >> 5;
  const unsigned l0 = (unsigned)(uintptr_t)lds_;
  LAS char* lds = (LAS char*)lds_;
  const int x = b & 7, jj = b >> 3;
  const int tbk = (x >> 1) * 4 + (jj & 3), fb = (x & 1) * 8 + (jj >> 2);
  gu32* cnt = (gu32*)(p.ws + WS_FLAG);
  if (wid >= 4) {
    const int lw = wid - 4;
    const char* wb = p.wsp + (size_t)tbk * 128 * 4096;
    unsigned wv4[4];
#pragma unroll
    for (int i = 0; i < 4; i++) { const unsigned row = 8 * (4 * lw + i) + (lane >> 3); wv4[i] = row * 4096 + (((lane & 7) ^ ((row >> 1) & 7)) << 4); }
    const char* zb = p.ws + WS_Z2N + (size_t)fb * 32 * 8192 + lw * 2048;
    const unsigned zv = lane * 16;
    auto issue_w = [&](int st) {
#pragma unroll
      for (int i = 0; i < 4; i++) gldsn(wb + st * 128, wv4[i], l0 + (st % 3) * SP_STAGE + (4 * lw + i) * 1024);
    };
    auto issue_z = [&](int st) {
      asm volatile("s_mov_b32 m0, %2\n\ts_nop 0\n\tglobal_load_lds_dwordx4 %0, %1 sc1\n\tglobal_load_lds_dwordx4 %0, %1 offset:1024 sc1"
                   :: "v"(zv), "s"(zb + (size_t)st * 8192), "s"(l0 + (st % 3) * SP_STAGE + SP_ZOFF + lw * 2048) : "memory");
    };
    BARRIER(); BARRIER();
    issue_w(0); issue_w(1);
    BARRIER();
    {
      const int u = b * 3 + lw - 1;
      if (lw > 0 && u < 752) {
        const int ks = u >> 3, nb = u & 7;
        const uintptr_t pwo = (uintptr_t)p.wo, pwout = (uintptr_t)p.wout;
        const GAS char* src = (const GAS char*)(ks < 40 ? pwo + (size_t)ks * 16 * 1280 : pwout + (size_t)(ks - 40) * 16 * 1280) + nb * 160;
        LAS char* sc = lds + 3 * SP_STAGE + (lw - 1) * 2560;
#pragma unroll
        for (int i = 0; i < 3; i++) {
          const int c = lane + 64 * i, rw = c / 10, cc = c - rw * 10;
          if (c < 160) *(LAS u32x4*)(sc + c * 16) = *(const GAS u32x4*)(src + rw * 1280 + cc * 16);
        }
        asm volatile("s_waitcnt vmcnt(0) lgkmcnt(0)" ::: "memory");
        LAS char* ta = sc + (8 * h + ((lane & 15) >> 2)) * 160 + 32 * ((lane >> 4) & 1) + 8 * (lane & 3);
        const unsigned dst = (nb * 94 + ks) * 2560;
        const u32x4 f0 = ldtr(ta, 0, 640), f1 = ldtr(ta, 64, 64 + 640), f2 = ldtr(ta, 128, 128 + 640);
        st16_wt(p.ws + WS_W3, dst + lane * 16, f0, 8 * 94 * 2560);
        st16_wt(p.ws + WS_W3, dst + 1024 + lane * 16, f1, 8 * 94 * 2560);
        if (r < 16) st16_wt(p.ws + WS_W3, dst + 2048 + (h * 16 + r) * 16, f2, 8 * 94 * 2560);
        asm volatile("s_waitcnt vmcnt(0) lgkmcnt(0)" ::: "memory");
        if (lane == 0) __hip_atomic_fetch_add(cnt + 1536 + nb * 16, 1u, __ATOMIC_RELAXED, __HIP_MEMORY_SCOPE_AGENT);
      }
    }
    if (lw > 0) {
      const char* src = p.wsp + (unsigned)RFL(b * 32768 + (lw - 1) * 12288);
      const int n2 = lw == 3 ? 4 : 6;
      for (int i = 0; i < n2; i++)
        asm volatile("s_mov_b32 m0, %2\n\ts_nop 0\n\tglobal_load_lds_dwordx4 %0, %1\n\tglobal_load_lds_dwordx4 %0, %1 offset:1024"
                     :: "v"(zv), "s"(src + i * 2048), "s"(l0 + 3 * SP_STAGE - 2048) : "memory");
    }
    if (lw == 0) {
      {
      __builtin_amdgcn_s_sleep(24);
      unsigned spins = 0;
      for (;;) {
        const unsigned c = __hip_atomic_load(cnt + (lane & 31) * 16, __ATOMIC_RELAXED, __HIP_MEMORY_SCOPE_AGENT);
        if (__all(c >= 8u)) break;
        if (++spins > (1u << 21)) break;
        __builtin_amdgcn_s_sleep(6);
      }
      }
      const char* z00 = p.ws + WS_Z2N + (size_t)fb * 32 * 8192;
#pragma unroll
      for (int i = 0; i < 4; i++)
        asm volatile("s_mov_b32 m0, %2\n\ts_nop 0\n\tglobal_load_lds_dwordx4 %0, %1 sc1\n\tglobal_load_lds_dwordx4 %0, %1 offset:1024 sc1\n\tglobal_load_lds_dwordx4 %0, %1 offset:2048 sc1\n\tglobal_load_lds_dwordx4 %0, %1 offset:3072 sc1"
                     :: "v"(zv), "s"(z00 + i * 4096), "s"(l0 + (i >> 1) * SP_STAGE + SP_ZOFF + (i & 1) * 4096) : "memory");
      WAITV(8);
    }
#pragma unroll 1
    for (int st = 0; st < 32; st++) {
      if (st == 0) { if (lw) WAITV(0); } else if (st < 31) WAITV(6); else WAITV(0);
      BARRIER();
      if (st + 2 < 32) { issue_w(st + 2); issue_z(st + 2); }
    }
    fin_role(p, lds_, b, wid);
    return;
  }
#define LNPRIO 3
  __builtin_amdgcn_s_setprio(LNPRIO);
  {
    LAS float* red = (LAS float*)(lds + K2_LDS - 256);
    const bool act = tid < 216;
    const int g8 = act ? tid >> 1 : 107, half = tid & 1;
    const char* src = p.ws + WS_Z2R + ((size_t)((b >> 2) * 108 + g8) * 32 + (b & 3) * 8 + 4 * half) * 16;
    u32x4 raw[4];
#pragma unroll
    for (int i = 0; i < 4; i++) raw[i] = *(const u32x4*)(src + i * 16);
    const unsigned ONES = act ? 0x3F803F80u : 0u;
    LAS float* part = (LAS float*)(lds + 2 * SP_STAGE);
#pragma unroll
    for (int t = 0; t < 4; t++) {
      float su = 0.f, sq = 0.f;
#pragma unroll
      for (int e = 0; e < 4; e++) { const unsigned a = act ? raw[t][e] : 0u; su = dot2(a, ONES, su); sq = dot2(a, a, sq); }
      part[(2 * (4 * half + t)) * 128 + (tid >> 1)] = su;
      part[(2 * (4 * half + t) + 1) * 128 + (tid >> 1)] = sq;
    }
    BARRIER();
    {
      float s4[4];
#pragma unroll
      for (int k = 0; k < 4; k++) { const f32x2 a = *(LAS f32x2*)(part + (4 * wid + k) * 128 + lane * 2); s4[k] = a[0] + a[1]; }
#pragma unroll
      for (int k = 0; k < 4; k++) s4[k] = wave_sum(s4[k]);
      if (lane < 2) {
        const float su = lane ? s4[2] : s4[0], sq = lane ? s4[3] : s4[1];
        const float mu = su * (1.f / 864.f), var = fmaxf(sq * (1.f / 864.f) - mu * mu, 0.f), rs = __builtin_amdgcn_rsqf(var + 1e-5f);
        ((LAS f32x2*)red)[2 * wid + lane] = f32x2{rs, -mu * rs};
      }
    }
    BARRIER();
    LAS char* tr = lds + 2 * SP_STAGE + 8192;
    {
      float v[4][8];
#pragma unroll
      for (int t = 0; t < 4; t++) {
        const f32x2 c = ((LAS f32x2*)red)[4 * half + t];
#pragma unroll
        for (int e = 0; e < 4; e++) { v[t][2 * e] = __builtin_fmaf(bflo(raw[t][e]), c[0], c[1]); v[t][2 * e + 1] = __builtin_fmaf(bfhi(raw[t][e]), c[0], c[1]); }
      }
      if (act) {
#pragma unroll
        for (int i = 0; i < 8; i++) *(LAS u32x2*)(tr + (8 * g8 + i) * 16 + half * 8) = u32x2{cvtpk(v[0][i], v[1][i]), cvtpk(v[2][i], v[3][i])};
      }
    }
    asm volatile("s_waitcnt lgkmcnt(0)" ::: "memory");
    {
      const __amdgpu_buffer_rsrc_t rsrc = __builtin_amdgcn_make_buffer_rsrc((void*)(p.ws + WS_Z2N), 0, 4194304, 0x00020000);
      const unsigned sbase = (((b >> 3) * 4 + ((b >> 1) & 3)) * 2 + (b & 1)) * 1024;
#pragma unroll
      for (int i = 0; i < 4; i++) {
        const int f = 256 * wid + 64 * i + lane;
        if (f < 864) {
          const u32x4 w = *(LAS u32x4*)(tr + f * 16);
          const int q4 = (f >= 216) + (f >= 432) + (f >= 648), rem = f - q4 * 216, sub = (rem >= 56) + (rem >= 112) + (rem >= 168);
          const unsigned off = (unsigned)(q4 * 4 + sub) * (32u * 8192u) + sbase + (rem - sub * 56) * 16;
          __builtin_amdgcn_raw_buffer_store_b128(w, rsrc, off, 0, 16);
        }
      }
    }
    if (tid < 160) {
      const int fbw = tid < 96 ? (tid >> 3) / 3 * 4 + (tid >> 3) % 3 : ((tid - 96) >> 4) * 4 + 3, row = tid < 96 ? 56 + (tid & 7) : 48 + (tid & 15);
      st16_wt(p.ws + WS_Z2N, (unsigned)fbw * (32u * 8192u) + (((b >> 3) * 4 + ((b >> 1) & 3)) * 2 + (b & 1)) * 1024 + row * 16, u32x4{0u, 0u, 0u, 0u}, 4194304);
    }
    asm volatile("s_waitcnt vmcnt(0)" ::: "memory");
    BARRIER();
    if (tid == 0) __hip_atomic_fetch_add(cnt + (b >> 3) * 16, 1u, __ATOMIC_RELAXED, __HIP_MEMORY_SCOPE_AGENT);
  }
  const unsigned row = 32 * wid + r;
  unsigned wa[4];
#pragma unroll
  for (int ks = 0; ks < 4; ks++) wa[ks] = row * 128 + ((32 * ks + 16 * h) ^ (16 * ((row >> 1) & 7)));
  const unsigned za = SP_ZOFF + h * 1024 + r * 16;
  const int tok = tbk * 128 + row, f0 = fb_start(fb), width = (fb & 3) == 3 ? 48 : 56;
  const float bias = bflo(*(const unsigned short*)(p.bsp + tok * 2));
  const char* z1b = p.ws + WS_Z1 + ((size_t)((tok >> 5) * 108 + (f0 >> 3)) * 32 + r) * 16 + h * 8;
  u32x2 zg[8];
#pragma unroll
  for (int g = 0; g < 8; g++) zg[g] = *(const u32x2*)(z1b + (8 * g < width ? g : 0) * 512);
  f32x16 acc0 = {}, acc1 = {};
#pragma unroll 1
  for (int st = 0; st < 32; st++) {
    BARRIER();
    LAS char* sl = lds + (st % 3) * SP_STAGE;
    u32x4 wf[4], z0[4], z1[4];
#pragma unroll
    for (int ks = 0; ks < 4; ks++) { wf[ks] = LDS128(sl + wa[ks]); z0[ks] = LDS128(sl + za + ks * 2048); z1[ks] = LDS128(sl + za + ks * 2048 + 512); }
#pragma unroll
    for (int ks = 0; ks < 4; ks++) { acc0 = MFMA32(z0[ks], wf[ks], acc0); acc1 = MFMA32(z1[ks], wf[ks], acc1); }
  }
#pragma unroll
  for (int j = 0; j < 2; j++) {
    const f32x16& a = j ? acc1 : acc0;
    u32x2 w[4];
#pragma unroll
    for (int g = 0; g < 4; g++) {
      const u32x2 z = zg[4 * j + g];
      w[g] = u32x2{cvtpk(bflo(z[0]) * (a[4 * g] + bias), bfhi(z[0]) * (a[4 * g + 1] + bias)), cvtpk(bflo(z[1]) * (a[4 * g + 2] + bias), bfhi(z[1]) * (a[4 * g + 3] + bias))};
    }
#pragma unroll
    for (int g = 0; g < 4; g += 2) {
      const u32x4 w8 = pair8(w[g], w[g + 1]);
      const int fl = 32 * j + 8 * (g + h);
      if (fl < width) st16_wt(p.ws + WS_AO, ((80 + ((f0 + fl) >> 3)) * 2048 + tok) * 16, w8, 6160384);
    }
  }
  asm volatile("s_waitcnt vmcnt(0)" ::: "memory");
  fin_role(p, lds_, b, wid);
}
__global__ void __launch_bounds__(512, 4) k2_mix(Params p) {
  extern __shared__ __attribute__((aligned(16))) char lds[];
  unsigned junk = 0;
  if (threadIdx.x >= 448) {
    unsigned long long pc; int codelen;
    asm volatile("s_getpc_b64 %0\nhipk_pc_mark2:\n\ts_mov_b32 %1, hipk_text_mark-hipk_pc_mark2" : "=s"(pc), "=s"(codelen));
    touch_lines((const char*)(pc & ~127ull), codelen > 0 ? codelen >> 7 : 1, junk, threadIdx.x & 63);
  }
  if (blockIdx.x < 256) sp_role(p, lds, blockIdx.x); else attn_role(p, lds, blockIdx.x - 256);
  asm volatile("hipk_text_mark:" :: "v"(junk));
}

extern "C" void kernel_launch(void* const* d_in, const int* in_sizes, int n_in, void* d_out, int out_size, void* d_ws, size_t ws_size, hipStream_t stream) {
  Params p;
  p.x = (const char*)d_in[0]; p.wq = (const char*)d_in[1]; p.wk = (const char*)d_in[2]; p.wv = (const char*)d_in[3]; p.wo = (const char*)d_in[4];
  p.win = (const char*)d_in[6]; p.wsp = (const char*)d_in[7]; p.bsp = (const char*)d_in[8]; p.wout = (const char*)d_in[9];
  p.ws = (char*)d_ws; p.out = (char*)d_out;
  (void)hipFuncSetAttribute((const void*)k1_proj, hipFuncAttributeMaxDynamicSharedMemorySize, K1_LDS);
  (void)hipFuncSetAttribute((const void*)k2_mix, hipFuncAttributeMaxDynamicSharedMemorySize, K2_LDS);
  k1_proj<<<256, 768, K1_LDS, stream>>>(p);
  k2_mix<<<512, 512, K2_LDS, stream>>>(p);
}
```
